```python
import torch

def _softmax(x, axis=-1):
    return torch.softmax(x, dim=axis)
def _gelu(x):
    return torch.nn.functional.gelu(x, approximate="tanh")
def _silu(x):
    return torch.nn.functional.silu(x)
def _sigmoid(x):
    return torch.sigmoid(x)
def _layernorm(x, axis=-1, eps=1e-5):
    m = x.mean(dim=axis, keepdim=True)
    v = x.var(dim=axis, keepdim=True, unbiased=False)
    return (x - m) / torch.sqrt(v + eps)
def _scatter_add(tgt, idx, src, axis=0):
    return tgt.clone().index_add_(axis, idx, src)
def _segment_sum(src, seg, n):
    out = torch.zeros((n,) + src.shape[1:], dtype=src.dtype)
    return out.index_add_(0, seg, src)
def _pad(x, pw):
    flat = tuple(v for pair in reversed(pw) for v in pair)
    return torch.nn.functional.pad(x, flat)

kernel_name = 'standalone__modern_conv_block_replknet__vision'

def setup_inputs():

    _w = (lambda a: (lambda t: t.to(torch.bfloat16) if t.is_floating_point() else t)(torch.as_tensor(a)))
    H = W = 384
    C = 96
    K = 3
    return {
        "x": _w((lambda *s: torch.randn(*s, dtype=torch.bfloat16) * 0.05)(1, H, W, C)),
        "dw_large": _w((lambda *s: torch.randn(*s, dtype=torch.bfloat16) * 0.4)(K, K, C) * (1.0 / K)),
        "dw_small": _w((lambda *s: torch.randn(*s, dtype=torch.bfloat16) * 0.4)(5, 5, C) * 0.2),
        "w1": _w((lambda *s: torch.randn(*s, dtype=torch.bfloat16) * 0.4)(C, 4 * C) * (C ** -0.5)),
        "w2": _w((lambda *s: torch.randn(*s, dtype=torch.bfloat16) * 0.1)(4 * C, C) * ((4 * C) ** -0.5)),
    }

def reference(x, dw_large, dw_small, w1, w2):
    B, H, W, C = x.shape
    Kl = dw_large.shape[0]
    pl = (Kl - 1) // 2
    xpl = _pad(x, ((0,0),(pl,pl),(pl,pl),(0,0)))
    out = torch.zeros_like(x)
    for i in range(Kl):
        for j in range(Kl):
            out = out + xpl[:, i:i+H, j:j+W, :] * dw_large[i, j][None, None, None, :]
    Ks = dw_small.shape[0]
    ps = (Ks - 1) // 2
    xps = _pad(x, ((0,0),(ps,ps),(ps,ps),(0,0)))
    for i in range(Ks):
        for j in range(Ks):
            out = out + xps[:, i:i+H, j:j+W, :] * dw_small[i, j][None, None, None, :]
    h = _layernorm(out, axis=-1)
    h = _gelu(torch.einsum('bhwc,ce->bhwe', h, w1))
    h = torch.einsum('bhwe,ec->bhwc', h, w2)
    return x + h
```

```cpp
#include <hip/hip_runtime.h>
#include <hip/hip_bf16.h>
constexpr int H=384, W=384, C=96, E=384;
__device__ __forceinline__ float bf2f(unsigned short u){ return __uint_as_float(((unsigned)u)<<16); }
__device__ __forceinline__ unsigned short f2bf(float f){ __hip_bfloat16 b=__float2bfloat16(f); return *(unsigned short*)&b; }
__global__ __launch_bounds__(384) void k_naive(const unsigned short* x,const unsigned short* w3,const unsigned short* w5,const unsigned short* w1,const unsigned short* w2,unsigned short* out){
  __shared__ float o[8][C]; __shared__ float y[8][E]; __shared__ float st[8][2];
  int t=threadIdx.x; int p0=blockIdx.x*8;
  for(int i=t;i<8*C;i+=384){ int p=p0+i/C,c=i%C; int r=p/W,q=p%W; float a=0;
    for(int dy=0;dy<5;dy++)for(int dx=0;dx<5;dx++){ int rr=r+dy-2,qq=q+dx-2; if(rr<0||rr>=H||qq<0||qq>=W)continue;
      float w=bf2f(w5[(dy*5+dx)*C+c]); if(dy>=1&&dy<=3&&dx>=1&&dx<=3) w+=bf2f(w3[((dy-1)*3+dx-1)*C+c]);
      a+=w*bf2f(x[(rr*W+qq)*C+c]); }
    o[i/C][c]=a; }
  __syncthreads();
  if(t<8){ float m=0; for(int c=0;c<C;c++)m+=o[t][c]; m/=C; float v=0; for(int c=0;c<C;c++){float d=o[t][c]-m; v+=d*d;} v/=C; st[t][0]=m; st[t][1]=rsqrtf(v+1e-5f);}
  __syncthreads();
  for(int i=t;i<8*C;i+=384){ int p=i/C,c=i%C; o[p][c]=bf2f(f2bf((o[p][c]-st[p][0])*st[p][1])); }
  __syncthreads();
  { float a[8]; for(int p=0;p<8;p++)a[p]=0; for(int c=0;c<C;c++){ float w=bf2f(w1[c*E+t]); for(int p=0;p<8;p++)a[p]+=w*o[p][c]; }
    for(int p=0;p<8;p++){ float v=a[p]; float u=0.7978845608f*(v+0.044715f*v*v*v); y[p][t]=bf2f(f2bf(v/(1.f+__expf(-2.f*u)))); } }
  __syncthreads();
  for(int i=t;i<8*C;i+=384){ int p=i/C,c=i%C; float a=0; for(int e=0;e<E;e++)a+=y[p][e]*bf2f(w2[e*C+c]); out[(p0+p)*C+c]=f2bf(a+bf2f(x[(p0+p)*C+c])); }
}
extern "C" void kernel_launch(void* const* d_in,const int* in_sizes,int n_in,void* d_out,int out_size,void* d_ws,size_t ws_size,hipStream_t stream){
  k_naive<<<H*W/8,384,0,stream>>>((const unsigned short*)d_in[0],(const unsigned short*)d_in[1],(const unsigned short*)d_in[2],(const unsigned short*)d_in[3],(const unsigned short*)d_in[4],(unsigned short*)d_out);
}
```

```cpp
#include <hip/hip_runtime.h>

__global__ __launch_bounds__(512, 2) void replk_fused(const void* x, const void* w3, const void* w5, const void* w1, const void* w2, void* out) {
  __shared__ __attribute__((aligned(16))) unsigned char lds[163840];
  const unsigned wave = __builtin_amdgcn_readfirstlane(threadIdx.x >> 6);
  const unsigned wg = blockIdx.x;
  const unsigned ldsbase = (unsigned)(size_t)(__attribute__((address_space(3))) unsigned char*)lds;
  const auto* args = __builtin_amdgcn_kernarg_segment_ptr();
  asm volatile(
  "s_load_dwordx8 s[8:15], %0, 0x0\n"
  "s_load_dwordx4 s[16:19], %0, 0x20\n"
  "s_getpc_b64 s[52:53]\n"
  "PC1_%=:\n"
  "v_mbcnt_lo_u32_b32 v1, -1, 0\n"
  "v_mbcnt_hi_u32_b32 v1, -1, v1\n"
  "v_min_u32 v1, 7, v1\n"
  "s_bfe_u32 s50, %1, 0x30003\n"
  "s_lshl_b32 s50, s50, 3\n"
  "s_add_u32 s50, s50, %2\n"
  "s_mul_i32 s50, s50, 8\n"
  "v_add_lshl_u32 v1, v1, s50, 7\n"
  "v_min_u32 v1, GF_start_%=-PC1_%=, v1\n"
  "global_load_dword v2, v1, s[52:53]\n"
  "s_and_b32 s50, %1, 7\n"
  "s_lshl_b32 s50, s50, 5\n"
  "s_lshr_b32 s45, %1, 3\n"
  "s_add_u32 s45, s45, s50\n"
  "s_and_b32 s44, %2, 3\n"
  "s_mov_b32 s46, 0xc09d30fc\n"
  "s_mov_b32 s47, 0xeeff558\n"
  "s_cmp_ge_u32 %2, 4\n"
  "s_cbranch_scc1 G_entry_%=\n"
  "s_setprio 3\n"
  "v_mbcnt_lo_u32_b32 v225, -1, 0\n"
  "v_mbcnt_hi_u32_b32 v225, -1, v225\n"
  "s_mul_i32 s50, s45, 3\n"
  "s_lshr_b32 s67, s50, 5\n"
  "s_mul_i32 s66, s45, 36\n"
  "s_mul_i32 s50, s67, 384\n"
  "s_sub_u32 s66, s66, s50\n"
  "s_mul_i32 s78, s66, 73728\n"
  "s_mul_i32 s50, s67, 3072\n"
  "s_add_u32 s78, s78, s50\n"
  "s_sub_u32 s56, 384, s66\n"
  "s_lshr_b32 s56, s56, 2\n"
  "s_cmp_eq_u32 s66, 0\n"
  "s_cselect_b32 s56, 0, s56\n"
  "s_cselect_b32 s50, 28308480, 0\n"
  "s_subb_u32 s67, s67, 0\n"
  "s_add_u32 s68, s78, s50\n"
  "s_lshr_b32 s51, s44, 1\n"
  "s_xor_b32 s51, s51, 1\n"
  "s_add_u32 s57, s56, s51\n"
  "s_mul_i32 s51, s51, 147456\n"
  "s_sub_u32 s68, s68, s51\n"
  "s_cmp_eq_u32 s57, 0\n"
  "s_cselect_b32 s50, 28308480, 0\n"
  "s_sub_u32 s68, s68, s50\n"
  "s_mul_i32 s64, s44, 4152\n"
  "s_add_u32 s64, s64, 100352\n"
  "s_and_b32 s52, s44, 1\n"
  "s_mul_i32 s52, s52, 73728\n"
  "s_add_u32 s52, s52, 240\n"
  "s_mov_b32 s65, 294912\n"
  "s_cmp_eq_u32 s57, 1\n"
  "s_cselect_b32 s50, 0xfe548c00, s65\n"
  "s_add_u32 s69, s68, s50\n"
  "v_mul_u32_u24 v162, 5042, v225\n"
  "v_lshrrev_b32 v162, 16, v162\n"
  "v_sub_u32 v161, v225, v162\n"
  "v_lshl_add_u32 v161, v161, 4, s52\n"
  "v_mov_b32 v164, 0x80000000\n"
  "s_mov_b32 s58, 0x2001000\n"
  "s_mov_b32 s59, 0x80040\n"
  "v_cndmask_b32_e64 v161, v161, v164, s[58:59]\n"
  "v_subrev_u32 v239, 80, v161\n"
  "v_subrev_u32 v240, 160, v161\n"
  "v_subrev_u32 v241, 240, v161\n"
  "s_cmp_eq_u32 s67, -1\n"
  "s_cselect_b32 s58, 0x3ffffff, 0\n"
  "s_cselect_b32 s59, 0x0, 0\n"
  "v_cndmask_b32_e64 v242, v161, v164, s[58:59]\n"
  "s_cmp_eq_u32 s67, 0\n"
  "s_cselect_b32 s58, 0x3ffffff, 0\n"
  "s_cselect_b32 s59, 0x0, 0\n"
  "v_cndmask_b32_e64 v238, v161, v164, s[58:59]\n"
  "s_cmp_eq_u32 s67, 22\n"
  "s_cselect_b32 s58, 0x0, 0\n"
  "s_cselect_b32 s59, 0xffffff80, 0\n"
  "v_cndmask_b32_e64 v243, v241, v164, s[58:59]\n"
  "s_cmp_eq_u32 s67, 23\n"
  "s_cselect_b32 s58, 0x0, 0\n"
  "s_cselect_b32 s59, 0xffffff80, 0\n"
  "v_cndmask_b32_e64 v241, v241, v164, s[58:59]\n"
  "s_lshr_b32 s50, %1, 3\n"
  "s_mul_i32 s50, s50, 18\n"
  "v_min_u32 v165, 17, v225\n"
  "v_add_lshl_u32 v165, v165, s50, 7\n"
  "s_cmp_lt_u32 s44, 2\n"
  "s_cselect_b32 s50, -1, GF_start_%=-PC0_%=\n"
  "v_min_u32 v165, s50, v165\n"
  "s_mul_i32 s55, s44, 48\n"
  "s_mul_i32 s54, s44, 3072\n"
  "s_add_u32 s54, s54, 75776\n"
  "v_min_u32 v140, 59, v225\n"
  "v_mul_u32_u24 v141, 43691, v140\n"
  "v_lshrrev_b32 v142, 19, v141\n"
  "v_lshrrev_b32 v141, 18, v141\n"
  "v_mul_u32_u24 v132, 336, v141\n"
  "v_lshl_add_u32 v132, v140, 3, v132\n"
  "v_mul_u32_u24 v161, 192, v142\n"
  "v_sub_u32 v134, v132, v161\n"
  "v_add_u32 v132, v132, v161\n"
  "v_subrev_u32 v134, 576, v134\n"
  "v_subrev_u32 v133, 192, v134\n"
  "s_mov_b32 s58, 0x3f03ffff\n"
  "s_mov_b32 s59, 0xffff03f0\n"
  "v_cndmask_b32_e64 v133, v133, v164, s[58:59]\n"
  "s_mov_b32 s58, 0xfff\n"
  "s_mov_b32 s59, 0xffff0000\n"
  "v_cndmask_b32_e64 v134, v134, v164, s[58:59]\n"
  "v_mul_u32_u24 v135, 912, v141\n"
  "v_lshl_add_u32 v135, v140, 3, v135\n"
  "s_getpc_b64 s[52:53]\n"
  "PC0_%=:\n"
  "s_mov_b32 s38, 4800\n"
  "s_mov_b32 s39, 0x20000\n"
  "s_mov_b32 s42, 1728\n"
  "s_mov_b32 s43, 0x20000\n"
  "s_mov_b32 s26, 28312176\n"
  "s_mov_b32 s27, 0x20000\n"
  "s_cmp_eq_u32 s57, 0\ns_cbranch_scc0 Hs1_%=\nv_mov_b32 v238, v242\nv_mov_b32 v241, v243\nHs1_%=:\n"
  "s_add_u32 m0, s64, 0\n"
  "s_waitcnt lgkmcnt(0)\n"
  "s_cmp_eq_u32 s44, 0\n"
  "s_cselect_b64 s[52:53], s[14:15], s[52:53]\n"
  "s_cmp_eq_u32 s44, 1\n"
  "s_cselect_b64 s[52:53], s[16:17], s[52:53]\n"
  "global_load_dword v166, v165, s[52:53]\n"
  "s_mov_b32 s36, s12\n"
  "s_and_b32 s37, s13, 0xffff\n"
  "s_mov_b32 s40, s10\n"
  "s_and_b32 s41, s11, 0xffff\n"
  "buffer_load_dwordx2 v[120:121], v132, s[36:39], s55 offen\n"
  "buffer_load_dwordx2 v[122:123], v132, s[36:39], s55 offen offset:192\n"
  "buffer_load_dwordx2 v[124:125], v133, s[40:43], s55 offen\n"
  "buffer_load_dwordx2 v[126:127], v134, s[40:43], s55 offen\n"
  "buffer_load_dwordx2 v[128:129], v135, s[36:39], s55 offen offset:768\n"
  "s_sub_u32 s24, s8, 624\n"
  "s_subb_u32 s25, s9, 0\n"
  "s_and_b32 s25, s25, 0xffff\n"
  "buffer_load_dwordx4 v238, s[24:27], s68 offen nt lds\n"
  "buffer_load_dwordx4 v239, s[24:27], s68 offen offset:1040 nt lds\n"
  "buffer_load_dwordx4 v240, s[24:27], s68 offen offset:2080 nt lds\n"
  "buffer_load_dwordx4 v241, s[24:27], s68 offen offset:3120 nt lds\n"
  "s_add_u32 s70, s69, s65\n"
  "v_and_b32 v167, 15, v225\n"
  "v_lshrrev_b32 v174, 4, v225\n"
  "v_and_b32 v176, 3, v225\n"
  "v_bfe_u32 v175, v225, 2, 2\n"
  "v_and_b32 v179, 1, v174\n"
  "v_lshlrev_b32 v179, 1, v179\n"
  "v_and_b32 v168, 2, v174\n"
  "v_mul_u32_u24 v169, 96, v168\n"
  "v_lshl_add_u32 v169, v176, 3, v169\n"
  "v_add_u32 v169, s54, v169\n"
  "v_mov_b32 v170, 61440\n"
  "s_mov_b32 s50, 480\n"
  "s_add_u32 s51, s54, 384\n"
  "v_sub_u32 v162, v179, v175\n"
  "v_add_u32 v161, 0, v162\n"
  "v_cmp_lt_u32 vcc, 4, v161\n"
  "v_mad_u32_u24 v180, v161, s50, v169\n"
  "s_nop 0\n"
  "v_cndmask_b32 v180, v180, v170, vcc\n"
  "v_add_u32 v161, 1, v162\n"
  "v_cmp_lt_u32 vcc, 4, v161\n"
  "v_mad_u32_u24 v181, v161, s50, v169\n"
  "s_nop 0\n"
  "v_cndmask_b32 v181, v181, v170, vcc\n"
  "v_add_u32 v161, 4, v162\n"
  "v_cmp_lt_u32 vcc, 4, v161\n"
  "v_mad_u32_u24 v182, v161, s50, v169\n"
  "s_nop 0\n"
  "v_cndmask_b32 v182, v182, v170, vcc\n"
  "s_cmp_eq_u32 s57, 1\ns_cbranch_scc0 Hs2_%=\nv_mov_b32 v238, v242\nv_mov_b32 v241, v243\nHs2_%=:\n"
  "s_add_u32 m0, s64, 16608\n"
  "s_nop 0\n"
  "buffer_load_dwordx4 v238, s[24:27], s69 offen nt lds\n"
  "buffer_load_dwordx4 v239, s[24:27], s69 offen offset:1040 nt lds\n"
  "buffer_load_dwordx4 v240, s[24:27], s69 offen offset:2080 nt lds\n"
  "buffer_load_dwordx4 v241, s[24:27], s69 offen offset:3120 nt lds\n"
  "v_add_u32 v161, 5, v162\n"
  "v_cmp_lt_u32 vcc, 4, v161\n"
  "v_mad_u32_u24 v183, v161, s50, v169\n"
  "s_nop 0\n"
  "v_cndmask_b32 v183, v183, v170, vcc\n"
  "v_sub_u32 v162, v174, v175\n"
  "v_mov_b32 v169, s51\n"
  "v_add_u32 v161, 0, v162\n"
  "v_cmp_lt_u32 vcc, 4, v161\n"
  "v_mad_u32_u24 v184, v161, s50, v169\n"
  "s_nop 0\n"
  "v_cndmask_b32 v184, v184, v170, vcc\n"
  "v_add_u32 v161, 4, v162\n"
  "v_cmp_lt_u32 vcc, 4, v161\n"
  "v_mad_u32_u24 v185, v161, s50, v169\n"
  "s_nop 0\n"
  "v_cndmask_b32 v185, v185, v170, vcc\n"
  "v_mul_u32_u24 v161, 96, v142\n"
  "v_lshl_add_u32 v138, v140, 5, v161\n"
  "v_add_u32 v138, s54, v138\n"
  "v_min_u32 v161, 5, v141\n"
  "v_mul_u32_u24 v161, 432, v161\n"
  "v_min_u32 v162, 35, v140\n"
  "v_lshl_add_u32 v139, v162, 3, v161\n"
  "v_add_u32 v139, s51, v139\n"
  "v_and_b32 v161, 1, v176\n"
  "v_lshlrev_b32 v161, 4, v161\n"
  "v_lshrrev_b32 v162, 1, v176\n"
  "v_lshlrev_b32 v178, v161, v162\n"
  "v_xor_b32 v162, 1, v162\n"
  "v_lshlrev_b32 v177, v161, v162\n"
  "v_mul_u32_u24 v204, 0xffff, v177\n"
  "v_mul_u32_u24 v205, 0xffff, v178\n"
  "v_mul_u32_u24 v234, 0x5555, v176\n"
  "v_mov_b32 v235, 0x3c2aaaab\n"
  "s_add_u32 s50, s55, 100352\n"
  "v_mul_u32_u24 v236, 4152, v174\n"
  "v_mul_u32_u24 v161, 208, v167\n"
  "v_add3_u32 v236, v236, v161, s50\n"
  "s_lshl_b32 s50, s44, 8\n"
  "s_add_u32 s50, s50, 61440\n"
  "v_lshl_add_u32 v161, v225, 2, s50\n"
  "v_mov_b32 v162, 0\n"
  "ds_write_b32 v161, v162\n"
  "ds_write_b32 v161, v162 offset:1024\n"
  "v_lshlrev_b32 v161, 3, v225\n"
  "v_add_u32 v250, 59392, v161\n"
  "s_lshl_b32 s50, s44, 9\n"
  "v_add_u32 v249, s50, v250\n"
  "s_mul_i32 s50, s44, 768\n"
  "s_add_u32 s50, s50, 75776\n"
  "v_mul_u32_u24 v161, 2816, v174\n"
  "v_lshl_add_u32 v161, v225, 4, v161\n"
  "v_add_u32 v248, s50, v161\n"
  "v_mov_b32 v163, 0x3f80\n"
  "v_cmp_eq_u32 vcc, v174, v175\n"
  "v_add_u32 v162, 1, v179\n"
  "s_mov_b32 s60, -1\n"
  "v_cndmask_b32 v161, 0, v163, vcc\n"
  "v_mul_u32_u24 v228, v161, v177\n"
  "v_mul_u32_u24 v229, v161, v178\n"
  "v_mov_b32 v163, 0xbbe34009\n"
  "v_cmp_eq_u32 vcc, v179, v175\n"
  "s_nop 1\n"
  "v_cndmask_b32 v230, 0, v163, vcc\n"
  "v_cndmask_b32 v231, 0, v163, vcc\n"
  "v_cmp_eq_u32 vcc, v162, v175\n"
  "s_nop 1\n"
  "v_cndmask_b32 v232, 0, v163, vcc\n"
  "v_cndmask_b32 v233, 0, v163, vcc\n"
  "v_mov_b32 v229, 62464\n"
  "s_mov_b32 s61, 0\n"
  "v_mov_b32 v255, 61440\n"
  "v_cndmask_b32_e64 v255, v236, v255, s[60:61]\n"
  "s_cmp_eq_u32 s56, 0\n"
  "s_cselect_b32 s58, -1, 0\n"
  "s_mov_b32 s59, 0\n"
  "v_mov_b32 v237, 61440\n"
  "v_cndmask_b32_e64 v237, v236, v237, s[58:59]\n"
  "s_cmp_eq_u32 s57, 3\ns_cselect_b32 s50, 0xfe548c00, s65\ns_add_u32 s71, s70, s50\n"
  "s_cmp_eq_u32 s57, 4\ns_cselect_b32 s50, 0xfe548c00, s65\ns_add_u32 s72, s71, s50\n"
  "s_add_u32 s73, s72, s65\n"
  "s_cmp_eq_u32 s57, 6\ns_cselect_b32 s50, 0xfe548c00, s65\ns_add_u32 s74, s73, s50\n"
  "s_cmp_eq_u32 s57, 7\ns_cselect_b32 s50, 0xfe548c00, s65\ns_add_u32 s75, s74, s50\n"
  "s_add_u32 s76, s75, s65\n"
  "s_cmp_eq_u32 s57, 9\ns_cselect_b32 s50, 0xfe548c00, s65\ns_add_u32 s77, s76, s50\n"
  "s_mov_b32 s28, s8\n"
  "s_and_b32 s29, s9, 0xffff\n"
  "s_mov_b32 s30, 28311552\n"
  "s_mov_b32 s31, 0x20000\n"
  "s_mov_b32 s36, s18\n"
  "s_and_b32 s37, s19, 0xffff\n"
  "s_mov_b32 s38, 28311552\n"
  "s_mov_b32 s39, 0x20000\n"
  "s_mov_b32 s49, 0xffff0000\n"
  "s_lshl_b32 s51, s56, 1\n"
  "s_mov_b32 s65, 147456\n"
  "s_add_u32 s79, s78, s65\n"
  "s_add_u32 s80, s79, s65\n"
  "s_add_u32 s81, s80, s65\n"
  "s_add_u32 s82, s81, s65\n"
  "s_add_u32 s83, s82, s65\n"
  "s_cmp_eq_u32 s51, 6\ns_cselect_b32 s50, 0xfe524c00, s65\ns_add_u32 s84, s83, s50\n"
  "s_add_u32 s85, s84, s65\n"
  "s_add_u32 s86, s85, s65\n"
  "s_add_u32 s87, s86, s65\n"
  "s_add_u32 s88, s87, s65\n"
  "s_add_u32 s89, s88, s65\n"
  "s_cmp_eq_u32 s51, 12\ns_cselect_b32 s50, 0xfe524c00, s65\ns_add_u32 s90, s89, s50\n"
  "s_add_u32 s91, s90, s65\n"
  "s_add_u32 s92, s91, s65\n"
  "s_add_u32 s93, s92, s65\n"
  "s_add_u32 s94, s93, s65\n"
  "s_add_u32 s95, s94, s65\n"
  "s_lshr_b32 s50, s44, 1\n"
  "s_mul_i32 s50, s50, 73728\n"
  "s_and_b32 s51, s44, 1\n"
  "s_mul_i32 s51, s51, 1536\n"
  "s_add_u32 s50, s50, s51\n"
  "v_lshl_add_u32 v247, v225, 3, s50\n"
  "s_mul_i32 s50, s44, 24\n"
  "v_bfe_u32 v253, v225, 2, 1\n"
  "v_lshlrev_b32 v253, 3, v253\n"
  "v_add_u32 v253, 10240, v253\n"
  "v_lshrrev_b32 v251, 3, v225\n"
  "v_add3_u32 v251, v251, s50, 0\n"
  "v_mul_u32_u24 v252, 43691, v251\n"
  "v_lshrrev_b32 v252, 18, v252\n"
  "v_xor_b32 v252, v252, v225\n"
  "v_and_b32 v252, 3, v252\n"
  "v_lshl_add_u32 v251, v251, 2, v252\n"
  "v_lshl_add_u32 v244, v251, 4, v253\n"
  "v_lshrrev_b32 v251, 3, v225\n"
  "v_add3_u32 v251, v251, s50, 8\n"
  "v_mul_u32_u24 v252, 43691, v251\n"
  "v_lshrrev_b32 v252, 18, v252\n"
  "v_xor_b32 v252, v252, v225\n"
  "v_and_b32 v252, 3, v252\n"
  "v_lshl_add_u32 v251, v251, 2, v252\n"
  "v_lshl_add_u32 v245, v251, 4, v253\n"
  "v_lshrrev_b32 v251, 3, v225\n"
  "v_add3_u32 v251, v251, s50, 16\n"
  "v_mul_u32_u24 v252, 43691, v251\n"
  "v_lshrrev_b32 v252, 18, v252\n"
  "v_xor_b32 v252, v252, v225\n"
  "v_and_b32 v252, 3, v252\n"
  "v_lshl_add_u32 v251, v251, 2, v252\n"
  "v_lshl_add_u32 v246, v251, 4, v253\n"
  "s_mov_b32 s50, 0x05040100\n"
  "s_mov_b32 s52, 0x07060302\n"
  "s_waitcnt lgkmcnt(0)\n"
  "s_barrier\n"
  "s_waitcnt vmcnt(10)\n"
  "v_perm_b32 v144, v124, v120, s50\n"
  "s_waitcnt vmcnt(9)\n"
  "v_perm_b32 v145, v126, v122, s50\n"
  "v_perm_b32 v146, v124, v120, s52\n"
  "v_perm_b32 v147, v126, v122, s52\n"
  "v_perm_b32 v148, v125, v121, s50\n"
  "v_perm_b32 v149, v127, v123, s50\n"
  "v_perm_b32 v150, v125, v121, s52\n"
  "v_perm_b32 v151, v127, v123, s52\n"
  "s_waitcnt vmcnt(8)\n"
  "ds_write_b64 v139, v[128:129]\n"
  "ds_write_b128 v138, v[144:147]\n"
  "ds_write_b128 v138, v[148:151] offset:16\n"
  "ds_read_b64 v[8:9], v184\n"
  "ds_read_b64 v[10:11], v185\n"
  "ds_read_b64 v[20:21], v184 offset:8\n"
  "ds_read_b64 v[22:23], v185 offset:8\n"
  "ds_read_b64 v[32:33], v184 offset:16\n"
  "ds_read_b64 v[34:35], v185 offset:16\n"
  "ds_read_b64 v[44:45], v184 offset:24\n"
  "ds_read_b64 v[46:47], v185 offset:24\n"
  "ds_read_b64 v[56:57], v184 offset:32\n"
  "ds_read_b64 v[58:59], v185 offset:32\n"
  "ds_read_b64 v[68:69], v184 offset:40\n"
  "ds_read_b64 v[70:71], v185 offset:40\n"
  "ds_read_b64 v[0:1], v180\n"
  "ds_read_b64 v[12:13], v180 offset:32\n"
  "ds_read_b64 v[24:25], v180 offset:64\n"
  "ds_read_b64 v[36:37], v180 offset:96\n"
  "ds_read_b64 v[48:49], v180 offset:128\n"
  "ds_read_b64 v[60:61], v180 offset:160\n"
  "ds_read_b64 v[2:3], v181\n"
  "ds_read_b64 v[14:15], v181 offset:32\n"
  "ds_read_b64 v[26:27], v181 offset:64\n"
  "ds_read_b64 v[38:39], v181 offset:96\n"
  "ds_read_b64 v[50:51], v181 offset:128\n"
  "ds_read_b64 v[62:63], v181 offset:160\n"
  "ds_read_b64 v[4:5], v182\n"
  "ds_read_b64 v[16:17], v182 offset:32\n"
  "ds_read_b64 v[28:29], v182 offset:64\n"
  "ds_read_b64 v[40:41], v182 offset:96\n"
  "ds_read_b64 v[52:53], v182 offset:128\n"
  "ds_read_b64 v[64:65], v182 offset:160\n"
  "ds_read_b64 v[6:7], v183\n"
  "ds_read_b64 v[18:19], v183 offset:32\n"
  "ds_read_b64 v[30:31], v183 offset:64\n"
  "ds_read_b64 v[42:43], v183 offset:96\n"
  "ds_read_b64 v[54:55], v183 offset:128\n"
  "ds_read_b64 v[66:67], v183 offset:160\n"
  "s_waitcnt lgkmcnt(14)\n"
  "v_and_b32 v8, v8, v204\n"
  "v_and_b32 v9, v9, v205\n"
  "v_and_b32 v10, v10, v204\n"
  "v_and_b32 v11, v11, v205\n"
  "v_and_b32 v20, v20, v204\n"
  "v_and_b32 v21, v21, v205\n"
  "v_and_b32 v22, v22, v204\n"
  "v_and_b32 v23, v23, v205\n"
  "v_and_b32 v32, v32, v204\n"
  "v_and_b32 v33, v33, v205\n"
  "v_and_b32 v34, v34, v204\n"
  "v_and_b32 v35, v35, v205\n"
  "v_and_b32 v44, v44, v204\n"
  "v_and_b32 v45, v45, v205\n"
  "v_and_b32 v46, v46, v204\n"
  "v_and_b32 v47, v47, v205\n"
  "v_and_b32 v56, v56, v204\n"
  "v_and_b32 v57, v57, v205\n"
  "v_and_b32 v58, v58, v204\n"
  "v_and_b32 v59, v59, v205\n"
  "v_and_b32 v68, v68, v204\n"
  "v_and_b32 v69, v69, v205\n"
  "v_and_b32 v70, v70, v204\n"
  "v_and_b32 v71, v71, v205\n"
  "s_add_u32 m0, s64, 33216\n"
  "s_nop 0\n"
  "buffer_load_dwordx4 v238, s[24:27], s70 offen nt lds\n"
  "buffer_load_dwordx4 v239, s[24:27], s70 offen offset:1040 nt lds\n"
  "buffer_load_dwordx4 v240, s[24:27], s70 offen offset:2080 nt lds\n"
  "buffer_load_dwordx4 v241, s[24:27], s70 offen offset:3120 nt lds\n"
  "s_waitcnt vmcnt(8)\n"
  "ds_append v253 offset:62468\n"
  "s_mov_b32 s48, 0x40000\nHh01_%=:\nds_read_b32 v253, v250 offset:3076\ns_waitcnt lgkmcnt(0)\nv_readfirstlane_b32 s22, v253\ns_sub_u32 s48, s48, 1\ns_cbranch_scc1 Hg01_%=\ns_cmp_ge_u32 s22, 256\ns_cbranch_scc0 Hh01_%=\nHg01_%=:\n"
  "ds_read_b64 v[152:153], v237 offset:832\n"
  "ds_read_b64 v[144:145], v237\n"
  "ds_read_b64 v[146:147], v237 offset:208\n"
  "ds_read_b64 v[148:149], v237 offset:416\n"
  "ds_read_b64 v[150:151], v237 offset:624\n"
  "ds_read_b64 v[162:163], v237 offset:840\n"
  "ds_read_b64 v[154:155], v237 offset:8\n"
  "ds_read_b64 v[156:157], v237 offset:216\n"
  "ds_read_b64 v[158:159], v237 offset:424\n"
  "ds_read_b64 v[160:161], v237 offset:632\n"
  "ds_read_b64 v[172:173], v237 offset:848\n"
  "ds_read_b64 v[164:165], v237 offset:16\n"
  "ds_read_b64 v[166:167], v237 offset:224\n"
  "ds_read_b64 v[168:169], v237 offset:432\n"
  "ds_read_b64 v[170:171], v237 offset:640\n"
  "s_waitcnt lgkmcnt(14)\n"
  "v_mfma_f32_16x16x16_bf16 v[72:75], v[8:9], v[152:153], 0\n"
  "ds_read_b64 v[182:183], v237 offset:856\n"
  "ds_read_b64 v[174:175], v237 offset:24\n"
  "ds_read_b64 v[176:177], v237 offset:232\n"
  "ds_read_b64 v[178:179], v237 offset:440\n"
  "ds_read_b64 v[180:181], v237 offset:648\n"
  "s_waitcnt lgkmcnt(14)\n"
  "v_mfma_f32_16x16x16_bf16 v[76:79], v[20:21], v[162:163], 0\n"
  "ds_read_b64 v[192:193], v237 offset:864\n"
  "ds_read_b64 v[184:185], v237 offset:32\n"
  "ds_read_b64 v[186:187], v237 offset:240\n"
  "ds_read_b64 v[188:189], v237 offset:448\n"
  "ds_read_b64 v[190:191], v237 offset:656\n"
  "s_waitcnt lgkmcnt(14)\n"
  "v_mfma_f32_16x16x16_bf16 v[80:83], v[32:33], v[172:173], 0\n"
  "v_smfmac_f32_16x16x64_bf16 v[72:75], v[0:3], v[144:151], v234\n"
  "ds_read_b64 v[202:203], v237 offset:872\n"
  "ds_read_b64 v[194:195], v237 offset:40\n"
  "ds_read_b64 v[196:197], v237 offset:248\n"
  "ds_read_b64 v[198:199], v237 offset:456\n"
  "ds_read_b64 v[200:201], v237 offset:664\n"
  "s_waitcnt lgkmcnt(14)\n"
  "v_mfma_f32_16x16x16_bf16 v[84:87], v[44:45], v[182:183], 0\n"
  "v_smfmac_f32_16x16x64_bf16 v[76:79], v[12:15], v[154:161], v234\n"
  "s_waitcnt lgkmcnt(9)\n"
  "v_mfma_f32_16x16x16_bf16 v[88:91], v[56:57], v[192:193], 0\n"
  "v_smfmac_f32_16x16x64_bf16 v[80:83], v[24:27], v[164:171], v234\n"
  "s_waitcnt lgkmcnt(4)\n"
  "v_mfma_f32_16x16x16_bf16 v[92:95], v[68:69], v[202:203], 0\n"
  "v_smfmac_f32_16x16x64_bf16 v[84:87], v[36:39], v[174:181], v234\n"
  "s_nop 1\n"
  "v_smfmac_f32_16x16x64_bf16 v[88:91], v[48:51], v[184:191], v234\n"
  "s_waitcnt lgkmcnt(0)\n"
  "s_nop 0\n"
  "v_smfmac_f32_16x16x64_bf16 v[92:95], v[60:63], v[194:201], v234\n"
  "s_waitcnt vmcnt(4)\n"
  "s_barrier\n"
  "ds_read_b64 v[152:153], v236 offset:17440\n"
  "ds_read_b64 v[144:145], v236 offset:16608\n"
  "ds_read_b64 v[146:147], v236 offset:16816\n"
  "ds_read_b64 v[148:149], v236 offset:17024\n"
  "ds_read_b64 v[150:151], v236 offset:17232\n"
  "ds_read_b64 v[162:163], v236 offset:17448\n"
  "ds_read_b64 v[154:155], v236 offset:16616\n"
  "ds_read_b64 v[156:157], v236 offset:16824\n"
  "ds_read_b64 v[158:159], v236 offset:17032\n"
  "ds_read_b64 v[160:161], v236 offset:17240\n"
  "ds_read_b64 v[172:173], v236 offset:17456\n"
  "ds_read_b64 v[164:165], v236 offset:16624\n"
  "ds_read_b64 v[166:167], v236 offset:16832\n"
  "ds_read_b64 v[168:169], v236 offset:17040\n"
  "ds_read_b64 v[170:171], v236 offset:17248\n"
  "s_waitcnt lgkmcnt(10)\n"
  "v_smfmac_f32_16x16x64_bf16 v[72:75], v[4:7], v[144:151], v234\n"
  "ds_read_b64 v[182:183], v236 offset:17464\n"
  "ds_read_b64 v[174:175], v236 offset:16632\n"
  "ds_read_b64 v[176:177], v236 offset:16840\n"
  "ds_read_b64 v[178:179], v236 offset:17048\n"
  "ds_read_b64 v[180:181], v236 offset:17256\n"
  "s_waitcnt lgkmcnt(10)\n"
  "v_smfmac_f32_16x16x64_bf16 v[76:79], v[16:19], v[154:161], v234\n"
  "ds_read_b64 v[192:193], v236 offset:17472\n"
  "ds_read_b64 v[184:185], v236 offset:16640\n"
  "ds_read_b64 v[186:187], v236 offset:16848\n"
  "ds_read_b64 v[188:189], v236 offset:17056\n"
  "ds_read_b64 v[190:191], v236 offset:17264\n"
  "s_waitcnt lgkmcnt(10)\n"
  "v_smfmac_f32_16x16x64_bf16 v[80:83], v[28:31], v[164:171], v234\n"
  "v_mfma_f32_16x16x16_bf16 v[72:75], v[10:11], v[152:153], v[72:75]\n"
  "ds_read_b64 v[202:203], v236 offset:17480\n"
  "ds_read_b64 v[194:195], v236 offset:16648\n"
  "ds_read_b64 v[196:197], v236 offset:16856\n"
  "ds_read_b64 v[198:199], v236 offset:17064\n"
  "ds_read_b64 v[200:201], v236 offset:17272\n"
  "s_waitcnt lgkmcnt(10)\n"
  "v_smfmac_f32_16x16x64_bf16 v[84:87], v[40:43], v[174:181], v234\n"
  "v_mfma_f32_16x16x16_bf16 v[76:79], v[22:23], v[162:163], v[76:79]\n"
  "s_waitcnt lgkmcnt(5)\n"
  "v_smfmac_f32_16x16x64_bf16 v[88:91], v[52:55], v[184:191], v234\n"
  "v_mfma_f32_16x16x16_bf16 v[80:83], v[34:35], v[172:173], v[80:83]\n"
  "s_waitcnt lgkmcnt(0)\n"
  "v_smfmac_f32_16x16x64_bf16 v[92:95], v[64:67], v[194:201], v234\n"
  "v_mfma_f32_16x16x16_bf16 v[84:87], v[46:47], v[182:183], v[84:87]\n"
  "s_nop 1\n"
  "v_mfma_f32_16x16x16_bf16 v[88:91], v[58:59], v[192:193], v[88:91]\n"
  "s_nop 1\n"
  "v_mfma_f32_16x16x16_bf16 v[92:95], v[70:71], v[202:203], v[92:95]\n"
  "v_pk_mul_f32 v[216:217], v[72:73], v[72:73]\n"
  "v_pk_add_f32 v[214:215], v[72:73], v[74:75]\n"
  "v_pk_fma_f32 v[216:217], v[74:75], v[74:75], v[216:217]\n"
  "v_pk_fma_f32 v[216:217], v[76:77], v[76:77], v[216:217]\n"
  "v_pk_add_f32 v[214:215], v[214:215], v[76:77]\n"
  "v_pk_fma_f32 v[216:217], v[78:79], v[78:79], v[216:217]\n"
  "v_pk_add_f32 v[214:215], v[214:215], v[78:79]\n"
  "v_pk_fma_f32 v[216:217], v[80:81], v[80:81], v[216:217]\n"
  "v_pk_add_f32 v[214:215], v[214:215], v[80:81]\n"
  "v_pk_fma_f32 v[216:217], v[82:83], v[82:83], v[216:217]\n"
  "v_pk_add_f32 v[214:215], v[214:215], v[82:83]\n"
  "v_pk_fma_f32 v[216:217], v[84:85], v[84:85], v[216:217]\n"
  "v_pk_add_f32 v[214:215], v[214:215], v[84:85]\n"
  "v_pk_fma_f32 v[216:217], v[86:87], v[86:87], v[216:217]\n"
  "v_pk_add_f32 v[214:215], v[214:215], v[86:87]\n"
  "v_pk_fma_f32 v[216:217], v[88:89], v[88:89], v[216:217]\n"
  "v_pk_add_f32 v[214:215], v[214:215], v[88:89]\n"
  "v_pk_fma_f32 v[216:217], v[90:91], v[90:91], v[216:217]\n"
  "v_pk_add_f32 v[214:215], v[214:215], v[90:91]\n"
  "v_pk_fma_f32 v[216:217], v[92:93], v[92:93], v[216:217]\n"
  "v_pk_add_f32 v[214:215], v[214:215], v[92:93]\n"
  "v_pk_fma_f32 v[216:217], v[94:95], v[94:95], v[216:217]\n"
  "v_pk_add_f32 v[214:215], v[214:215], v[94:95]\n"
  "v_add_f32 v223, v216, v217\n"
  "v_add_f32 v222, v214, v215\n"
  "ds_write_b64 v249, v[222:223]\n"
  "ds_append v253 offset:62468\n"
  "s_mov_b32 s48, 0x40000\nHh02_%=:\nds_read_b32 v253, v250 offset:3076\nds_read_b64 v[214:215], v250\nds_read_b64 v[216:217], v250 offset:512\nds_read_b64 v[218:219], v250 offset:1024\nds_read_b64 v[220:221], v250 offset:1536\ns_waitcnt lgkmcnt(0)\nv_readfirstlane_b32 s22, v253\ns_sub_u32 s48, s48, 1\ns_cbranch_scc1 Hg02_%=\ns_cmp_ge_u32 s22, 512\ns_cbranch_scc0 Hh02_%=\nHg02_%=:\n"
  "v_add_f32 v214, v214, v216\n"
  "v_add_f32 v215, v215, v217\n"
  "v_add_f32 v218, v218, v220\n"
  "v_add_f32 v219, v219, v221\n"
  "v_add_f32 v214, v214, v218\n"
  "v_add_f32 v215, v215, v219\n"
  "v_mul_f32 v251, v235, v214\n"
  "v_fmaak_f32 v252, v235, v215, 0x3727c5ac\n"
  "v_fma_f32 v252, -v251, v251, v252\n"
  "v_max_f32 v252, 0x3727c5ac, v252\n"
  "v_rsq_f32 v252, v252\n"
  "s_nop 0\n"
  "v_mul_f32 v226, s47, v252\n"
  "v_mul_f32_e64 v227, -v251, v226\n"
  "v_pk_fma_f32 v[72:73], v[72:73], v[226:227], v[226:227] op_sel:[0,0,1] op_sel_hi:[1,0,1]\n"
  "v_pk_fma_f32 v[74:75], v[74:75], v[226:227], v[226:227] op_sel:[0,0,1] op_sel_hi:[1,0,1]\n"
  "v_pk_fma_f32 v[76:77], v[76:77], v[226:227], v[226:227] op_sel:[0,0,1] op_sel_hi:[1,0,1]\n"
  "v_pk_fma_f32 v[78:79], v[78:79], v[226:227], v[226:227] op_sel:[0,0,1] op_sel_hi:[1,0,1]\n"
  "v_cvt_pk_bf16_f32 v214, v72, v73\n"
  "v_cvt_pk_bf16_f32 v215, v74, v75\n"
  "v_cvt_pk_bf16_f32 v216, v76, v77\n"
  "v_cvt_pk_bf16_f32 v217, v78, v79\n"
  "ds_write_b128 v248, v[214:217]\n"
  "v_pk_fma_f32 v[80:81], v[80:81], v[226:227], v[226:227] op_sel:[0,0,1] op_sel_hi:[1,0,1]\n"
  "v_pk_fma_f32 v[82:83], v[82:83], v[226:227], v[226:227] op_sel:[0,0,1] op_sel_hi:[1,0,1]\n"
  "v_pk_fma_f32 v[84:85], v[84:85], v[226:227], v[226:227] op_sel:[0,0,1] op_sel_hi:[1,0,1]\n"
  "v_pk_fma_f32 v[86:87], v[86:87], v[226:227], v[226:227] op_sel:[0,0,1] op_sel_hi:[1,0,1]\n"
  "v_cvt_pk_bf16_f32 v218, v80, v81\n"
  "v_cvt_pk_bf16_f32 v219, v82, v83\n"
  "v_cvt_pk_bf16_f32 v220, v84, v85\n"
  "v_cvt_pk_bf16_f32 v221, v86, v87\n"
  "ds_write_b128 v248, v[218:221] offset:256\n"
  "v_pk_fma_f32 v[88:89], v[88:89], v[226:227], v[226:227] op_sel:[0,0,1] op_sel_hi:[1,0,1]\n"
  "v_pk_fma_f32 v[90:91], v[90:91], v[226:227], v[226:227] op_sel:[0,0,1] op_sel_hi:[1,0,1]\n"
  "v_pk_fma_f32 v[92:93], v[92:93], v[226:227], v[226:227] op_sel:[0,0,1] op_sel_hi:[1,0,1]\n"
  "v_pk_fma_f32 v[94:95], v[94:95], v[226:227], v[226:227] op_sel:[0,0,1] op_sel_hi:[1,0,1]\n"
  "v_cvt_pk_bf16_f32 v214, v88, v89\n"
  "v_cvt_pk_bf16_f32 v215, v90, v91\n"
  "v_cvt_pk_bf16_f32 v216, v92, v93\n"
  "v_cvt_pk_bf16_f32 v217, v94, v95\n"
  "ds_write_b128 v248, v[214:217] offset:512\n"
  "v_mfma_f32_16x16x16_bf16 v[96:99], v[8:9], v[152:153], 0\n"
  "v_mfma_f32_16x16x16_bf16 v[100:103], v[20:21], v[162:163], 0\n"
  "v_mfma_f32_16x16x16_bf16 v[104:107], v[32:33], v[172:173], 0\n"
  "s_nop 3\n"
  "v_smfmac_f32_16x16x64_bf16 v[96:99], v[0:3], v[144:151], v234\n"
  "v_mfma_f32_16x16x16_bf16 v[108:111], v[44:45], v[182:183], 0\n"
  "v_smfmac_f32_16x16x64_bf16 v[100:103], v[12:15], v[154:161], v234\n"
  "v_mfma_f32_16x16x16_bf16 v[112:115], v[56:57], v[192:193], 0\n"
  "v_smfmac_f32_16x16x64_bf16 v[104:107], v[24:27], v[164:171], v234\n"
  "v_mfma_f32_16x16x16_bf16 v[116:119], v[68:69], v[202:203], 0\n"
  "s_nop 1\n"
  "v_smfmac_f32_16x16x64_bf16 v[108:111], v[36:39], v[174:181], v234\n"
  "s_nop 0\n"
  "v_smfmac_f32_16x16x64_bf16 v[112:115], v[48:51], v[184:191], v234\n"
  "s_nop 0\n"
  "v_smfmac_f32_16x16x64_bf16 v[116:119], v[60:63], v[194:201], v234\n"
  "s_waitcnt vmcnt(0) lgkmcnt(0)\n"
  "s_barrier\n"
  "s_setprio 1\n"
  "ds_read_b64 v[152:153], v236 offset:34048\n"
  "ds_read_b64 v[144:145], v236 offset:33216\n"
  "ds_read_b64 v[146:147], v236 offset:33424\n"
  "ds_read_b64 v[148:149], v236 offset:33632\n"
  "ds_read_b64 v[150:151], v236 offset:33840\n"
  "ds_read_b64 v[162:163], v236 offset:34056\n"
  "ds_read_b64 v[154:155], v236 offset:33224\n"
  "ds_read_b64 v[156:157], v236 offset:33432\n"
  "ds_read_b64 v[158:159], v236 offset:33640\n"
  "ds_read_b64 v[160:161], v236 offset:33848\n"
  "ds_read_b64 v[172:173], v236 offset:34064\n"
  "ds_read_b64 v[164:165], v236 offset:33232\n"
  "ds_read_b64 v[166:167], v236 offset:33440\n"
  "ds_read_b64 v[168:169], v236 offset:33648\n"
  "ds_read_b64 v[170:171], v236 offset:33856\n"
  "s_waitcnt lgkmcnt(10)\n"
  "v_smfmac_f32_16x16x64_bf16 v[96:99], v[4:7], v[144:151], v234\n"
  "ds_read_b64 v[182:183], v236 offset:34072\n"
  "ds_read_b64 v[174:175], v236 offset:33240\n"
  "ds_read_b64 v[176:177], v236 offset:33448\n"
  "ds_read_b64 v[178:179], v236 offset:33656\n"
  "ds_read_b64 v[180:181], v236 offset:33864\n"
  "s_waitcnt lgkmcnt(10)\n"
  "v_smfmac_f32_16x16x64_bf16 v[100:103], v[16:19], v[154:161], v234\n"
  "ds_read_b64 v[192:193], v236 offset:34080\n"
  "ds_read_b64 v[184:185], v236 offset:33248\n"
  "ds_read_b64 v[186:187], v236 offset:33456\n"
  "ds_read_b64 v[188:189], v236 offset:33664\n"
  "ds_read_b64 v[190:191], v236 offset:33872\n"
  "s_waitcnt lgkmcnt(10)\n"
  "v_smfmac_f32_16x16x64_bf16 v[104:107], v[28:31], v[164:171], v234\n"
  "v_mfma_f32_16x16x16_bf16 v[96:99], v[10:11], v[152:153], v[96:99]\n"
  "ds_read_b64 v[202:203], v236 offset:34088\n"
  "ds_read_b64 v[194:195], v236 offset:33256\n"
  "ds_read_b64 v[196:197], v236 offset:33464\n"
  "ds_read_b64 v[198:199], v236 offset:33672\n"
  "ds_read_b64 v[200:201], v236 offset:33880\n"
  "s_waitcnt lgkmcnt(10)\n"
  "v_smfmac_f32_16x16x64_bf16 v[108:111], v[40:43], v[174:181], v234\n"
  "v_mfma_f32_16x16x16_bf16 v[100:103], v[22:23], v[162:163], v[100:103]\n"
  "v_mul_f32 v223, v96, v96\n"
  "v_add_f32 v222, v96, v97\n"
  "v_fmac_f32 v223, v97, v97\n"
  "v_add_f32 v222, v222, v98\n"
  "v_fmac_f32 v223, v98, v98\n"
  "v_add_f32 v222, v222, v99\n"
  "v_fmac_f32 v223, v99, v99\n"
  "s_waitcnt lgkmcnt(5)\n"
  "v_smfmac_f32_16x16x64_bf16 v[112:115], v[52:55], v[184:191], v234\n"
  "v_mfma_f32_16x16x16_bf16 v[104:107], v[34:35], v[172:173], v[104:107]\n"
  "v_add_f32 v222, v222, v100\n"
  "v_fmac_f32 v223, v100, v100\n"
  "v_add_f32 v222, v222, v101\n"
  "v_fmac_f32 v223, v101, v101\n"
  "v_add_f32 v222, v222, v102\n"
  "v_fmac_f32 v223, v102, v102\n"
  "v_add_f32 v222, v222, v103\n"
  "v_fmac_f32 v223, v103, v103\n"
  "s_waitcnt lgkmcnt(0)\n"
  "v_smfmac_f32_16x16x64_bf16 v[116:119], v[64:67], v[194:201], v234\n"
  "v_mfma_f32_16x16x16_bf16 v[108:111], v[46:47], v[182:183], v[108:111]\n"
  "v_add_f32 v222, v222, v104\n"
  "v_fmac_f32 v223, v104, v104\n"
  "v_add_f32 v222, v222, v105\n"
  "v_fmac_f32 v223, v105, v105\n"
  "v_add_f32 v222, v222, v106\n"
  "v_fmac_f32 v223, v106, v106\n"
  "v_add_f32 v222, v222, v107\n"
  "v_fmac_f32 v223, v107, v107\n"
  "v_mfma_f32_16x16x16_bf16 v[112:115], v[58:59], v[192:193], v[112:115]\n"
  "v_add_f32 v222, v222, v108\n"
  "v_fmac_f32 v223, v108, v108\n"
  "v_add_f32 v222, v222, v109\n"
  "v_fmac_f32 v223, v109, v109\n"
  "v_add_f32 v222, v222, v110\n"
  "v_fmac_f32 v223, v110, v110\n"
  "v_add_f32 v222, v222, v111\n"
  "v_fmac_f32 v223, v111, v111\n"
  "v_mfma_f32_16x16x16_bf16 v[116:119], v[70:71], v[202:203], v[116:119]\n"
  "v_add_f32 v222, v222, v112\n"
  "v_fmac_f32 v223, v112, v112\n"
  "v_add_f32 v222, v222, v113\n"
  "v_fmac_f32 v223, v113, v113\n"
  "v_add_f32 v222, v222, v114\n"
  "v_fmac_f32 v223, v114, v114\n"
  "v_add_f32 v222, v222, v115\n"
  "v_fmac_f32 v223, v115, v115\n"
  "v_add_f32 v222, v222, v116\n"
  "v_fmac_f32 v223, v116, v116\n"
  "v_add_f32 v222, v222, v117\n"
  "v_fmac_f32 v223, v117, v117\n"
  "v_add_f32 v222, v222, v118\n"
  "v_fmac_f32 v223, v118, v118\n"
  "v_add_f32 v222, v222, v119\n"
  "v_fmac_f32 v223, v119, v119\n"
  "s_barrier\n"
  "s_cmp_eq_u32 s57, 3\ns_cbranch_scc0 Hs3_%=\nv_mov_b32 v238, v242\nv_mov_b32 v241, v243\nHs3_%=:\n"
  "s_add_u32 m0, s64, 0\n"
  "s_nop 0\n"
  "buffer_load_dwordx4 v238, s[24:27], s71 offen lds\n"
  "buffer_load_dwordx4 v239, s[24:27], s71 offen offset:1040 lds\n"
  "buffer_load_dwordx4 v240, s[24:27], s71 offen offset:2080 lds\n"
  "buffer_load_dwordx4 v241, s[24:27], s71 offen offset:3120 lds\n"
  "v_mfma_f32_16x16x16_bf16 v[120:123], v[8:9], v[152:153], 0\n"
  "v_mfma_f32_16x16x16_bf16 v[124:127], v[20:21], v[162:163], 0\n"
  "v_mfma_f32_16x16x16_bf16 v[128:131], v[32:33], v[172:173], 0\n"
  "s_nop 3\n"
  "v_smfmac_f32_16x16x64_bf16 v[120:123], v[0:3], v[144:151], v234\n"
  "v_mfma_f32_16x16x16_bf16 v[132:135], v[44:45], v[182:183], 0\n"
  "v_smfmac_f32_16x16x64_bf16 v[124:127], v[12:15], v[154:161], v234\n"
  "v_mfma_f32_16x16x16_bf16 v[136:139], v[56:57], v[192:193], 0\n"
  "v_smfmac_f32_16x16x64_bf16 v[128:131], v[24:27], v[164:171], v234\n"
  "v_mfma_f32_16x16x16_bf16 v[140:143], v[68:69], v[202:203], 0\n"
  "s_nop 1\n"
  "v_smfmac_f32_16x16x64_bf16 v[132:135], v[36:39], v[174:181], v234\n"
  "s_nop 0\n"
  "v_smfmac_f32_16x16x64_bf16 v[136:139], v[48:51], v[184:191], v234\n"
  "s_nop 0\n"
  "v_smfmac_f32_16x16x64_bf16 v[140:143], v[60:63], v[194:201], v234\n"
  "s_setprio 3\n"
  "ds_write_b64 v249, v[222:223]\n"
  "ds_append v253 offset:62468\n"
  "s_mov_b32 s48, 0x40000\nHh03_%=:\nds_read_b32 v253, v250 offset:3076\nds_read_b64 v[214:215], v250\nds_read_b64 v[216:217], v250 offset:512\nds_read_b64 v[218:219], v250 offset:1024\nds_read_b64 v[220:221], v250 offset:1536\ns_waitcnt lgkmcnt(0)\nv_readfirstlane_b32 s22, v253\ns_sub_u32 s48, s48, 1\ns_cbranch_scc1 Hg03_%=\ns_cmp_ge_u32 s22, 768\ns_cbranch_scc0 Hh03_%=\nHg03_%=:\n"
  "v_add_f32 v214, v214, v216\n"
  "v_add_f32 v215, v215, v217\n"
  "v_add_f32 v218, v218, v220\n"
  "v_add_f32 v219, v219, v221\n"
  "v_add_f32 v214, v214, v218\n"
  "v_add_f32 v215, v215, v219\n"
  "v_mul_f32 v251, v235, v214\n"
  "v_fmaak_f32 v252, v235, v215, 0x3727c5ac\n"
  "v_fma_f32 v252, -v251, v251, v252\n"
  "v_max_f32 v252, 0x3727c5ac, v252\n"
  "v_rsq_f32 v252, v252\n"
  "s_nop 0\n"
  "v_mul_f32 v226, s47, v252\n"
  "v_mul_f32_e64 v227, -v251, v226\n"
  "v_fma_f32 v96, v96, v226, v227\n"
  "v_fma_f32 v97, v97, v226, v227\n"
  "v_fma_f32 v98, v98, v226, v227\n"
  "v_fma_f32 v99, v99, v226, v227\n"
  "v_fma_f32 v100, v100, v226, v227\n"
  "v_fma_f32 v101, v101, v226, v227\n"
  "v_fma_f32 v102, v102, v226, v227\n"
  "v_fma_f32 v103, v103, v226, v227\n"
  "v_cvt_pk_bf16_f32 v214, v96, v97\n"
  "v_cvt_pk_bf16_f32 v215, v98, v99\n"
  "v_cvt_pk_bf16_f32 v216, v100, v101\n"
  "v_cvt_pk_bf16_f32 v217, v102, v103\n"
  "ds_write_b128 v248, v[214:217] offset:12288\n"
  "v_fma_f32 v104, v104, v226, v227\n"
  "v_fma_f32 v105, v105, v226, v227\n"
  "v_fma_f32 v106, v106, v226, v227\n"
  "v_fma_f32 v107, v107, v226, v227\n"
  "v_fma_f32 v108, v108, v226, v227\n"
  "v_fma_f32 v109, v109, v226, v227\n"
  "v_fma_f32 v110, v110, v226, v227\n"
  "v_fma_f32 v111, v111, v226, v227\n"
  "v_cvt_pk_bf16_f32 v218, v104, v105\n"
  "v_cvt_pk_bf16_f32 v219, v106, v107\n"
  "v_cvt_pk_bf16_f32 v220, v108, v109\n"
  "v_cvt_pk_bf16_f32 v221, v110, v111\n"
  "ds_write_b128 v248, v[218:221] offset:12544\n"
  "v_fma_f32 v112, v112, v226, v227\n"
  "v_fma_f32 v113, v113, v226, v227\n"
  "v_fma_f32 v114, v114, v226, v227\n"
  "v_fma_f32 v115, v115, v226, v227\n"
  "v_fma_f32 v116, v116, v226, v227\n"
  "v_fma_f32 v117, v117, v226, v227\n"
  "v_fma_f32 v118, v118, v226, v227\n"
  "v_fma_f32 v119, v119, v226, v227\n"
  "v_cvt_pk_bf16_f32 v214, v112, v113\n"
  "v_cvt_pk_bf16_f32 v215, v114, v115\n"
  "v_cvt_pk_bf16_f32 v216, v116, v117\n"
  "v_cvt_pk_bf16_f32 v217, v118, v119\n"
  "ds_write_b128 v248, v[214:217] offset:12800\n"
  "ds_append v224 offset:62464\n"
  "s_cmp_eq_u32 s57, 4\ns_cbranch_scc0 Hs4_%=\nv_mov_b32 v238, v242\nv_mov_b32 v241, v243\nHs4_%=:\n"
  "s_add_u32 m0, s64, 16608\n"
  "s_nop 0\n"
  "buffer_load_dwordx4 v238, s[24:27], s72 offen lds\n"
  "buffer_load_dwordx4 v239, s[24:27], s72 offen offset:1040 lds\n"
  "buffer_load_dwordx4 v240, s[24:27], s72 offen offset:2080 lds\n"
  "buffer_load_dwordx4 v241, s[24:27], s72 offen offset:3120 lds\n"
  "buffer_load_dwordx2 v[190:191], v247, s[28:31], s78 offen nt\n"
  "buffer_load_dwordx2 v[192:193], v247, s[28:31], s78 offen offset:512 nt\n"
  "buffer_load_dwordx2 v[194:195], v247, s[28:31], s78 offen offset:1024 nt\n"
  "s_waitcnt vmcnt(7)\n"
  "ds_append v253 offset:62468\n"
  "s_mov_b32 s48, 0x40000\nHh04_%=:\nds_read_b32 v253, v250 offset:3076\ns_waitcnt lgkmcnt(0)\nv_readfirstlane_b32 s22, v253\ns_sub_u32 s48, s48, 1\ns_cbranch_scc1 Hg04_%=\ns_cmp_ge_u32 s22, 1024\ns_cbranch_scc0 Hh04_%=\nHg04_%=:\n"
  "s_branch H_cont_%=\n"
  "G_entry_%=:\n"
  "v_mbcnt_lo_u32_b32 v144, -1, 0\n"
  "v_mbcnt_hi_u32_b32 v144, -1, v144\n"
  "s_mul_i32 s52, s44, 6144\n"
  "s_add_u32 s51, s52, 10240\n"
  "s_mov_b32 s65, 75776\n"
  "s_cmp_lt_u32 s44, 2\n"
  "s_cselect_b32 s65, 150176, s65\n"
  "s_add_u32 s52, s52, s65\n"
  "s_lshl_b32 s56, s44, 6\n"
  "s_mov_b32 s57, 0x6f800000\n"
  "s_lshl_b32 s50, s44, 6\n"
  "s_mul_i32 s53, s44, 6144\n"
  "s_mov_b32 s54, 12288\n"
  "s_mov_b32 s55, 8\n"
  "v_lshrrev_b32 v153, 4, v144\n"
  "v_bfe_u32 v155, v144, 2, 2\n"
  "v_and_b32 v156, 3, v144\n"
  "v_and_b32 v148, 2, v153\n"
  "v_xor_b32 v148, v148, v156\n"
  "v_lshrrev_b32 v176, 2, v144\n"
  "v_mul_u32_u24 v176, 768, v176\n"
  "v_lshl_add_u32 v176, v148, 4, v176\n"
  "v_lshl_add_u32 v178, v153, 3, v155\n"
  "v_lshlrev_b32 v178, 6, v178\n"
  "v_lshl_add_u32 v178, v156, 3, v178\n"
  "v_and_b32 v148, 1, v153\n"
  "v_lshl_add_u32 v178, v148, 5, v178\n"
  "v_xor_b32 v179, 32, v178\n"
  "v_add_u32 v180, s52, v178\n"
  "v_add_u32 v178, s51, v178\n"
  "v_add_u32 v181, s52, v179\n"
  "v_add_u32 v179, s51, v179\n"
  "v_lshrrev_b32 v250, 2, v144\n"
  "v_mul_u32_u24 v250, 192, v250\n"
  "v_lshl_add_u32 v250, v156, 3, v250\n"
  "v_lshl_add_u32 v250, v148, 5, v250\n"
  "v_add_u32 v250, s51, v250\n"
  "v_xor_b32 v254, 32, v250\n"
  "v_add_u32 v148, 0, v144\n"
  "v_mul_u32_u24 v148, 43691, v148\n"
  "v_bfe_u32 v148, v148, 21, 1\n"
  "v_lshlrev_b32 v148, 1, v148\n"
  "v_xor_b32 v148, v148, v144\n"
  "v_lshlrev_b32 v172, 4, v148\n"
  "v_add_u32 v148, 64, v144\n"
  "v_mul_u32_u24 v148, 43691, v148\n"
  "v_bfe_u32 v148, v148, 21, 1\n"
  "v_lshlrev_b32 v148, 1, v148\n"
  "v_xor_b32 v148, v148, v144\n"
  "v_lshlrev_b32 v173, 4, v148\n"
  "v_add_u32 v148, 128, v144\n"
  "v_mul_u32_u24 v148, 43691, v148\n"
  "v_bfe_u32 v148, v148, 21, 1\n"
  "v_lshlrev_b32 v148, 1, v148\n"
  "v_xor_b32 v148, v148, v144\n"
  "v_lshlrev_b32 v174, 4, v148\n"
  "v_lshlrev_b32 v148, 4, v144\n"
  "v_add_u32 v248, 75776, v148\n"
  "v_and_b32 v159, 15, v144\n"
  "v_bfe_u32 v148, v144, 1, 2\n"
  "v_xor_b32 v148, v148, v153\n"
  "v_mul_u32_u24 v249, 192, v159\n"
  "v_lshl_add_u32 v249, v148, 4, v249\n"
  "v_add_u32 v249, s51, v249\n"
  "v_mov_b32 v184, 0x30003000\n"
  "v_bfe_i32 v148, v144, 1, 1\n"
  "v_bfi_b32 v185, v148, 0, v184\n"
  "v_mov_b32 v188, 0\n"
  "v_mov_b32 v189, 0\n"
  "v_add_u32 v150, s56, v144\n"
  "v_lshlrev_b32 v151, 18, v150\n"
  "v_cmp_ne_u32 vcc, 0, v150\n"
  "v_lshlrev_b32 v150, 3, v150\n"
  "v_add_u32 v152, 0x10000, v150\n"
  "v_add_u32 v72, 0x8000, v151\n"
  "v_add_u32 v73, 0x18000, v151\n"
  "v_add_u32 v74, 0x28000, v151\n"
  "v_add_u32 v75, 0x38000, v151\n"
  "v_add_u32 v76, 0x4008000, v151\n"
  "v_add_u32 v77, 0x4018000, v151\n"
  "v_add_u32 v78, 0x4028000, v151\n"
  "v_add_u32 v79, 0x4038000, v151\n"
  "v_add_u32 v80, 0x8008000, v151\n"
  "v_add_u32 v81, 0x8018000, v151\n"
  "v_add_u32 v82, 0x8028000, v151\n"
  "v_add_u32 v83, 0x8038000, v151\n"
  "v_add_u32 v84, 0xc008000, v151\n"
  "v_add_u32 v85, 0xc018000, v151\n"
  "v_add_u32 v86, 0xc028000, v151\n"
  "v_add_u32 v87, 0xc038000, v151\n"
  "v_add_u32 v88, 0x10008000, v151\n"
  "v_add_u32 v89, 0x10018000, v151\n"
  "v_add_u32 v90, 0x10028000, v151\n"
  "v_add_u32 v91, 0x10038000, v151\n"
  "v_cndmask_b32 v72, 0, v72, vcc\n"
  "v_pk_mul_f32 v[72:73], v[72:73], s[56:57] op_sel:[0,1] op_sel_hi:[1,1]\n"
  "v_pk_mul_f32 v[74:75], v[74:75], s[56:57] op_sel:[0,1] op_sel_hi:[1,1]\n"
  "v_pk_mul_f32 v[76:77], v[76:77], s[56:57] op_sel:[0,1] op_sel_hi:[1,1]\n"
  "v_pk_mul_f32 v[78:79], v[78:79], s[56:57] op_sel:[0,1] op_sel_hi:[1,1]\n"
  "v_pk_mul_f32 v[80:81], v[80:81], s[56:57] op_sel:[0,1] op_sel_hi:[1,1]\n"
  "v_pk_mul_f32 v[82:83], v[82:83], s[56:57] op_sel:[0,1] op_sel_hi:[1,1]\n"
  "v_pk_mul_f32 v[84:85], v[84:85], s[56:57] op_sel:[0,1] op_sel_hi:[1,1]\n"
  "v_pk_mul_f32 v[86:87], v[86:87], s[56:57] op_sel:[0,1] op_sel_hi:[1,1]\n"
  "v_pk_mul_f32 v[88:89], v[88:89], s[56:57] op_sel:[0,1] op_sel_hi:[1,1]\n"
  "v_pk_mul_f32 v[90:91], v[90:91], s[56:57] op_sel:[0,1] op_sel_hi:[1,1]\n"
  "v_pk_fma_f32 v[92:93], v[72:73], v[72:73], s[46:47] op_sel_hi:[1,1,0] neg_lo:[1,0,0] neg_hi:[1,0,0]\n"
  "v_pk_fma_f32 v[94:95], v[74:75], v[74:75], s[46:47] op_sel_hi:[1,1,0] neg_lo:[1,0,0] neg_hi:[1,0,0]\n"
  "v_pk_fma_f32 v[96:97], v[76:77], v[76:77], s[46:47] op_sel_hi:[1,1,0] neg_lo:[1,0,0] neg_hi:[1,0,0]\n"
  "v_pk_fma_f32 v[98:99], v[78:79], v[78:79], s[46:47] op_sel_hi:[1,1,0] neg_lo:[1,0,0] neg_hi:[1,0,0]\n"
  "v_pk_fma_f32 v[100:101], v[80:81], v[80:81], s[46:47] op_sel_hi:[1,1,0] neg_lo:[1,0,0] neg_hi:[1,0,0]\n"
  "v_pk_fma_f32 v[102:103], v[82:83], v[82:83], s[46:47] op_sel_hi:[1,1,0] neg_lo:[1,0,0] neg_hi:[1,0,0]\n"
  "v_pk_fma_f32 v[104:105], v[84:85], v[84:85], s[46:47] op_sel_hi:[1,1,0] neg_lo:[1,0,0] neg_hi:[1,0,0]\n"
  "v_pk_fma_f32 v[106:107], v[86:87], v[86:87], s[46:47] op_sel_hi:[1,1,0] neg_lo:[1,0,0] neg_hi:[1,0,0]\n"
  "v_pk_fma_f32 v[108:109], v[88:89], v[88:89], s[46:47] op_sel_hi:[1,1,0] neg_lo:[1,0,0] neg_hi:[1,0,0]\n"
  "v_pk_fma_f32 v[110:111], v[90:91], v[90:91], s[46:47] op_sel_hi:[1,1,0] neg_lo:[1,0,0] neg_hi:[1,0,0]\n"
  "v_pk_mul_f32 v[92:93], v[72:73], v[92:93]\n"
  "v_pk_mul_f32 v[94:95], v[74:75], v[94:95]\n"
  "v_pk_mul_f32 v[96:97], v[76:77], v[96:97]\n"
  "v_pk_mul_f32 v[98:99], v[78:79], v[98:99]\n"
  "v_pk_mul_f32 v[100:101], v[80:81], v[100:101]\n"
  "v_pk_mul_f32 v[102:103], v[82:83], v[102:103]\n"
  "v_pk_mul_f32 v[104:105], v[84:85], v[104:105]\n"
  "v_pk_mul_f32 v[106:107], v[86:87], v[106:107]\n"
  "v_pk_mul_f32 v[108:109], v[88:89], v[108:109]\n"
  "v_pk_mul_f32 v[110:111], v[90:91], v[110:111]\n"
  "v_exp_f32 v112, v92\n"
  "v_exp_f32 v113, v93\n"
  "v_exp_f32 v114, v94\n"
  "v_exp_f32 v115, v95\n"
  "v_exp_f32 v116, v96\n"
  "v_exp_f32 v117, v97\n"
  "v_exp_f32 v118, v98\n"
  "v_exp_f32 v119, v99\n"
  "v_exp_f32 v120, v100\n"
  "s_waitcnt lgkmcnt(0)\n"
  "s_mov_b32 s36, s14\n"
  "s_and_b32 s37, s15, 0xffff\n"
  "s_mov_b32 s38, 73728\n"
  "s_mov_b32 s39, 0x20000\n"
  "s_mov_b32 s40, s16\n"
  "s_and_b32 s41, s17, 0xffff\n"
  "s_mov_b32 s42, 73728\n"
  "s_mov_b32 s43, 0x20000\n"
  "s_barrier\n"
  "v_exp_f32 v121, v101\n"
  "v_exp_f32 v122, v102\n"
  "v_exp_f32 v123, v103\n"
  "v_exp_f32 v124, v104\n"
  "v_exp_f32 v125, v105\n"
  "v_exp_f32 v126, v106\n"
  "v_exp_f32 v127, v107\n"
  "v_exp_f32 v128, v108\n"
  "v_exp_f32 v129, v109\n"
  "v_exp_f32 v130, v110\n"
  "v_exp_f32 v131, v111\n"
  "v_pk_add_f32 v[92:93], v[112:113], 1.0 op_sel_hi:[1,0]\n"
  "v_pk_add_f32 v[94:95], v[114:115], 1.0 op_sel_hi:[1,0]\n"
  "v_pk_add_f32 v[96:97], v[116:117], 1.0 op_sel_hi:[1,0]\n"
  "v_pk_add_f32 v[98:99], v[118:119], 1.0 op_sel_hi:[1,0]\n"
  "v_pk_add_f32 v[100:101], v[120:121], 1.0 op_sel_hi:[1,0]\n"
  "v_pk_add_f32 v[102:103], v[122:123], 1.0 op_sel_hi:[1,0]\n"
  "v_pk_add_f32 v[104:105], v[124:125], 1.0 op_sel_hi:[1,0]\n"
  "v_pk_add_f32 v[106:107], v[126:127], 1.0 op_sel_hi:[1,0]\n"
  "v_pk_add_f32 v[108:109], v[128:129], 1.0 op_sel_hi:[1,0]\n"
  "v_pk_add_f32 v[110:111], v[130:131], 1.0 op_sel_hi:[1,0]\n"
  "v_rcp_f32 v92, v92\n"
  "v_rcp_f32 v93, v93\n"
  "v_rcp_f32 v94, v94\n"
  "v_rcp_f32 v95, v95\n"
  "v_rcp_f32 v96, v96\n"
  "v_rcp_f32 v97, v97\n"
  "v_rcp_f32 v98, v98\n"
  "v_rcp_f32 v99, v99\n"
  "v_rcp_f32 v100, v100\n"
  "v_rcp_f32 v101, v101\n"
  "v_rcp_f32 v102, v102\n"
  "v_rcp_f32 v103, v103\n"
  "v_rcp_f32 v104, v104\n"
  "v_rcp_f32 v105, v105\n"
  "v_rcp_f32 v106, v106\n"
  "v_rcp_f32 v107, v107\n"
  "v_rcp_f32 v108, v108\n"
  "v_rcp_f32 v109, v109\n"
  "v_rcp_f32 v110, v110\n"
  "s_add_u32 m0, s52, 0\n"
  "s_add_u32 s65, s50, 0\n"
  "buffer_load_dword v190, off, s[36:39], 0\n"
  "buffer_load_dword v190, off, s[36:39], 0\n"
  "buffer_load_dwordx4 v176, s[36:39], s65 offen lds\n"
  "v_rcp_f32 v111, v111\n"
  "v_pk_mul_f32 v[72:73], v[72:73], v[92:93]\n"
  "v_pk_mul_f32 v[74:75], v[74:75], v[94:95]\n"
  "v_pk_mul_f32 v[76:77], v[76:77], v[96:97]\n"
  "v_pk_mul_f32 v[78:79], v[78:79], v[98:99]\n"
  "v_pk_mul_f32 v[80:81], v[80:81], v[100:101]\n"
  "v_pk_mul_f32 v[82:83], v[82:83], v[102:103]\n"
  "v_pk_mul_f32 v[84:85], v[84:85], v[104:105]\n"
  "v_pk_mul_f32 v[86:87], v[86:87], v[106:107]\n"
  "v_pk_mul_f32 v[88:89], v[88:89], v[108:109]\n"
  "v_pk_mul_f32 v[90:91], v[90:91], v[110:111]\n"
  "v_pk_mul_f32 v[112:113], v[72:73], v[112:113] neg_lo:[1,0] neg_hi:[1,0]\n"
  "v_pk_mul_f32 v[114:115], v[74:75], v[114:115] neg_lo:[1,0] neg_hi:[1,0]\n"
  "v_pk_mul_f32 v[116:117], v[76:77], v[116:117] neg_lo:[1,0] neg_hi:[1,0]\n"
  "v_pk_mul_f32 v[118:119], v[78:79], v[118:119] neg_lo:[1,0] neg_hi:[1,0]\n"
  "v_pk_mul_f32 v[120:121], v[80:81], v[120:121] neg_lo:[1,0] neg_hi:[1,0]\n"
  "v_pk_mul_f32 v[122:123], v[82:83], v[122:123] neg_lo:[1,0] neg_hi:[1,0]\n"
  "v_pk_mul_f32 v[124:125], v[84:85], v[124:125] neg_lo:[1,0] neg_hi:[1,0]\n"
  "v_pk_mul_f32 v[126:127], v[86:87], v[126:127] neg_lo:[1,0] neg_hi:[1,0]\n"
  "v_pk_mul_f32 v[128:129], v[88:89], v[128:129] neg_lo:[1,0] neg_hi:[1,0]\n"
  "v_pk_mul_f32 v[130:131], v[90:91], v[130:131] neg_lo:[1,0] neg_hi:[1,0]\n"
  "v_cvt_pk_bf16_f32 v72, v72, v73\n"
  "v_cvt_pk_bf16_f32 v73, v74, v75\n"
  "v_cvt_pk_bf16_f32 v74, v76, v77\n"
  "v_cvt_pk_bf16_f32 v75, v78, v79\n"
  "v_cvt_pk_bf16_f32 v76, v80, v81\n"
  "s_add_u32 s65, s50, 11264\n"
  "buffer_load_dword v190, off, s[36:39], 0\n"
  "buffer_load_dword v190, off, s[36:39], 0\n"
  "buffer_load_dwordx4 v176, s[36:39], s65 offen offset:1024 lds\n"
  "v_cvt_pk_bf16_f32 v77, v82, v83\n"
  "v_cvt_pk_bf16_f32 v78, v84, v85\n"
  "v_cvt_pk_bf16_f32 v79, v86, v87\n"
  "v_cvt_pk_bf16_f32 v80, v88, v89\n"
  "v_cvt_pk_bf16_f32 v81, v90, v91\n"
  "v_cvt_pk_bf16_f32 v112, v112, v113\n"
  "v_cvt_pk_bf16_f32 v113, v114, v115\n"
  "v_cvt_pk_bf16_f32 v114, v116, v117\n"
  "v_cvt_pk_bf16_f32 v115, v118, v119\n"
  "v_cvt_pk_bf16_f32 v116, v120, v121\n"
  "v_cvt_pk_bf16_f32 v117, v122, v123\n"
  "v_cvt_pk_bf16_f32 v118, v124, v125\n"
  "v_cvt_pk_bf16_f32 v119, v126, v127\n"
  "v_cvt_pk_bf16_f32 v120, v128, v129\n"
  "v_cvt_pk_bf16_f32 v121, v130, v131\n"
  "ds_write_b64 v150, v[72:73]\n"
  "ds_write_b64 v152, v[112:113]\n"
  "ds_write_b64 v150, v[74:75] offset:2048\n"
  "ds_write_b64 v152, v[114:115] offset:2048\n"
  "ds_write_b64 v150, v[76:77] offset:4096\n"
  "ds_write_b64 v152, v[116:117] offset:4096\n"
  "ds_write_b64 v150, v[78:79] offset:6144\n"
  "ds_write_b64 v152, v[118:119] offset:6144\n"
  "ds_write_b64 v150, v[80:81] offset:8192\n"
  "ds_write_b64 v152, v[120:121] offset:8192\n"
  "s_add_u32 s65, s50, 22528\n"
  "buffer_load_dword v190, off, s[36:39], 0\n"
  "buffer_load_dwordx4 v176, s[36:39], s65 offen offset:2048 lds\n"
  "s_add_u32 m0, s52, 3072\n"
  "s_add_u32 s65, s50, 36864\n"
  "buffer_load_dword v190, off, s[36:39], 0\n"
  "buffer_load_dwordx4 v176, s[36:39], s65 offen lds\n"
  "s_barrier\n"
  "s_add_u32 s65, s50, 48128\n"
  "buffer_load_dword v190, off, s[36:39], 0\n"
  "buffer_load_dwordx4 v176, s[36:39], s65 offen offset:1024 lds\n"
  "s_add_u32 s65, s50, 59392\n"
  "buffer_load_dword v190, off, s[36:39], 0\n"
  "buffer_load_dword v190, off, s[36:39], 0\n"
  "buffer_load_dwordx4 v176, s[36:39], s65 offen offset:2048 lds\n"
  "s_add_u32 m0, s51, 0\n"
  "s_add_u32 s65, s50, 256\n"
  "buffer_load_dword v190, off, s[36:39], 0\n"
  "buffer_load_dwordx4 v176, s[36:39], s65 offen lds\n"
  "s_add_u32 s65, s50, 11520\n"
  "buffer_load_dword v190, off, s[36:39], 0\n"
  "buffer_load_dword v190, off, s[36:39], 0\n"
  "buffer_load_dword v190, off, s[36:39], 0\n"
  "buffer_load_dword v190, off, s[36:39], 0\n"
  "buffer_load_dwordx4 v176, s[36:39], s65 offen offset:1024 lds\n"
  "s_add_u32 s65, s50, 22784\n"
  "buffer_load_dword v190, off, s[36:39], 0\n"
  "buffer_load_dword v190, off, s[36:39], 0\n"
  "buffer_load_dword v190, off, s[36:39], 0\n"
  "buffer_load_dword v190, off, s[36:39], 0\n"
  "buffer_load_dwordx4 v176, s[36:39], s65 offen offset:2048 lds\n"
  "s_add_u32 m0, s51, 3072\n"
  "s_add_u32 s65, s50, 37120\n"
  "buffer_load_dword v190, off, s[36:39], 0\n"
  "buffer_load_dword v190, off, s[36:39], 0\n"
  "buffer_load_dwordx4 v176, s[36:39], s65 offen lds\n"
  "s_add_u32 s65, s50, 48384\n"
  "buffer_load_dword v190, off, s[36:39], 0\n"
  "buffer_load_dword v190, off, s[36:39], 0\n"
  "buffer_load_dwordx4 v176, s[36:39], s65 offen offset:1024 lds\n"
  "s_add_u32 s65, s50, 59648\n"
  "buffer_load_dword v190, off, s[36:39], 0\n"
  "buffer_load_dwordx4 v176, s[36:39], s65 offen offset:2048 lds\n"
  "s_add_u32 m0, s51, 24576\n"
  "s_add_u32 s65, s53, 0\n"
  "buffer_load_dwordx4 v172, s[40:43], s65 offen lds\n"
  "buffer_load_dwordx4 v173, s[40:43], s65 offen offset:1024 lds\n"
  "buffer_load_dwordx4 v174, s[40:43], s65 offen offset:2048 lds\n"
  "s_add_u32 m0, s51, 27648\n"
  "s_add_u32 s65, s53, 3072\n"
  "buffer_load_dwordx4 v172, s[40:43], s65 offen lds\n"
  "buffer_load_dwordx4 v173, s[40:43], s65 offen offset:1024 lds\n"
  "buffer_load_dwordx4 v174, s[40:43], s65 offen offset:2048 lds\n"
  "s_waitcnt vmcnt(26)\n"
  "ds_read_b64_tr_b16 v[0:1], v180\n"
  "ds_read_b64_tr_b16 v[2:3], v180 offset:256\n"
  "ds_read_b64_tr_b16 v[12:13], v181\n"
  "ds_read_b64_tr_b16 v[14:15], v181 offset:256\n"
  "ds_read_b64_tr_b16 v[4:5], v180 offset:2048\n"
  "ds_read_b64_tr_b16 v[6:7], v180 offset:2304\n"
  "ds_read_b64_tr_b16 v[16:17], v181 offset:2048\n"
  "ds_read_b64_tr_b16 v[18:19], v181 offset:2304\n"
  "ds_read_b64_tr_b16 v[8:9], v180 offset:4096\n"
  "ds_read_b64_tr_b16 v[10:11], v180 offset:4352\n"
  "ds_read_b64_tr_b16 v[20:21], v181 offset:4096\n"
  "ds_read_b64_tr_b16 v[22:23], v181 offset:4352\n"
  "s_waitcnt lgkmcnt(0)\n"
  "v_or3_b32 v188, v0, v1, v188\n"
  "v_or3_b32 v189, v2, v3, v189\n"
  "v_or3_b32 v188, v4, v5, v188\n"
  "v_or3_b32 v189, v6, v7, v189\n"
  "v_or3_b32 v188, v8, v9, v188\n"
  "v_or3_b32 v189, v10, v11, v189\n"
  "v_or3_b32 v188, v12, v13, v188\n"
  "v_or3_b32 v189, v14, v15, v189\n"
  "v_or3_b32 v188, v16, v17, v188\n"
  "v_or3_b32 v189, v18, v19, v189\n"
  "v_or3_b32 v188, v20, v21, v188\n"
  "v_or3_b32 v189, v22, v23, v189\n"
  "s_setprio 2\n"
  "s_barrier\n"
  "ds_read_b128 v[224:227], v248\n"
  "ds_read_b128 v[228:231], v248 offset:1024\n"
  "ds_read_b128 v[232:235], v248 offset:2048\n"
  "ds_read_b128 v[236:239], v248 offset:3072\n"
  "ds_read_b128 v[240:243], v248 offset:4096\n"
  "ds_read_b128 v[244:247], v248 offset:5120\n"
  "s_add_u32 m0, s52, 0\n"
  "s_add_u32 s65, s50, 512\n"
  "buffer_load_dwordx4 v176, s[36:39], s65 offen lds\n"
  "s_add_u32 s65, s50, 11776\n"
  "buffer_load_dwordx4 v176, s[36:39], s65 offen offset:1024 lds\n"
  "s_waitcnt lgkmcnt(5)\n"
  "v_mfma_f32_16x16x32_bf16 v[192:195], v[0:3], v[224:227], 0\n"
  "v_mfma_f32_16x16x32_bf16 v[196:199], v[12:15], v[224:227], 0\n"
  "s_waitcnt lgkmcnt(4)\n"
  "v_mfma_f32_16x16x32_bf16 v[192:195], v[4:7], v[228:231], v[192:195]\n"
  "v_mfma_f32_16x16x32_bf16 v[196:199], v[16:19], v[228:231], v[196:199]\n"
  "s_waitcnt lgkmcnt(3)\n"
  "v_mfma_f32_16x16x32_bf16 v[192:195], v[8:11], v[232:235], v[192:195]\n"
  "v_mfma_f32_16x16x32_bf16 v[196:199], v[20:23], v[232:235], v[196:199]\n"
  "s_waitcnt lgkmcnt(2)\n"
  "v_mfma_f32_16x16x32_bf16 v[200:203], v[0:3], v[236:239], 0\n"
  "s_add_u32 s65, s50, 23040\n"
  "buffer_load_dwordx4 v176, s[36:39], s65 offen offset:2048 lds\n"
  "s_nop 2\n"
  "v_lshlrev_b32_sdwa v192, 1, v192 dst_sel:DWORD dst_unused:UNUSED_PAD src0_sel:DWORD src1_sel:WORD_1\n"
  "v_mfma_f32_16x16x32_bf16 v[204:207], v[12:15], v[236:239], 0\n"
  "s_add_u32 m0, s52, 3072\n"
  "s_add_u32 s65, s50, 37376\n"
  "buffer_load_dwordx4 v176, s[36:39], s65 offen lds\n"
  "ds_read_u16 v192, v192\n"
  "v_lshlrev_b32_sdwa v193, 1, v193 dst_sel:DWORD dst_unused:UNUSED_PAD src0_sel:DWORD src1_sel:WORD_1\n"
  "s_waitcnt lgkmcnt(2)\n"
  "v_mfma_f32_16x16x32_bf16 v[200:203], v[4:7], v[240:243], v[200:203]\n"
  "s_add_u32 s65, s50, 48640\n"
  "buffer_load_dword v190, off, s[36:39], 0\n"
  "buffer_load_dword v190, off, s[36:39], 0\n"
  "buffer_load_dwordx4 v176, s[36:39], s65 offen offset:1024 lds\n"
  "ds_read_u16 v193, v193\n"
  "v_lshlrev_b32_sdwa v194, 1, v194 dst_sel:DWORD dst_unused:UNUSED_PAD src0_sel:DWORD src1_sel:WORD_1\n"
  "v_mfma_f32_16x16x32_bf16 v[204:207], v[16:19], v[240:243], v[204:207]\n"
  "s_waitcnt vmcnt(13)\n"
  "ds_read_b64_tr_b16 v[24:25], v178\n"
  "ds_read_b64_tr_b16 v[26:27], v178 offset:256\n"
  "ds_read_b64_tr_b16 v[36:37], v179\n"
  "ds_read_b64_tr_b16 v[38:39], v179 offset:256\n"
  "ds_read_b64_tr_b16 v[28:29], v178 offset:2048\n"
  "ds_read_b64_tr_b16 v[30:31], v178 offset:2304\n"
  "s_add_u32 s65, s50, 59904\n"
  "buffer_load_dword v190, off, s[36:39], 0\n"
  "buffer_load_dword v190, off, s[36:39], 0\n"
  "buffer_load_dwordx4 v176, s[36:39], s65 offen offset:2048 lds\n"
  "ds_read_u16 v194, v194\n"
  "v_lshlrev_b32_sdwa v195, 1, v195 dst_sel:DWORD dst_unused:UNUSED_PAD src0_sel:DWORD src1_sel:WORD_1\n"
  "s_waitcnt lgkmcnt(9)\n"
  "v_mfma_f32_16x16x32_bf16 v[200:203], v[8:11], v[244:247], v[200:203]\n"
  "ds_read_b64_tr_b16 v[40:41], v179 offset:2048\n"
  "ds_read_b64_tr_b16 v[42:43], v179 offset:2304\n"
  "ds_read_b64_tr_b16 v[32:33], v178 offset:4096\n"
  "ds_read_b64_tr_b16 v[34:35], v178 offset:4352\n"
  "ds_read_b64_tr_b16 v[44:45], v179 offset:4096\n"
  "ds_read_b64_tr_b16 v[46:47], v179 offset:4352\n"
  "ds_read_u16 v195, v195\n"
  "v_lshlrev_b32_sdwa v196, 1, v196 dst_sel:DWORD dst_unused:UNUSED_PAD src0_sel:DWORD src1_sel:WORD_1\n"
  "v_lshlrev_b32_sdwa v197, 1, v197 dst_sel:DWORD dst_unused:UNUSED_PAD src0_sel:DWORD src1_sel:WORD_1\n"
  "v_lshlrev_b32_sdwa v198, 1, v198 dst_sel:DWORD dst_unused:UNUSED_PAD src0_sel:DWORD src1_sel:WORD_1\n"
  "v_mfma_f32_16x16x32_bf16 v[204:207], v[20:23], v[244:247], v[204:207]\n"
  "ds_read_u16 v196, v196\n"
  "ds_read_u16 v197, v197\n"
  "v_lshlrev_b32_sdwa v199, 1, v199 dst_sel:DWORD dst_unused:UNUSED_PAD src0_sel:DWORD src1_sel:WORD_1\n"
  "s_waitcnt lgkmcnt(3)\n"
  "v_or3_b32 v188, v24, v25, v188\n"
  "v_or3_b32 v189, v26, v27, v189\n"
  "v_or3_b32 v188, v28, v29, v188\n"
  "v_or3_b32 v189, v30, v31, v189\n"
  "v_or3_b32 v188, v32, v33, v188\n"
  "v_or3_b32 v189, v34, v35, v189\n"
  "v_or3_b32 v188, v36, v37, v188\n"
  "v_or3_b32 v189, v38, v39, v189\n"
  "v_or3_b32 v188, v40, v41, v188\n"
  "v_or3_b32 v189, v42, v43, v189\n"
  "v_or3_b32 v188, v44, v45, v188\n"
  "v_or3_b32 v189, v46, v47, v189\n"
  "v_mfma_f32_16x16x32_bf16 v[208:211], v[24:27], v[224:227], 0\n"
  "s_add_u32 m0, s51, 0\n"
  "s_add_u32 s65, s53, 24576\n"
  "buffer_load_dwordx4 v172, s[40:43], s65 offen lds\n"
  "ds_read_u16 v198, v198\n"
  "ds_read_u16 v199, v199\n"
  "v_lshlrev_b32_sdwa v200, 1, v200 dst_sel:DWORD dst_unused:UNUSED_PAD src0_sel:DWORD src1_sel:WORD_1\n"
  "v_lshlrev_b32_sdwa v201, 1, v201 dst_sel:DWORD dst_unused:UNUSED_PAD src0_sel:DWORD src1_sel:WORD_1\n"
  "v_mfma_f32_16x16x32_bf16 v[212:215], v[36:39], v[224:227], 0\n"
  "buffer_load_dwordx4 v173, s[40:43], s65 offen offset:1024 lds\n"
  "ds_read_u16 v200, v200\n"
  "ds_read_u16 v201, v201\n"
  "v_lshlrev_b32_sdwa v202, 1, v202 dst_sel:DWORD dst_unused:UNUSED_PAD src0_sel:DWORD src1_sel:WORD_1\n"
  "v_mfma_f32_16x16x32_bf16 v[208:211], v[28:31], v[228:231], v[208:211]\n"
  "buffer_load_dwordx4 v174, s[40:43], s65 offen offset:2048 lds\n"
  "ds_read_u16 v202, v202\n"
  "v_lshlrev_b32_sdwa v203, 1, v203 dst_sel:DWORD dst_unused:UNUSED_PAD src0_sel:DWORD src1_sel:WORD_1\n"
  "v_lshlrev_b32_sdwa v204, 1, v204 dst_sel:DWORD dst_unused:UNUSED_PAD src0_sel:DWORD src1_sel:WORD_1\n"
  "v_mfma_f32_16x16x32_bf16 v[212:215], v[40:43], v[228:231], v[212:215]\n"
  "s_add_u32 m0, s51, 3072\n"
  "s_add_u32 s65, s53, 27648\n"
  "buffer_load_dwordx4 v172, s[40:43], s65 offen lds\n"
  "ds_read_u16 v203, v203\n"
  "ds_read_u16 v204, v204\n"
  "v_lshlrev_b32_sdwa v205, 1, v205 dst_sel:DWORD dst_unused:UNUSED_PAD src0_sel:DWORD src1_sel:WORD_1\n"
  "v_mfma_f32_16x16x32_bf16 v[208:211], v[32:35], v[232:235], v[208:211]\n"
  "s_waitcnt vmcnt(14)\n"
  "ds_read_b64_tr_b16 v[72:73], v250 offset:24576\n"
  "ds_read_b64_tr_b16 v[74:75], v250 offset:27648\n"
  "ds_read_b64_tr_b16 v[76:77], v254 offset:24576\n"
  "ds_read_b64_tr_b16 v[78:79], v254 offset:27648\n"
  "ds_read_b64_tr_b16 v[80:81], v250 offset:24640\n"
  "ds_read_b64_tr_b16 v[82:83], v250 offset:27712\n"
  "buffer_load_dwordx4 v173, s[40:43], s65 offen offset:1024 lds\n"
  "ds_read_u16 v205, v205\n"
  "v_lshlrev_b32_sdwa v206, 1, v206 dst_sel:DWORD dst_unused:UNUSED_PAD src0_sel:DWORD src1_sel:WORD_1\n"
  "v_lshlrev_b32_sdwa v207, 1, v207 dst_sel:DWORD dst_unused:UNUSED_PAD src0_sel:DWORD src1_sel:WORD_1\n"
  "v_mfma_f32_16x16x32_bf16 v[212:215], v[44:47], v[232:235], v[212:215]\n"
  "ds_read_b64_tr_b16 v[84:85], v254 offset:24640\n"
  "ds_read_b64_tr_b16 v[86:87], v254 offset:27712\n"
  "ds_read_b64_tr_b16 v[88:89], v250 offset:24704\n"
  "ds_read_b64_tr_b16 v[90:91], v250 offset:27776\n"
  "ds_read_b64_tr_b16 v[92:93], v254 offset:24704\n"
  "ds_read_b64_tr_b16 v[94:95], v254 offset:27776\n"
  "buffer_load_dwordx4 v174, s[40:43], s65 offen offset:2048 lds\n"
  "s_waitcnt lgkmcnt(14)\n"
  "ds_read_u16 v206, v206\n"
  "v_cvt_pk_u16_u32 v192, v192, v193\n"
  "v_cvt_pk_u16_u32 v193, v194, v195\n"
  "v_cvt_pk_u16_u32 v194, v196, v197\n"
  "v_cvt_pk_u16_u32 v195, v198, v199\n"
  "v_mfma_f32_16x16x32_bf16 v[216:219], v[24:27], v[236:239], 0\n"
  "ds_read_u16 v207, v207\n"
  "v_lshlrev_b32_sdwa v208, 1, v208 dst_sel:DWORD dst_unused:UNUSED_PAD src0_sel:DWORD src1_sel:WORD_1\n"
  "v_mfma_f32_16x16x32_bf16 v[220:223], v[36:39], v[236:239], 0\n"
  "ds_read_u16 v208, v208\n"
  "v_lshlrev_b32_sdwa v209, 1, v209 dst_sel:DWORD dst_unused:UNUSED_PAD src0_sel:DWORD src1_sel:WORD_1\n"
  "v_mfma_f32_16x16x32_bf16 v[216:219], v[28:31], v[240:243], v[216:219]\n"
  "ds_read_u16 v209, v209\n"
  "v_lshlrev_b32_sdwa v210, 1, v210 dst_sel:DWORD dst_unused:UNUSED_PAD src0_sel:DWORD src1_sel:WORD_1\n"
  "v_mfma_f32_16x16x32_bf16 v[220:223], v[40:43], v[240:243], v[220:223]\n"
  "ds_read_u16 v210, v210\n"
  "v_lshlrev_b32_sdwa v211, 1, v211 dst_sel:DWORD dst_unused:UNUSED_PAD src0_sel:DWORD src1_sel:WORD_1\n"
  "v_mfma_f32_16x16x32_bf16 v[216:219], v[32:35], v[244:247], v[216:219]\n"
  "s_add_u32 m0, s51, 24576\n"
  "s_add_u32 s65, s53, 49152\n"
  "s_waitcnt lgkmcnt(5)\n"
  "buffer_load_dwordx4 v172, s[40:43], s65 offen lds\n"
  "ds_read_u16 v211, v211\n"
  "v_lshlrev_b32_sdwa v212, 1, v212 dst_sel:DWORD dst_unused:UNUSED_PAD src0_sel:DWORD src1_sel:WORD_1\n"
  "v_mfma_f32_16x16x32_bf16 v[220:223], v[44:47], v[244:247], v[220:223]\n"
  "buffer_load_dwordx4 v173, s[40:43], s65 offen offset:1024 lds\n"
  "ds_read_u16 v212, v212\n"
  "v_lshlrev_b32_sdwa v213, 1, v213 dst_sel:DWORD dst_unused:UNUSED_PAD src0_sel:DWORD src1_sel:WORD_1\n"
  "v_mfma_f32_16x16x32_bf16 v[144:147], v[72:75], v[192:195], 0\n"
  "buffer_load_dwordx4 v174, s[40:43], s65 offen offset:2048 lds\n"
  "ds_read_u16 v213, v213\n"
  "v_lshlrev_b32_sdwa v214, 1, v214 dst_sel:DWORD dst_unused:UNUSED_PAD src0_sel:DWORD src1_sel:WORD_1\n"
  "v_mfma_f32_16x16x32_bf16 v[148:151], v[76:79], v[192:195], 0\n"
  "s_add_u32 m0, s51, 27648\n"
  "s_add_u32 s65, s53, 52224\n"
  "buffer_load_dwordx4 v172, s[40:43], s65 offen lds\n"
  "ds_read_u16 v214, v214\n"
  "v_lshlrev_b32_sdwa v215, 1, v215 dst_sel:DWORD dst_unused:UNUSED_PAD src0_sel:DWORD src1_sel:WORD_1\n"
  "v_mfma_f32_16x16x32_bf16 v[152:155], v[80:83], v[192:195], 0\n"
  "s_waitcnt vmcnt(10)\n"
  "ds_read_b64_tr_b16 v[48:49], v180\n"
  "ds_read_b64_tr_b16 v[50:51], v180 offset:256\n"
  "ds_read_b64_tr_b16 v[60:61], v181\n"
  "ds_read_b64_tr_b16 v[62:63], v181 offset:256\n"
  "ds_read_b64_tr_b16 v[52:53], v180 offset:2048\n"
  "ds_read_b64_tr_b16 v[54:55], v180 offset:2304\n"
  "buffer_load_dwordx4 v173, s[40:43], s65 offen offset:1024 lds\n"
  "ds_read_u16 v215, v215\n"
  "v_mfma_f32_16x16x32_bf16 v[156:159], v[84:87], v[192:195], 0\n"
  "ds_read_b64_tr_b16 v[64:65], v181 offset:2048\n"
  "ds_read_b64_tr_b16 v[66:67], v181 offset:2304\n"
  "ds_read_b64_tr_b16 v[56:57], v180 offset:4096\n"
  "ds_read_b64_tr_b16 v[58:59], v180 offset:4352\n"
  "ds_read_b64_tr_b16 v[68:69], v181 offset:4096\n"
  "ds_read_b64_tr_b16 v[70:71], v181 offset:4352\n"
  "buffer_load_dwordx4 v174, s[40:43], s65 offen offset:2048 lds\n"
  "v_mfma_f32_16x16x32_bf16 v[160:163], v[88:91], v[192:195], 0\n"
  "s_waitcnt lgkmcnt(14)\n"
  "v_cvt_pk_u16_u32 v200, v200, v201\n"
  "v_cvt_pk_u16_u32 v201, v202, v203\n"
  "v_cvt_pk_u16_u32 v202, v204, v205\n"
  "v_cvt_pk_u16_u32 v203, v206, v207\n"
  "v_mfma_f32_16x16x32_bf16 v[164:167], v[92:95], v[192:195], 0\n"
  "s_waitcnt lgkmcnt(0)\n"
  "v_or3_b32 v188, v48, v49, v188\n"
  "v_or3_b32 v189, v50, v51, v189\n"
  "v_add_u32 v48, v48, v185\n"
  "v_add_u32 v49, v49, v185\n"
  "v_add_u32 v50, v50, v185\n"
  "v_add_u32 v51, v51, v185\n"
  "v_or3_b32 v188, v52, v53, v188\n"
  "v_or3_b32 v189, v54, v55, v189\n"
  "v_add_u32 v52, v52, v185\n"
  "v_add_u32 v53, v53, v185\n"
  "v_add_u32 v54, v54, v185\n"
  "v_add_u32 v55, v55, v185\n"
  "v_or3_b32 v188, v56, v57, v188\n"
  "v_or3_b32 v189, v58, v59, v189\n"
  "v_add_u32 v56, v56, v185\n"
  "v_add_u32 v57, v57, v185\n"
  "v_add_u32 v58, v58, v185\n"
  "v_add_u32 v59, v59, v185\n"
  "v_or3_b32 v188, v60, v61, v188\n"
  "v_or3_b32 v189, v62, v63, v189\n"
  "v_or3_b32 v188, v64, v65, v188\n"
  "v_or3_b32 v189, v66, v67, v189\n"
  "v_or3_b32 v188, v68, v69, v188\n"
  "v_or3_b32 v189, v70, v71, v189\n"
  "v_or_b32 v188, v188, v189\n"
  "v_and_b32 v188, 0x40004000, v188\n"
  "v_cmp_ne_u32_e64 s[48:49], 0, v188\n"
  "s_mov_b32 m0, 0\n"
  "s_mov_b32 s22, 0\n"
  "s_mov_b32 s23, 0\n"
  "s_barrier\n"
  "s_cmp_lg_u64 s[48:49], 0\n"
  "s_cbranch_scc1 GF_start_%=\n"
  "G_go_%=:\n"
  "v_mfma_f32_16x16x32_bf16 v[192:195], v[48:51], v[224:227], 0\n"
  "s_setprio 2\n"
  "s_nop 2\n"
  "v_mfma_f32_16x16x32_bf16 v[196:199], v[60:63], v[224:227], 0\n"
  "v_lshlrev_b32_sdwa v216, 1, v216 dst_sel:DWORD dst_unused:UNUSED_PAD src0_sel:DWORD src1_sel:WORD_1\n"
  "v_lshlrev_b32_sdwa v217, 1, v217 dst_sel:DWORD dst_unused:UNUSED_PAD src0_sel:DWORD src1_sel:WORD_1\n"
  "v_mfma_f32_16x16x32_bf16 v[192:195], v[52:55], v[228:231], v[192:195]\n"
  "ds_read_u16 v216, v216\n"
  "ds_read_u16 v217, v217\n"
  "v_mfma_f32_16x16x32_bf16 v[196:199], v[64:67], v[228:231], v[196:199]\n"
  "v_lshlrev_b32_sdwa v218, 1, v218 dst_sel:DWORD dst_unused:UNUSED_PAD src0_sel:DWORD src1_sel:WORD_1\n"
  "v_lshlrev_b32_sdwa v219, 1, v219 dst_sel:DWORD dst_unused:UNUSED_PAD src0_sel:DWORD src1_sel:WORD_1\n"
  "v_mfma_f32_16x16x32_bf16 v[192:195], v[56:59], v[232:235], v[192:195]\n"
  "ds_read_u16 v218, v218\n"
  "ds_read_u16 v219, v219\n"
  "v_mfma_f32_16x16x32_bf16 v[196:199], v[68:71], v[232:235], v[196:199]\n"
  "v_mfma_f32_16x16x32_bf16 v[168:171], v[72:75], v[200:203], 0\n"
  "s_waitcnt vmcnt(6)\n"
  "ds_read_b64_tr_b16 v[96:97], v250\n"
  "ds_read_b64_tr_b16 v[98:99], v250 offset:3072\n"
  "ds_read_b64_tr_b16 v[100:101], v254\n"
  "ds_read_b64_tr_b16 v[102:103], v254 offset:3072\n"
  "v_lshlrev_b32_sdwa v220, 1, v220 dst_sel:DWORD dst_unused:UNUSED_PAD src0_sel:DWORD src1_sel:WORD_1\n"
  "v_lshlrev_b32_sdwa v221, 1, v221 dst_sel:DWORD dst_unused:UNUSED_PAD src0_sel:DWORD src1_sel:WORD_1\n"
  "v_mfma_f32_16x16x32_bf16 v[172:175], v[76:79], v[200:203], 0\n"
  "ds_read_b64_tr_b16 v[104:105], v250 offset:64\n"
  "ds_read_b64_tr_b16 v[106:107], v250 offset:3136\n"
  "ds_read_b64_tr_b16 v[108:109], v254 offset:64\n"
  "ds_read_b64_tr_b16 v[110:111], v254 offset:3136\n"
  "ds_read_b128 v[224:227], v248 offset:6144\n"
  "ds_read_u16 v220, v220\n"
  "v_mfma_f32_16x16x32_bf16 v[176:179], v[80:83], v[200:203], 0\n"
  "ds_read_b64_tr_b16 v[112:113], v250 offset:128\n"
  "ds_read_b64_tr_b16 v[114:115], v250 offset:3200\n"
  "ds_read_b64_tr_b16 v[116:117], v254 offset:128\n"
  "ds_read_b64_tr_b16 v[118:119], v254 offset:3200\n"
  "s_setprio 0\n"
  "ds_read_u16 v221, v221\n"
  "v_mfma_f32_16x16x32_bf16 v[180:183], v[84:87], v[200:203], 0\n"
  "v_lshlrev_b32_sdwa v222, 1, v222 dst_sel:DWORD dst_unused:UNUSED_PAD src0_sel:DWORD src1_sel:WORD_1\n"
  "v_lshlrev_b32_sdwa v223, 1, v223 dst_sel:DWORD dst_unused:UNUSED_PAD src0_sel:DWORD src1_sel:WORD_1\n"
  "v_mfma_f32_16x16x32_bf16 v[184:187], v[88:91], v[200:203], 0\n"
  "v_cvt_pk_u16_u32 v208, v208, v209\n"
  "v_cvt_pk_u16_u32 v209, v210, v211\n"
  "v_cvt_pk_u16_u32 v210, v212, v213\n"
  "v_cvt_pk_u16_u32 v211, v214, v215\n"
  "v_mfma_f32_16x16x32_bf16 v[188:191], v[92:95], v[200:203], 0\n"
  "ds_read_b128 v[228:231], v248 offset:7168\n"
  "v_mfma_f32_16x16x32_bf16 v[200:203], v[48:51], v[236:239], 0\n"
  "ds_read_b128 v[232:235], v248 offset:8192\n"
  "v_lshlrev_b32_sdwa v194, 1, v194 dst_sel:DWORD dst_unused:UNUSED_PAD src0_sel:DWORD src1_sel:WORD_1\n"
  "v_lshlrev_b32_sdwa v195, 1, v195 dst_sel:DWORD dst_unused:UNUSED_PAD src0_sel:DWORD src1_sel:WORD_1\n"
  "v_mfma_f32_16x16x32_bf16 v[204:207], v[60:63], v[236:239], 0\n"
  "v_fma_f32 v251, -v192, v192, s46\n"
  "ds_read_u16 v222, v222\n"
  "v_fma_f32 v252, -v193, v193, s46\n"
  "v_mfma_f32_16x16x32_bf16 v[200:203], v[52:55], v[240:243], v[200:203]\n"
  "ds_read_u16 v223, v223\n"
  "v_lshlrev_b32_sdwa v196, 1, v196 dst_sel:DWORD dst_unused:UNUSED_PAD src0_sel:DWORD src1_sel:WORD_1\n"
  "v_mul_f32 v251, v192, v251\n"
  "v_mfma_f32_16x16x32_bf16 v[204:207], v[64:67], v[240:243], v[204:207]\n"
  "s_setprio 0\n"
  "ds_read_u16 v194, v194\n"
  "v_mul_f32 v252, v193, v252\n"
  "v_mfma_f32_16x16x32_bf16 v[200:203], v[56:59], v[244:247], v[200:203]\n"
  "s_setprio 2\n"
  "ds_read_u16 v195, v195\n"
  "v_lshlrev_b32_sdwa v197, 1, v197 dst_sel:DWORD dst_unused:UNUSED_PAD src0_sel:DWORD src1_sel:WORD_1\n"
  "v_exp_f32 v251, v251\n"
  "v_mfma_f32_16x16x32_bf16 v[204:207], v[68:71], v[244:247], v[204:207]\n"
  "ds_read_u16 v196, v196\n"
  "v_exp_f32 v252, v252\n"
  "s_waitcnt lgkmcnt(14)\n"
  "v_mfma_f32_16x16x32_bf16 v[144:147], v[96:99], v[208:211], v[144:147]\n"
  "ds_read_u16 v197, v197\n"
  "v_lshlrev_b32_sdwa v198, 1, v198 dst_sel:DWORD dst_unused:UNUSED_PAD src0_sel:DWORD src1_sel:WORD_1\n"
  "v_add_f32 v251, 1.0, v251\n"
  "v_mfma_f32_16x16x32_bf16 v[148:151], v[100:103], v[208:211], v[148:151]\n"
  "ds_read_u16 v198, v198\n"
  "v_add_f32 v252, 1.0, v252\n"
  "v_lshlrev_b32_sdwa v199, 1, v199 dst_sel:DWORD dst_unused:UNUSED_PAD src0_sel:DWORD src1_sel:WORD_1\n"
  "v_mfma_f32_16x16x32_bf16 v[152:155], v[104:107], v[208:211], v[152:155]\n"
  "s_setprio 0\n"
  "ds_read_u16 v199, v199\n"
  "v_rcp_f32 v251, v251\n"
  "v_mfma_f32_16x16x32_bf16 v[156:159], v[108:111], v[208:211], v[156:159]\n"
  "s_waitcnt vmcnt(0)\n"
  "ds_read_b64_tr_b16 v[120:121], v250 offset:24576\n"
  "ds_read_b64_tr_b16 v[122:123], v250 offset:27648\n"
  "ds_read_b64_tr_b16 v[124:125], v254 offset:24576\n"
  "ds_read_b64_tr_b16 v[126:127], v254 offset:27648\n"
  "s_setprio 2\n"
  "v_rcp_f32 v252, v252\n"
  "s_waitcnt lgkmcnt(14)\n"
  "v_mfma_f32_16x16x32_bf16 v[160:163], v[112:115], v[208:211], v[160:163]\n"
  "ds_read_b64_tr_b16 v[128:129], v250 offset:24640\n"
  "ds_read_b64_tr_b16 v[130:131], v250 offset:27712\n"
  "ds_read_b64_tr_b16 v[132:133], v254 offset:24640\n"
  "ds_read_b64_tr_b16 v[134:135], v254 offset:27712\n"
  "s_waitcnt lgkmcnt(14)\n"
  "v_cvt_pk_u16_u32 v216, v216, v217\n"
  "v_cvt_pk_u16_u32 v217, v218, v219\n"
  "v_cvt_pk_u16_u32 v218, v220, v221\n"
  "v_cvt_pk_u16_u32 v219, v222, v223\n"
  "v_mfma_f32_16x16x32_bf16 v[164:167], v[116:119], v[208:211], v[164:167]\n"
  "ds_read_b64_tr_b16 v[136:137], v250 offset:24704\n"
  "ds_read_b64_tr_b16 v[138:139], v250 offset:27776\n"
  "ds_read_b64_tr_b16 v[140:141], v254 offset:24704\n"
  "ds_read_b64_tr_b16 v[142:143], v254 offset:27776\n"
  "v_mul_f32 v192, v192, v251\n"
  "v_mul_f32 v193, v193, v252\n"
  "v_cvt_pk_bf16_f32 v192, v192, v193\n"
  "v_mfma_f32_16x16x32_bf16 v[208:211], v[0:3], v[224:227], 0\n"
  "v_lshlrev_b32_sdwa v202, 1, v202 dst_sel:DWORD dst_unused:UNUSED_PAD src0_sel:DWORD src1_sel:WORD_1\n"
  "v_mfma_f32_16x16x32_bf16 v[212:215], v[12:15], v[224:227], 0\n"
  "v_lshlrev_b32_sdwa v203, 1, v203 dst_sel:DWORD dst_unused:UNUSED_PAD src0_sel:DWORD src1_sel:WORD_1\n"
  "v_fma_f32 v251, -v200, v200, s46\n"
  "v_fma_f32 v252, -v201, v201, s46\n"
  "v_mfma_f32_16x16x32_bf16 v[208:211], v[4:7], v[228:231], v[208:211]\n"
  "ds_read_u16 v202, v202\n"
  "ds_read_u16 v203, v203\n"
  "v_mfma_f32_16x16x32_bf16 v[212:215], v[16:19], v[228:231], v[212:215]\n"
  "s_setprio 0\n"
  "v_lshlrev_b32_sdwa v204, 1, v204 dst_sel:DWORD dst_unused:UNUSED_PAD src0_sel:DWORD src1_sel:WORD_1\n"
  "v_mul_f32 v251, v200, v251\n"
  "v_mfma_f32_16x16x32_bf16 v[208:211], v[8:11], v[232:235], v[208:211]\n"
  "s_setprio 2\n"
  "ds_read_u16 v204, v204\n"
  "v_mul_f32 v252, v201, v252\n"
  "v_lshlrev_b32_sdwa v205, 1, v205 dst_sel:DWORD dst_unused:UNUSED_PAD src0_sel:DWORD src1_sel:WORD_1\n"
  "v_mfma_f32_16x16x32_bf16 v[212:215], v[20:23], v[232:235], v[212:215]\n"
  "ds_read_u16 v205, v205\n"
  "v_mfma_f32_16x16x32_bf16 v[168:171], v[96:99], v[216:219], v[168:171]\n"
  "v_exp_f32 v251, v251\n"
  "v_exp_f32 v252, v252\n"
  "v_mfma_f32_16x16x32_bf16 v[172:175], v[100:103], v[216:219], v[172:175]\n"
  "v_lshlrev_b32_sdwa v206, 1, v206 dst_sel:DWORD dst_unused:UNUSED_PAD src0_sel:DWORD src1_sel:WORD_1\n"
  "v_add_f32 v251, 1.0, v251\n"
  "v_add_f32 v252, 1.0, v252\n"
  "v_lshlrev_b32_sdwa v207, 1, v207 dst_sel:DWORD dst_unused:UNUSED_PAD src0_sel:DWORD src1_sel:WORD_1\n"
  "v_mfma_f32_16x16x32_bf16 v[176:179], v[104:107], v[216:219], v[176:179]\n"
  "s_setprio 0\n"
  "ds_read_u16 v206, v206\n"
  "v_rcp_f32 v251, v251\n"
  "v_mfma_f32_16x16x32_bf16 v[180:183], v[108:111], v[216:219], v[180:183]\n"
  "s_setprio 2\n"
  "ds_read_u16 v207, v207\n"
  "v_rcp_f32 v252, v252\n"
  "v_mfma_f32_16x16x32_bf16 v[184:187], v[112:115], v[216:219], v[184:187]\n"
  "s_waitcnt lgkmcnt(14)\n"
  "v_cvt_pk_u16_u32 v193, v194, v195\n"
  "v_cvt_pk_u16_u32 v194, v196, v197\n"
  "v_cvt_pk_u16_u32 v195, v198, v199\n"
  "v_mfma_f32_16x16x32_bf16 v[188:191], v[116:119], v[216:219], v[188:191]\n"
  "v_mul_f32 v200, v200, v251\n"
  "v_mul_f32 v201, v201, v252\n"
  "v_cvt_pk_bf16_f32 v200, v200, v201\n"
  "v_mfma_f32_16x16x32_bf16 v[216:219], v[24:27], v[224:227], 0\n"
  "v_mfma_f32_16x16x32_bf16 v[220:223], v[36:39], v[224:227], 0\n"
  "v_lshlrev_b32_sdwa v208, 1, v208 dst_sel:DWORD dst_unused:UNUSED_PAD src0_sel:DWORD src1_sel:WORD_1\n"
  "v_lshlrev_b32_sdwa v209, 1, v209 dst_sel:DWORD dst_unused:UNUSED_PAD src0_sel:DWORD src1_sel:WORD_1\n"
  "v_mfma_f32_16x16x32_bf16 v[216:219], v[28:31], v[228:231], v[216:219]\n"
  "ds_append v254 offset:62464\n"
  "ds_read_u16 v208, v208\n"
  "ds_read_u16 v209, v209\n"
  "v_mfma_f32_16x16x32_bf16 v[220:223], v[40:43], v[228:231], v[220:223]\n"
  "s_setprio 0\n"
  "v_lshlrev_b32_sdwa v210, 1, v210 dst_sel:DWORD dst_unused:UNUSED_PAD src0_sel:DWORD src1_sel:WORD_1\n"
  "v_mfma_f32_16x16x32_bf16 v[216:219], v[32:35], v[232:235], v[216:219]\n"
  "s_setprio 2\n"
  "ds_read_u16 v210, v210\n"
  "v_mfma_f32_16x16x32_bf16 v[220:223], v[44:47], v[232:235], v[220:223]\n"
  "v_mfma_f32_16x16x32_bf16 v[144:147], v[120:123], v[192:195], v[144:147]\n"
  "v_lshlrev_b32_sdwa v211, 1, v211 dst_sel:DWORD dst_unused:UNUSED_PAD src0_sel:DWORD src1_sel:WORD_1\n"
  "v_lshlrev_b32_sdwa v212, 1, v212 dst_sel:DWORD dst_unused:UNUSED_PAD src0_sel:DWORD src1_sel:WORD_1\n"
  "v_mfma_f32_16x16x32_bf16 v[148:151], v[124:127], v[192:195], v[148:151]\n"
  "ds_read_u16 v211, v211\n"
  "ds_read_u16 v212, v212\n"
  "s_waitcnt lgkmcnt(14)\n"
  "v_mfma_f32_16x16x32_bf16 v[152:155], v[128:131], v[192:195], v[152:155]\n"
  "s_setprio 0\n"
  "ds_read_b128 v[236:239], v248 offset:9216\n"
  "v_mfma_f32_16x16x32_bf16 v[156:159], v[132:135], v[192:195], v[156:159]\n"
  "s_setprio 2\n"
  "v_lshlrev_b32_sdwa v213, 1, v213 dst_sel:DWORD dst_unused:UNUSED_PAD src0_sel:DWORD src1_sel:WORD_1\n"
  "v_lshlrev_b32_sdwa v214, 1, v214 dst_sel:DWORD dst_unused:UNUSED_PAD src0_sel:DWORD src1_sel:WORD_1\n"
  "v_mfma_f32_16x16x32_bf16 v[160:163], v[136:139], v[192:195], v[160:163]\n"
  "s_waitcnt lgkmcnt(7)\n"
  "v_cvt_pk_u16_u32 v201, v202, v203\n"
  "v_cvt_pk_u16_u32 v202, v204, v205\n"
  "v_cvt_pk_u16_u32 v203, v206, v207\n"
  "v_mfma_f32_16x16x32_bf16 v[164:167], v[140:143], v[192:195], v[164:167]\n"
  "ds_read_u16 v213, v213\n"
  "v_lshlrev_b32_sdwa v215, 1, v215 dst_sel:DWORD dst_unused:UNUSED_PAD src0_sel:DWORD src1_sel:WORD_1\n"
  "v_mfma_f32_16x16x32_bf16 v[192:195], v[48:51], v[224:227], 0\n"
  "ds_read_u16 v214, v214\n"
  "v_lshlrev_b32_sdwa v216, 1, v216 dst_sel:DWORD dst_unused:UNUSED_PAD src0_sel:DWORD src1_sel:WORD_1\n"
  "v_mfma_f32_16x16x32_bf16 v[196:199], v[60:63], v[224:227], 0\n"
  "ds_read_u16 v215, v215\n"
  "v_cvt_pk_bf16_f32 v144, v144, v145\n"
  "v_cvt_pk_bf16_f32 v145, v146, v147\n"
  "v_cvt_pk_bf16_f32 v146, v148, v149\n"
  "v_cvt_pk_bf16_f32 v147, v150, v151\n"
  "v_mfma_f32_16x16x32_bf16 v[192:195], v[52:55], v[228:231], v[192:195]\n"
  "ds_write_b128 v249, v[144:147]\n"
  "v_mfma_f32_16x16x32_bf16 v[196:199], v[64:67], v[228:231], v[196:199]\n"
  "s_setprio 0\n"
  "s_nop 1\n"
  "v_mfma_f32_16x16x32_bf16 v[192:195], v[56:59], v[232:235], v[192:195]\n"
  "s_nop 2\n"
  "v_mfma_f32_16x16x32_bf16 v[196:199], v[68:71], v[232:235], v[196:199]\n"
  "v_lshlrev_b32_sdwa v217, 1, v217 dst_sel:DWORD dst_unused:UNUSED_PAD src0_sel:DWORD src1_sel:WORD_1\n"
  "v_lshlrev_b32_sdwa v218, 1, v218 dst_sel:DWORD dst_unused:UNUSED_PAD src0_sel:DWORD src1_sel:WORD_1\n"
  "v_lshlrev_b32_sdwa v219, 1, v219 dst_sel:DWORD dst_unused:UNUSED_PAD src0_sel:DWORD src1_sel:WORD_1\n"
  "v_mfma_f32_16x16x32_bf16 v[168:171], v[120:123], v[200:203], v[168:171]\n"
  "ds_read_b128 v[240:243], v248 offset:10240\n"
  "v_cvt_pk_bf16_f32 v152, v152, v153\n"
  "v_cvt_pk_bf16_f32 v153, v154, v155\n"
  "v_cvt_pk_bf16_f32 v154, v156, v157\n"
  "v_cvt_pk_bf16_f32 v155, v158, v159\n"
  "v_mfma_f32_16x16x32_bf16 v[172:175], v[124:127], v[200:203], v[172:175]\n"
  "ds_write_b128 v249, v[152:155] offset:64\n"
  "v_mfma_f32_16x16x32_bf16 v[176:179], v[128:131], v[200:203], v[176:179]\n"
  "s_setprio 0\n"
  "ds_read_b128 v[244:247], v248 offset:11264\n"
  "v_lshlrev_b32_sdwa v220, 1, v220 dst_sel:DWORD dst_unused:UNUSED_PAD src0_sel:DWORD src1_sel:WORD_1\n"
  "v_mfma_f32_16x16x32_bf16 v[180:183], v[132:135], v[200:203], v[180:183]\n"
  "s_setprio 2\n"
  "ds_read_u16 v216, v216\n"
  "v_lshlrev_b32_sdwa v221, 1, v221 dst_sel:DWORD dst_unused:UNUSED_PAD src0_sel:DWORD src1_sel:WORD_1\n"
  "v_lshlrev_b32_sdwa v222, 1, v222 dst_sel:DWORD dst_unused:UNUSED_PAD src0_sel:DWORD src1_sel:WORD_1\n"
  "v_mfma_f32_16x16x32_bf16 v[184:187], v[136:139], v[200:203], v[184:187]\n"
  "s_waitcnt lgkmcnt(5)\n"
  "v_cvt_pk_u16_u32 v208, v208, v209\n"
  "v_cvt_pk_u16_u32 v209, v210, v211\n"
  "v_cvt_pk_u16_u32 v210, v212, v213\n"
  "v_cvt_pk_u16_u32 v211, v214, v215\n"
  "v_cvt_pk_bf16_f32 v160, v160, v161\n"
  "v_cvt_pk_bf16_f32 v161, v162, v163\n"
  "v_cvt_pk_bf16_f32 v162, v164, v165\n"
  "v_cvt_pk_bf16_f32 v163, v166, v167\n"
  "v_mfma_f32_16x16x32_bf16 v[188:191], v[140:143], v[200:203], v[188:191]\n"
  "s_setprio 0\n"
  "s_waitcnt lgkmcnt(1)\n"
  "v_lshlrev_b32_sdwa v223, 1, v223 dst_sel:DWORD dst_unused:UNUSED_PAD src0_sel:DWORD src1_sel:WORD_1\n"
  "v_mfma_f32_16x16x32_bf16 v[200:203], v[0:3], v[236:239], 0\n"
  "ds_write_b128 v249, v[160:163] offset:128\n"
  "v_lshlrev_b32_sdwa v194, 1, v194 dst_sel:DWORD dst_unused:UNUSED_PAD src0_sel:DWORD src1_sel:WORD_1\n"
  "v_lshlrev_b32_sdwa v195, 1, v195 dst_sel:DWORD dst_unused:UNUSED_PAD src0_sel:DWORD src1_sel:WORD_1\n"
  "v_fma_f32 v251, -v192, v192, s46\n"
  "v_mfma_f32_16x16x32_bf16 v[204:207], v[12:15], v[236:239], 0\n"
  "s_add_u32 s23, s23, 512\n"
  "s_nop 3\n"
  "v_mfma_f32_16x16x32_bf16 v[200:203], v[4:7], v[240:243], v[200:203]\n"
  "ds_read_u16 v217, v217\n"
  "v_fma_f32 v252, -v193, v193, s46\n"
  "v_lshlrev_b32_sdwa v196, 1, v196 dst_sel:DWORD dst_unused:UNUSED_PAD src0_sel:DWORD src1_sel:WORD_1\n"
  "v_mul_f32 v251, v192, v251\n"
  "v_mul_f32 v252, v193, v252\n"
  "v_lshlrev_b32_sdwa v197, 1, v197 dst_sel:DWORD dst_unused:UNUSED_PAD src0_sel:DWORD src1_sel:WORD_1\n"
  "v_mfma_f32_16x16x32_bf16 v[204:207], v[16:19], v[240:243], v[204:207]\n"
  "s_setprio 0\n"
  "ds_read_u16 v218, v218\n"
  "ds_read_u16 v219, v219\n"
  "v_exp_f32 v251, v251\n"
  "v_mfma_f32_16x16x32_bf16 v[200:203], v[8:11], v[244:247], v[200:203]\n"
  "s_setprio 2\n"
  "ds_read_u16 v220, v220\n"
  "v_exp_f32 v252, v252\n"
  "v_lshlrev_b32_sdwa v198, 1, v198 dst_sel:DWORD dst_unused:UNUSED_PAD src0_sel:DWORD src1_sel:WORD_1\n"
  "v_add_u32 v248, s54, v248\n"
  "v_mfma_f32_16x16x32_bf16 v[204:207], v[20:23], v[244:247], v[204:207]\n"
  "s_sub_u32 s54, 0, s54\n"
  "ds_read_addtid_b32 v254 offset:62464\n"
  "ds_read_u16 v221, v221\n"
  "ds_read_u16 v222, v222\n"
  "v_add_f32 v251, 1.0, v251\n"
  "v_mfma_f32_16x16x32_bf16 v[144:147], v[72:75], v[208:211], 0\n"
  "ds_read_u16 v223, v223\n"
  "v_add_f32 v252, 1.0, v252\n"
  "v_lshlrev_b32_sdwa v199, 1, v199 dst_sel:DWORD dst_unused:UNUSED_PAD src0_sel:DWORD src1_sel:WORD_1\n"
  "v_rcp_f32 v251, v251\n"
  "v_mfma_f32_16x16x32_bf16 v[148:151], v[76:79], v[208:211], 0\n"
  "ds_read_u16 v194, v194\n"
  "v_rcp_f32 v252, v252\n"
  "v_mul_f32 v192, v192, v251\n"
  "v_mul_f32 v193, v193, v252\n"
  "v_mfma_f32_16x16x32_bf16 v[152:155], v[80:83], v[208:211], 0\n"
  "s_sub_u32 s55, s55, 1\n"
  "s_setprio 0\n"
  "ds_read_u16 v195, v195\n"
  "ds_read_u16 v196, v196\n"
  "v_cvt_pk_bf16_f32 v192, v192, v193\n"
  "v_mfma_f32_16x16x32_bf16 v[156:159], v[84:87], v[208:211], 0\n"
  "ds_read_u16 v197, v197\n"
  "ds_read_u16 v198, v198\n"
  "ds_read_u16 v199, v199\n"
  "v_cvt_pk_bf16_f32 v168, v168, v169\n"
  "v_cvt_pk_bf16_f32 v169, v170, v171\n"
  "v_cvt_pk_bf16_f32 v170, v172, v173\n"
  "v_cvt_pk_bf16_f32 v171, v174, v175\n"
  "v_mfma_f32_16x16x32_bf16 v[160:163], v[88:91], v[208:211], 0\n"
  "s_waitcnt lgkmcnt(6)\n"
  "v_readfirstlane_b32 s22, v254\n"
  "v_cvt_pk_u16_u32 v216, v216, v217\n"
  "v_cvt_pk_u16_u32 v217, v218, v219\n"
  "v_cvt_pk_u16_u32 v218, v220, v221\n"
  "v_cvt_pk_u16_u32 v219, v222, v223\n"
  "v_mfma_f32_16x16x32_bf16 v[164:167], v[92:95], v[208:211], 0\n"
  "ds_write_b128 v249, v[168:171] offset:3072\n"
  ".p2align 6\n"
  "G_ev_%=:\n"
  "v_mfma_f32_16x16x32_bf16 v[208:211], v[24:27], v[236:239], 0\n"
  "s_cmp_ge_u32 s22, s23\n"
  "s_cbranch_scc0 Ga_slow_%=\n"
  "Ga_back_%=:\n"
  "v_mfma_f32_16x16x32_bf16 v[212:215], v[36:39], v[236:239], 0\n"
  "v_lshlrev_b32_sdwa v200, 1, v200 dst_sel:DWORD dst_unused:UNUSED_PAD src0_sel:DWORD src1_sel:WORD_1\n"
  "v_lshlrev_b32_sdwa v201, 1, v201 dst_sel:DWORD dst_unused:UNUSED_PAD src0_sel:DWORD src1_sel:WORD_1\n"
  "v_mfma_f32_16x16x32_bf16 v[208:211], v[28:31], v[240:243], v[208:211]\n"
  "ds_read_u16 v200, v200\n"
  "ds_read_u16 v201, v201\n"
  "v_mfma_f32_16x16x32_bf16 v[212:215], v[40:43], v[240:243], v[212:215]\n"
  "s_setprio 2\n"
  "v_lshlrev_b32_sdwa v202, 1, v202 dst_sel:DWORD dst_unused:UNUSED_PAD src0_sel:DWORD src1_sel:WORD_1\n"
  "v_lshlrev_b32_sdwa v203, 1, v203 dst_sel:DWORD dst_unused:UNUSED_PAD src0_sel:DWORD src1_sel:WORD_1\n"
  "v_mfma_f32_16x16x32_bf16 v[208:211], v[32:35], v[244:247], v[208:211]\n"
  "s_setprio 2\n"
  "ds_read_u16 v202, v202\n"
  "v_lshlrev_b32_sdwa v204, 1, v204 dst_sel:DWORD dst_unused:UNUSED_PAD src0_sel:DWORD src1_sel:WORD_1\n"
  "v_mfma_f32_16x16x32_bf16 v[212:215], v[44:47], v[244:247], v[212:215]\n"
  "ds_read_u16 v203, v203\n"
  "ds_read_u16 v204, v204\n"
  "v_mfma_f32_16x16x32_bf16 v[144:147], v[96:99], v[216:219], v[144:147]\n"
  "v_lshlrev_b32_sdwa v205, 1, v205 dst_sel:DWORD dst_unused:UNUSED_PAD src0_sel:DWORD src1_sel:WORD_1\n"
  "v_lshlrev_b32_sdwa v206, 1, v206 dst_sel:DWORD dst_unused:UNUSED_PAD src0_sel:DWORD src1_sel:WORD_1\n"
  "v_mfma_f32_16x16x32_bf16 v[148:151], v[100:103], v[216:219], v[148:151]\n"
  "ds_read_u16 v205, v205\n"
  "v_cvt_pk_bf16_f32 v176, v176, v177\n"
  "v_cvt_pk_bf16_f32 v177, v178, v179\n"
  "v_cvt_pk_bf16_f32 v178, v180, v181\n"
  "v_cvt_pk_bf16_f32 v179, v182, v183\n"
  "v_mfma_f32_16x16x32_bf16 v[152:155], v[104:107], v[216:219], v[152:155]\n"
  "s_setprio 0\n"
  "ds_write_b128 v249, v[176:179] offset:3136\n"
  "v_mfma_f32_16x16x32_bf16 v[156:159], v[108:111], v[216:219], v[156:159]\n"
  "s_setprio 2\n"
  "ds_read_u16 v206, v206\n"
  "v_mfma_f32_16x16x32_bf16 v[160:163], v[112:115], v[216:219], v[160:163]\n"
  "s_waitcnt lgkmcnt(9)\n"
  "v_cvt_pk_u16_u32 v193, v194, v195\n"
  "v_cvt_pk_u16_u32 v194, v196, v197\n"
  "v_cvt_pk_u16_u32 v195, v198, v199\n"
  "v_mfma_f32_16x16x32_bf16 v[164:167], v[116:119], v[216:219], v[164:167]\n"
  "ds_read_b128 v[224:227], v248\n"
  "v_lshlrev_b32_sdwa v207, 1, v207 dst_sel:DWORD dst_unused:UNUSED_PAD src0_sel:DWORD src1_sel:WORD_1\n"
  "v_mfma_f32_16x16x32_bf16 v[216:219], v[48:51], v[236:239], 0\n"
  "ds_read_u16 v207, v207\n"
  "v_cvt_pk_bf16_f32 v184, v184, v185\n"
  "v_cvt_pk_bf16_f32 v185, v186, v187\n"
  "v_cvt_pk_bf16_f32 v186, v188, v189\n"
  "v_cvt_pk_bf16_f32 v187, v190, v191\n"
  "v_mfma_f32_16x16x32_bf16 v[220:223], v[60:63], v[236:239], 0\n"
  "ds_write_b128 v249, v[184:187] offset:3200\n"
  "v_mfma_f32_16x16x32_bf16 v[216:219], v[52:55], v[240:243], v[216:219]\n"
  "ds_append v254 offset:62464\n"
  "v_mfma_f32_16x16x32_bf16 v[220:223], v[64:67], v[240:243], v[220:223]\n"
  "s_setprio 0\n"
  "ds_read_b128 v[228:231], v248 offset:1024\n"
  "v_mfma_f32_16x16x32_bf16 v[216:219], v[56:59], v[244:247], v[216:219]\n"
  "s_setprio 2\n"
  "ds_read_b128 v[232:235], v248 offset:2048\n"
  "v_lshlrev_b32_sdwa v208, 1, v208 dst_sel:DWORD dst_unused:UNUSED_PAD src0_sel:DWORD src1_sel:WORD_1\n"
  "v_mfma_f32_16x16x32_bf16 v[220:223], v[68:71], v[244:247], v[220:223]\n"
  "ds_read_u16 v208, v208\n"
  "v_lshlrev_b32_sdwa v209, 1, v209 dst_sel:DWORD dst_unused:UNUSED_PAD src0_sel:DWORD src1_sel:WORD_1\n"
  "v_lshlrev_b32_sdwa v210, 1, v210 dst_sel:DWORD dst_unused:UNUSED_PAD src0_sel:DWORD src1_sel:WORD_1\n"
  "v_mfma_f32_16x16x32_bf16 v[144:147], v[120:123], v[192:195], v[144:147]\n"
  "ds_read_u16 v209, v209\n"
  "v_lshlrev_b32_sdwa v211, 1, v211 dst_sel:DWORD dst_unused:UNUSED_PAD src0_sel:DWORD src1_sel:WORD_1\n"
  "v_lshlrev_b32_sdwa v212, 1, v212 dst_sel:DWORD dst_unused:UNUSED_PAD src0_sel:DWORD src1_sel:WORD_1\n"
  "v_mfma_f32_16x16x32_bf16 v[148:151], v[124:127], v[192:195], v[148:151]\n"
  "ds_read_u16 v210, v210\n"
  "v_lshlrev_b32_sdwa v213, 1, v213 dst_sel:DWORD dst_unused:UNUSED_PAD src0_sel:DWORD src1_sel:WORD_1\n"
  "v_mfma_f32_16x16x32_bf16 v[152:155], v[128:131], v[192:195], v[152:155]\n"
  "s_setprio 0\n"
  "ds_read_u16 v211, v211\n"
  "ds_read_u16 v212, v212\n"
  "ds_read_u16 v213, v213\n"
  "v_mfma_f32_16x16x32_bf16 v[156:159], v[132:135], v[192:195], v[156:159]\n"
  "s_setprio 2\n"
  "v_lshlrev_b32_sdwa v214, 1, v214 dst_sel:DWORD dst_unused:UNUSED_PAD src0_sel:DWORD src1_sel:WORD_1\n"
  "v_mfma_f32_16x16x32_bf16 v[160:163], v[136:139], v[192:195], v[160:163]\n"
  "s_waitcnt lgkmcnt(10)\n"
  "v_cvt_pk_u16_u32 v200, v200, v201\n"
  "v_cvt_pk_u16_u32 v201, v202, v203\n"
  "v_cvt_pk_u16_u32 v202, v204, v205\n"
  "v_cvt_pk_u16_u32 v203, v206, v207\n"
  "v_mfma_f32_16x16x32_bf16 v[164:167], v[140:143], v[192:195], v[164:167]\n"
  "s_waitcnt lgkmcnt(6)\n"
  "ds_read_u16 v214, v214\n"
  "v_lshlrev_b32_sdwa v215, 1, v215 dst_sel:DWORD dst_unused:UNUSED_PAD src0_sel:DWORD src1_sel:WORD_1\n"
  "v_mfma_f32_16x16x32_bf16 v[192:195], v[0:3], v[224:227], 0\n"
  "ds_read_u16 v215, v215\n"
  "v_lshlrev_b32_sdwa v218, 1, v218 dst_sel:DWORD dst_unused:UNUSED_PAD src0_sel:DWORD src1_sel:WORD_1\n"
  "v_lshlrev_b32_sdwa v219, 1, v219 dst_sel:DWORD dst_unused:UNUSED_PAD src0_sel:DWORD src1_sel:WORD_1\n"
  "v_mfma_f32_16x16x32_bf16 v[196:199], v[12:15], v[224:227], 0\n"
  "ds_read_u16 v218, v218\n"
  "v_fma_f32 v251, -v216, v216, s46\n"
  "v_mfma_f32_16x16x32_bf16 v[192:195], v[4:7], v[228:231], v[192:195]\n"
  "ds_read_u16 v219, v219\n"
  "v_fma_f32 v252, -v217, v217, s46\n"
  "v_lshlrev_b32_sdwa v220, 1, v220 dst_sel:DWORD dst_unused:UNUSED_PAD src0_sel:DWORD src1_sel:WORD_1\n"
  "v_mul_f32 v251, v216, v251\n"
  "v_mfma_f32_16x16x32_bf16 v[196:199], v[16:19], v[228:231], v[196:199]\n"
  "s_setprio 0\n"
  "v_mul_f32 v252, v217, v252\n"
  "v_lshlrev_b32_sdwa v221, 1, v221 dst_sel:DWORD dst_unused:UNUSED_PAD src0_sel:DWORD src1_sel:WORD_1\n"
  "v_exp_f32 v251, v251\n"
  "v_mfma_f32_16x16x32_bf16 v[192:195], v[8:11], v[232:235], v[192:195]\n"
  "s_setprio 2\n"
  "ds_read_u16 v220, v220\n"
  "v_exp_f32 v252, v252\n"
  "v_mfma_f32_16x16x32_bf16 v[196:199], v[20:23], v[232:235], v[196:199]\n"
  "v_lshlrev_b32_sdwa v222, 1, v222 dst_sel:DWORD dst_unused:UNUSED_PAD src0_sel:DWORD src1_sel:WORD_1\n"
  "v_add_f32 v251, 1.0, v251\n"
  "v_add_f32 v252, 1.0, v252\n"
  "v_lshlrev_b32_sdwa v223, 1, v223 dst_sel:DWORD dst_unused:UNUSED_PAD src0_sel:DWORD src1_sel:WORD_1\n"
  "v_mfma_f32_16x16x32_bf16 v[168:171], v[72:75], v[200:203], 0\n"
  "ds_read_u16 v221, v221\n"
  "v_rcp_f32 v251, v251\n"
  "v_mfma_f32_16x16x32_bf16 v[172:175], v[76:79], v[200:203], 0\n"
  "ds_read_u16 v222, v222\n"
  "ds_read_u16 v223, v223\n"
  "v_mfma_f32_16x16x32_bf16 v[176:179], v[80:83], v[200:203], 0\n"
  "s_setprio 0\n"
  "v_rcp_f32 v252, v252\n"
  "v_mfma_f32_16x16x32_bf16 v[180:183], v[84:87], v[200:203], 0\n"
  "s_setprio 2\n"
  "v_cvt_pk_bf16_f32 v144, v144, v145\n"
  "v_cvt_pk_bf16_f32 v145, v146, v147\n"
  "v_cvt_pk_bf16_f32 v146, v148, v149\n"
  "v_cvt_pk_bf16_f32 v147, v150, v151\n"
  "v_mfma_f32_16x16x32_bf16 v[184:187], v[88:91], v[200:203], 0\n"
  "s_waitcnt lgkmcnt(6)\n"
  "v_cvt_pk_u16_u32 v208, v208, v209\n"
  "v_cvt_pk_u16_u32 v209, v210, v211\n"
  "v_cvt_pk_u16_u32 v210, v212, v213\n"
  "v_cvt_pk_u16_u32 v211, v214, v215\n"
  "v_mfma_f32_16x16x32_bf16 v[188:191], v[92:95], v[200:203], 0\n"
  "v_mul_f32 v216, v216, v251\n"
  "v_mul_f32 v217, v217, v252\n"
  "v_cvt_pk_bf16_f32 v216, v216, v217\n"
  "v_mfma_f32_16x16x32_bf16 v[200:203], v[24:27], v[224:227], 0\n"
  "ds_write_b128 v249, v[144:147] offset:24576\n"
  "v_mfma_f32_16x16x32_bf16 v[204:207], v[36:39], v[224:227], 0\n"
  "v_lshlrev_b32_sdwa v192, 1, v192 dst_sel:DWORD dst_unused:UNUSED_PAD src0_sel:DWORD src1_sel:WORD_1\n"
  "v_lshlrev_b32_sdwa v193, 1, v193 dst_sel:DWORD dst_unused:UNUSED_PAD src0_sel:DWORD src1_sel:WORD_1\n"
  "v_mfma_f32_16x16x32_bf16 v[200:203], v[28:31], v[228:231], v[200:203]\n"
  "ds_read_u16 v192, v192\n"
  "ds_read_u16 v193, v193\n"
  "v_mfma_f32_16x16x32_bf16 v[204:207], v[40:43], v[228:231], v[204:207]\n"
  "s_setprio 0\n"
  "v_lshlrev_b32_sdwa v194, 1, v194 dst_sel:DWORD dst_unused:UNUSED_PAD src0_sel:DWORD src1_sel:WORD_1\n"
  "v_mfma_f32_16x16x32_bf16 v[200:203], v[32:35], v[232:235], v[200:203]\n"
  "s_setprio 2\n"
  "ds_read_u16 v194, v194\n"
  "v_lshlrev_b32_sdwa v195, 1, v195 dst_sel:DWORD dst_unused:UNUSED_PAD src0_sel:DWORD src1_sel:WORD_1\n"
  "v_mfma_f32_16x16x32_bf16 v[204:207], v[44:47], v[232:235], v[204:207]\n"
  "ds_read_u16 v195, v195\n"
  "v_mfma_f32_16x16x32_bf16 v[168:171], v[96:99], v[208:211], v[168:171]\n"
  "v_lshlrev_b32_sdwa v196, 1, v196 dst_sel:DWORD dst_unused:UNUSED_PAD src0_sel:DWORD src1_sel:WORD_1\n"
  "v_lshlrev_b32_sdwa v197, 1, v197 dst_sel:DWORD dst_unused:UNUSED_PAD src0_sel:DWORD src1_sel:WORD_1\n"
  "v_mfma_f32_16x16x32_bf16 v[172:175], v[100:103], v[208:211], v[172:175]\n"
  "v_cvt_pk_bf16_f32 v152, v152, v153\n"
  "v_cvt_pk_bf16_f32 v153, v154, v155\n"
  "v_cvt_pk_bf16_f32 v154, v156, v157\n"
  "v_cvt_pk_bf16_f32 v155, v158, v159\n"
  "v_mfma_f32_16x16x32_bf16 v[176:179], v[104:107], v[208:211], v[176:179]\n"
  "s_setprio 0\n"
  "ds_write_b128 v249, v[152:155] offset:24640\n"
  "v_mfma_f32_16x16x32_bf16 v[180:183], v[108:111], v[208:211], v[180:183]\n"
  "s_setprio 2\n"
  "ds_read_u16 v196, v196\n"
  "v_lshlrev_b32_sdwa v198, 1, v198 dst_sel:DWORD dst_unused:UNUSED_PAD src0_sel:DWORD src1_sel:WORD_1\n"
  "v_mfma_f32_16x16x32_bf16 v[184:187], v[112:115], v[208:211], v[184:187]\n"
  "s_waitcnt lgkmcnt(7)\n"
  "v_cvt_pk_u16_u32 v217, v218, v219\n"
  "v_cvt_pk_u16_u32 v218, v220, v221\n"
  "v_cvt_pk_u16_u32 v219, v222, v223\n"
  "v_mfma_f32_16x16x32_bf16 v[188:191], v[116:119], v[208:211], v[188:191]\n"
  "ds_read_u16 v197, v197\n"
  "v_lshlrev_b32_sdwa v199, 1, v199 dst_sel:DWORD dst_unused:UNUSED_PAD src0_sel:DWORD src1_sel:WORD_1\n"
  "v_mfma_f32_16x16x32_bf16 v[208:211], v[48:51], v[224:227], 0\n"
  "ds_read_b128 v[236:239], v248 offset:3072\n"
  "v_cvt_pk_bf16_f32 v160, v160, v161\n"
  "v_cvt_pk_bf16_f32 v161, v162, v163\n"
  "v_cvt_pk_bf16_f32 v162, v164, v165\n"
  "v_cvt_pk_bf16_f32 v163, v166, v167\n"
  "v_mfma_f32_16x16x32_bf16 v[212:215], v[60:63], v[224:227], 0\n"
  "ds_write_b128 v249, v[160:163] offset:24704\n"
  "v_mfma_f32_16x16x32_bf16 v[208:211], v[52:55], v[228:231], v[208:211]\n"
  "ds_read_u16 v198, v198\n"
  "v_mfma_f32_16x16x32_bf16 v[212:215], v[64:67], v[228:231], v[212:215]\n"
  "s_setprio 0\n"
  "s_nop 1\n"
  "v_mfma_f32_16x16x32_bf16 v[208:211], v[56:59], v[232:235], v[208:211]\n"
  "s_setprio 2\n"
  "ds_read_b128 v[240:243], v248 offset:4096\n"
  "v_mfma_f32_16x16x32_bf16 v[212:215], v[68:71], v[232:235], v[212:215]\n"
  "v_lshlrev_b32_sdwa v200, 1, v200 dst_sel:DWORD dst_unused:UNUSED_PAD src0_sel:DWORD src1_sel:WORD_1\n"
  "v_lshlrev_b32_sdwa v201, 1, v201 dst_sel:DWORD dst_unused:UNUSED_PAD src0_sel:DWORD src1_sel:WORD_1\n"
  "v_mfma_f32_16x16x32_bf16 v[168:171], v[120:123], v[216:219], v[168:171]\n"
  "ds_read_b128 v[244:247], v248 offset:5120\n"
  "v_lshlrev_b32_sdwa v202, 1, v202 dst_sel:DWORD dst_unused:UNUSED_PAD src0_sel:DWORD src1_sel:WORD_1\n"
  "v_lshlrev_b32_sdwa v203, 1, v203 dst_sel:DWORD dst_unused:UNUSED_PAD src0_sel:DWORD src1_sel:WORD_1\n"
  "v_mfma_f32_16x16x32_bf16 v[172:175], v[124:127], v[216:219], v[172:175]\n"
  "ds_read_u16 v199, v199\n"
  "v_lshlrev_b32_sdwa v204, 1, v204 dst_sel:DWORD dst_unused:UNUSED_PAD src0_sel:DWORD src1_sel:WORD_1\n"
  "v_mfma_f32_16x16x32_bf16 v[176:179], v[128:131], v[216:219], v[176:179]\n"
  "s_setprio 0\n"
  "ds_read_u16 v200, v200\n"
  "v_lshlrev_b32_sdwa v205, 1, v205 dst_sel:DWORD dst_unused:UNUSED_PAD src0_sel:DWORD src1_sel:WORD_1\n"
  "v_mfma_f32_16x16x32_bf16 v[180:183], v[132:135], v[216:219], v[180:183]\n"
  "s_setprio 2\n"
  "ds_read_u16 v201, v201\n"
  "v_lshlrev_b32_sdwa v206, 1, v206 dst_sel:DWORD dst_unused:UNUSED_PAD src0_sel:DWORD src1_sel:WORD_1\n"
  "v_mfma_f32_16x16x32_bf16 v[184:187], v[136:139], v[216:219], v[184:187]\n"
  "s_waitcnt lgkmcnt(2)\n"
  "v_cvt_pk_u16_u32 v192, v192, v193\n"
  "v_cvt_pk_u16_u32 v193, v194, v195\n"
  "v_cvt_pk_u16_u32 v194, v196, v197\n"
  "v_cvt_pk_u16_u32 v195, v198, v199\n"
  "v_mfma_f32_16x16x32_bf16 v[188:191], v[140:143], v[216:219], v[188:191]\n"
  "ds_read_u16 v202, v202\n"
  "v_lshlrev_b32_sdwa v207, 1, v207 dst_sel:DWORD dst_unused:UNUSED_PAD src0_sel:DWORD src1_sel:WORD_1\n"
  "v_mfma_f32_16x16x32_bf16 v[216:219], v[0:3], v[236:239], 0\n"
  "ds_read_u16 v203, v203\n"
  "v_lshlrev_b32_sdwa v210, 1, v210 dst_sel:DWORD dst_unused:UNUSED_PAD src0_sel:DWORD src1_sel:WORD_1\n"
  "v_lshlrev_b32_sdwa v211, 1, v211 dst_sel:DWORD dst_unused:UNUSED_PAD src0_sel:DWORD src1_sel:WORD_1\n"
  "v_fma_f32 v251, -v208, v208, s46\n"
  "v_mfma_f32_16x16x32_bf16 v[220:223], v[12:15], v[236:239], 0\n"
  "s_add_u32 s23, s23, 512\n"
  "ds_read_u16 v204, v204\n"
  "v_fma_f32 v252, -v209, v209, s46\n"
  "v_lshlrev_b32_sdwa v212, 1, v212 dst_sel:DWORD dst_unused:UNUSED_PAD src0_sel:DWORD src1_sel:WORD_1\n"
  "v_mul_f32 v251, v208, v251\n"
  "v_mfma_f32_16x16x32_bf16 v[216:219], v[4:7], v[240:243], v[216:219]\n"
  "ds_read_u16 v205, v205\n"
  "v_mul_f32 v252, v209, v252\n"
  "v_lshlrev_b32_sdwa v213, 1, v213 dst_sel:DWORD dst_unused:UNUSED_PAD src0_sel:DWORD src1_sel:WORD_1\n"
  "v_exp_f32 v251, v251\n"
  "v_mfma_f32_16x16x32_bf16 v[220:223], v[16:19], v[240:243], v[220:223]\n"
  "s_setprio 0\n"
  "ds_read_u16 v206, v206\n"
  "ds_read_u16 v207, v207\n"
  "v_mfma_f32_16x16x32_bf16 v[216:219], v[8:11], v[244:247], v[216:219]\n"
  "s_setprio 2\n"
  "ds_read_u16 v210, v210\n"
  "v_exp_f32 v252, v252\n"
  "v_lshlrev_b32_sdwa v214, 1, v214 dst_sel:DWORD dst_unused:UNUSED_PAD src0_sel:DWORD src1_sel:WORD_1\n"
  "v_mfma_f32_16x16x32_bf16 v[220:223], v[20:23], v[244:247], v[220:223]\n"
  "ds_read_addtid_b32 v254 offset:62464\n"
  "ds_read_u16 v211, v211\n"
  "ds_read_u16 v212, v212\n"
  "v_mfma_f32_16x16x32_bf16 v[144:147], v[72:75], v[192:195], 0\n"
  "ds_read_u16 v213, v213\n"
  "v_add_f32 v251, 1.0, v251\n"
  "v_add_f32 v252, 1.0, v252\n"
  "v_lshlrev_b32_sdwa v215, 1, v215 dst_sel:DWORD dst_unused:UNUSED_PAD src0_sel:DWORD src1_sel:WORD_1\n"
  "v_rcp_f32 v251, v251\n"
  "v_mfma_f32_16x16x32_bf16 v[148:151], v[76:79], v[192:195], 0\n"
  "ds_read_u16 v214, v214\n"
  "v_rcp_f32 v252, v252\n"
  "v_mfma_f32_16x16x32_bf16 v[152:155], v[80:83], v[192:195], 0\n"
  "s_setprio 0\n"
  "ds_read_u16 v215, v215\n"
  "v_mfma_f32_16x16x32_bf16 v[156:159], v[84:87], v[192:195], 0\n"
  "s_setprio 2\n"
  "v_cvt_pk_bf16_f32 v168, v168, v169\n"
  "v_cvt_pk_bf16_f32 v169, v170, v171\n"
  "v_cvt_pk_bf16_f32 v170, v172, v173\n"
  "v_cvt_pk_bf16_f32 v171, v174, v175\n"
  "v_mfma_f32_16x16x32_bf16 v[160:163], v[88:91], v[192:195], 0\n"
  "s_waitcnt lgkmcnt(5)\n"
  "v_readfirstlane_b32 s22, v254\n"
  "v_cvt_pk_u16_u32 v200, v200, v201\n"
  "v_cvt_pk_u16_u32 v201, v202, v203\n"
  "v_cvt_pk_u16_u32 v202, v204, v205\n"
  "v_cvt_pk_u16_u32 v203, v206, v207\n"
  "v_mfma_f32_16x16x32_bf16 v[164:167], v[92:95], v[192:195], 0\n"
  "ds_write_b128 v249, v[168:171] offset:27648\n"
  "v_mul_f32 v208, v208, v251\n"
  "v_mul_f32 v209, v209, v252\n"
  "v_cvt_pk_bf16_f32 v208, v208, v209\n"
  "G_od_%=:\n"
  "v_mfma_f32_16x16x32_bf16 v[192:195], v[24:27], v[236:239], 0\n"
  "s_cmp_ge_u32 s22, s23\n"
  "s_cbranch_scc0 Gb_slow_%=\n"
  "Gb_back_%=:\n"
  "v_mfma_f32_16x16x32_bf16 v[196:199], v[36:39], v[236:239], 0\n"
  "v_lshlrev_b32_sdwa v216, 1, v216 dst_sel:DWORD dst_unused:UNUSED_PAD src0_sel:DWORD src1_sel:WORD_1\n"
  "v_lshlrev_b32_sdwa v217, 1, v217 dst_sel:DWORD dst_unused:UNUSED_PAD src0_sel:DWORD src1_sel:WORD_1\n"
  "v_mfma_f32_16x16x32_bf16 v[192:195], v[28:31], v[240:243], v[192:195]\n"
  "ds_read_u16 v216, v216\n"
  "ds_read_u16 v217, v217\n"
  "v_mfma_f32_16x16x32_bf16 v[196:199], v[40:43], v[240:243], v[196:199]\n"
  "s_setprio 2\n"
  "v_lshlrev_b32_sdwa v218, 1, v218 dst_sel:DWORD dst_unused:UNUSED_PAD src0_sel:DWORD src1_sel:WORD_1\n"
  "v_lshlrev_b32_sdwa v219, 1, v219 dst_sel:DWORD dst_unused:UNUSED_PAD src0_sel:DWORD src1_sel:WORD_1\n"
  "v_mfma_f32_16x16x32_bf16 v[192:195], v[32:35], v[244:247], v[192:195]\n"
  "s_setprio 2\n"
  "ds_read_u16 v218, v218\n"
  "v_lshlrev_b32_sdwa v220, 1, v220 dst_sel:DWORD dst_unused:UNUSED_PAD src0_sel:DWORD src1_sel:WORD_1\n"
  "v_mfma_f32_16x16x32_bf16 v[196:199], v[44:47], v[244:247], v[196:199]\n"
  "ds_read_u16 v219, v219\n"
  "ds_read_u16 v220, v220\n"
  "v_mfma_f32_16x16x32_bf16 v[144:147], v[96:99], v[200:203], v[144:147]\n"
  "v_lshlrev_b32_sdwa v221, 1, v221 dst_sel:DWORD dst_unused:UNUSED_PAD src0_sel:DWORD src1_sel:WORD_1\n"
  "v_lshlrev_b32_sdwa v222, 1, v222 dst_sel:DWORD dst_unused:UNUSED_PAD src0_sel:DWORD src1_sel:WORD_1\n"
  "v_mfma_f32_16x16x32_bf16 v[148:151], v[100:103], v[200:203], v[148:151]\n"
  "ds_read_u16 v221, v221\n"
  "v_cvt_pk_bf16_f32 v176, v176, v177\n"
  "v_cvt_pk_bf16_f32 v177, v178, v179\n"
  "v_cvt_pk_bf16_f32 v178, v180, v181\n"
  "v_cvt_pk_bf16_f32 v179, v182, v183\n"
  "v_mfma_f32_16x16x32_bf16 v[152:155], v[104:107], v[200:203], v[152:155]\n"
  "s_setprio 0\n"
  "ds_write_b128 v249, v[176:179] offset:27712\n"
  "v_mfma_f32_16x16x32_bf16 v[156:159], v[108:111], v[200:203], v[156:159]\n"
  "s_setprio 2\n"
  "ds_read_u16 v222, v222\n"
  "v_mfma_f32_16x16x32_bf16 v[160:163], v[112:115], v[200:203], v[160:163]\n"
  "s_waitcnt lgkmcnt(9)\n"
  "v_cvt_pk_u16_u32 v209, v210, v211\n"
  "v_cvt_pk_u16_u32 v210, v212, v213\n"
  "v_cvt_pk_u16_u32 v211, v214, v215\n"
  "v_mfma_f32_16x16x32_bf16 v[164:167], v[116:119], v[200:203], v[164:167]\n"
  "ds_read_b128 v[224:227], v248 offset:6144\n"
  "v_lshlrev_b32_sdwa v223, 1, v223 dst_sel:DWORD dst_unused:UNUSED_PAD src0_sel:DWORD src1_sel:WORD_1\n"
  "v_mfma_f32_16x16x32_bf16 v[200:203], v[48:51], v[236:239], 0\n"
  "ds_read_u16 v223, v223\n"
  "v_cvt_pk_bf16_f32 v184, v184, v185\n"
  "v_cvt_pk_bf16_f32 v185, v186, v187\n"
  "v_cvt_pk_bf16_f32 v186, v188, v189\n"
  "v_cvt_pk_bf16_f32 v187, v190, v191\n"
  "v_mfma_f32_16x16x32_bf16 v[204:207], v[60:63], v[236:239], 0\n"
  "ds_write_b128 v249, v[184:187] offset:27776\n"
  "v_mfma_f32_16x16x32_bf16 v[200:203], v[52:55], v[240:243], v[200:203]\n"
  "ds_append v254 offset:62464\n"
  "v_mfma_f32_16x16x32_bf16 v[204:207], v[64:67], v[240:243], v[204:207]\n"
  "s_setprio 0\n"
  "ds_read_b128 v[228:231], v248 offset:7168\n"
  "v_mfma_f32_16x16x32_bf16 v[200:203], v[56:59], v[244:247], v[200:203]\n"
  "s_setprio 2\n"
  "ds_read_b128 v[232:235], v248 offset:8192\n"
  "v_lshlrev_b32_sdwa v192, 1, v192 dst_sel:DWORD dst_unused:UNUSED_PAD src0_sel:DWORD src1_sel:WORD_1\n"
  "v_mfma_f32_16x16x32_bf16 v[204:207], v[68:71], v[244:247], v[204:207]\n"
  "ds_read_u16 v192, v192\n"
  "v_lshlrev_b32_sdwa v193, 1, v193 dst_sel:DWORD dst_unused:UNUSED_PAD src0_sel:DWORD src1_sel:WORD_1\n"
  "v_mfma_f32_16x16x32_bf16 v[144:147], v[120:123], v[208:211], v[144:147]\n"
  "ds_read_u16 v193, v193\n"
  "v_lshlrev_b32_sdwa v194, 1, v194 dst_sel:DWORD dst_unused:UNUSED_PAD src0_sel:DWORD src1_sel:WORD_1\n"
  "v_lshlrev_b32_sdwa v195, 1, v195 dst_sel:DWORD dst_unused:UNUSED_PAD src0_sel:DWORD src1_sel:WORD_1\n"
  "v_mfma_f32_16x16x32_bf16 v[148:151], v[124:127], v[208:211], v[148:151]\n"
  "ds_read_u16 v194, v194\n"
  "v_lshlrev_b32_sdwa v196, 1, v196 dst_sel:DWORD dst_unused:UNUSED_PAD src0_sel:DWORD src1_sel:WORD_1\n"
  "v_mfma_f32_16x16x32_bf16 v[152:155], v[128:131], v[208:211], v[152:155]\n"
  "s_setprio 0\n"
  "ds_read_u16 v195, v195\n"
  "v_lshlrev_b32_sdwa v197, 1, v197 dst_sel:DWORD dst_unused:UNUSED_PAD src0_sel:DWORD src1_sel:WORD_1\n"
  "v_mfma_f32_16x16x32_bf16 v[156:159], v[132:135], v[208:211], v[156:159]\n"
  "s_setprio 2\n"
  "ds_read_u16 v196, v196\n"
  "v_lshlrev_b32_sdwa v198, 1, v198 dst_sel:DWORD dst_unused:UNUSED_PAD src0_sel:DWORD src1_sel:WORD_1\n"
  "v_mfma_f32_16x16x32_bf16 v[160:163], v[136:139], v[208:211], v[160:163]\n"
  "s_waitcnt lgkmcnt(9)\n"
  "v_cvt_pk_u16_u32 v216, v216, v217\n"
  "v_cvt_pk_u16_u32 v217, v218, v219\n"
  "v_cvt_pk_u16_u32 v218, v220, v221\n"
  "v_cvt_pk_u16_u32 v219, v222, v223\n"
  "v_mfma_f32_16x16x32_bf16 v[164:167], v[140:143], v[208:211], v[164:167]\n"
  "s_waitcnt lgkmcnt(5)\n"
  "ds_read_u16 v197, v197\n"
  "v_lshlrev_b32_sdwa v199, 1, v199 dst_sel:DWORD dst_unused:UNUSED_PAD src0_sel:DWORD src1_sel:WORD_1\n"
  "v_mfma_f32_16x16x32_bf16 v[208:211], v[0:3], v[224:227], 0\n"
  "ds_read_u16 v198, v198\n"
  "v_lshlrev_b32_sdwa v202, 1, v202 dst_sel:DWORD dst_unused:UNUSED_PAD src0_sel:DWORD src1_sel:WORD_1\n"
  "v_lshlrev_b32_sdwa v203, 1, v203 dst_sel:DWORD dst_unused:UNUSED_PAD src0_sel:DWORD src1_sel:WORD_1\n"
  "v_mfma_f32_16x16x32_bf16 v[212:215], v[12:15], v[224:227], 0\n"
  "ds_read_u16 v199, v199\n"
  "v_fma_f32 v251, -v200, v200, s46\n"
  "v_fma_f32 v252, -v201, v201, s46\n"
  "v_mfma_f32_16x16x32_bf16 v[208:211], v[4:7], v[228:231], v[208:211]\n"
  "ds_read_u16 v202, v202\n"
  "v_lshlrev_b32_sdwa v204, 1, v204 dst_sel:DWORD dst_unused:UNUSED_PAD src0_sel:DWORD src1_sel:WORD_1\n"
  "v_mul_f32 v251, v200, v251\n"
  "v_mul_f32 v252, v201, v252\n"
  "v_mfma_f32_16x16x32_bf16 v[212:215], v[16:19], v[228:231], v[212:215]\n"
  "s_setprio 0\n"
  "v_lshlrev_b32_sdwa v205, 1, v205 dst_sel:DWORD dst_unused:UNUSED_PAD src0_sel:DWORD src1_sel:WORD_1\n"
  "v_exp_f32 v251, v251\n"
  "v_mfma_f32_16x16x32_bf16 v[208:211], v[8:11], v[232:235], v[208:211]\n"
  "s_setprio 2\n"
  "ds_read_u16 v203, v203\n"
  "v_exp_f32 v252, v252\n"
  "v_lshlrev_b32_sdwa v206, 1, v206 dst_sel:DWORD dst_unused:UNUSED_PAD src0_sel:DWORD src1_sel:WORD_1\n"
  "v_mfma_f32_16x16x32_bf16 v[212:215], v[20:23], v[232:235], v[212:215]\n"
  "v_add_f32 v251, 1.0, v251\n"
  "v_add_f32 v252, 1.0, v252\n"
  "v_lshlrev_b32_sdwa v207, 1, v207 dst_sel:DWORD dst_unused:UNUSED_PAD src0_sel:DWORD src1_sel:WORD_1\n"
  "v_mfma_f32_16x16x32_bf16 v[168:171], v[72:75], v[216:219], 0\n"
  "ds_read_u16 v204, v204\n"
  "v_rcp_f32 v251, v251\n"
  "v_rcp_f32 v252, v252\n"
  "v_mfma_f32_16x16x32_bf16 v[172:175], v[76:79], v[216:219], 0\n"
  "ds_read_u16 v205, v205\n"
  "ds_read_u16 v206, v206\n"
  "ds_read_u16 v207, v207\n"
  "v_mfma_f32_16x16x32_bf16 v[176:179], v[80:83], v[216:219], 0\n"
  "s_setprio 0\n"
  "v_mfma_f32_16x16x32_bf16 v[180:183], v[84:87], v[216:219], 0\n"
  "s_setprio 2\n"
  "v_cvt_pk_bf16_f32 v144, v144, v145\n"
  "v_cvt_pk_bf16_f32 v145, v146, v147\n"
  "v_cvt_pk_bf16_f32 v146, v148, v149\n"
  "v_cvt_pk_bf16_f32 v147, v150, v151\n"
  "v_mfma_f32_16x16x32_bf16 v[184:187], v[88:91], v[216:219], 0\n"
  "s_waitcnt lgkmcnt(6)\n"
  "v_cvt_pk_u16_u32 v192, v192, v193\n"
  "v_cvt_pk_u16_u32 v193, v194, v195\n"
  "v_cvt_pk_u16_u32 v194, v196, v197\n"
  "v_cvt_pk_u16_u32 v195, v198, v199\n"
  "v_mfma_f32_16x16x32_bf16 v[188:191], v[92:95], v[216:219], 0\n"
  "v_mul_f32 v200, v200, v251\n"
  "v_mul_f32 v201, v201, v252\n"
  "v_cvt_pk_bf16_f32 v200, v200, v201\n"
  "v_mfma_f32_16x16x32_bf16 v[216:219], v[24:27], v[224:227], 0\n"
  "ds_write_b128 v249, v[144:147]\n"
  "v_mfma_f32_16x16x32_bf16 v[220:223], v[36:39], v[224:227], 0\n"
  "v_lshlrev_b32_sdwa v208, 1, v208 dst_sel:DWORD dst_unused:UNUSED_PAD src0_sel:DWORD src1_sel:WORD_1\n"
  "v_lshlrev_b32_sdwa v209, 1, v209 dst_sel:DWORD dst_unused:UNUSED_PAD src0_sel:DWORD src1_sel:WORD_1\n"
  "v_mfma_f32_16x16x32_bf16 v[216:219], v[28:31], v[228:231], v[216:219]\n"
  "ds_read_u16 v208, v208\n"
  "ds_read_u16 v209, v209\n"
  "v_mfma_f32_16x16x32_bf16 v[220:223], v[40:43], v[228:231], v[220:223]\n"
  "s_setprio 0\n"
  "v_lshlrev_b32_sdwa v210, 1, v210 dst_sel:DWORD dst_unused:UNUSED_PAD src0_sel:DWORD src1_sel:WORD_1\n"
  "v_mfma_f32_16x16x32_bf16 v[216:219], v[32:35], v[232:235], v[216:219]\n"
  "s_setprio 2\n"
  "ds_read_u16 v210, v210\n"
  "v_lshlrev_b32_sdwa v211, 1, v211 dst_sel:DWORD dst_unused:UNUSED_PAD src0_sel:DWORD src1_sel:WORD_1\n"
  "v_mfma_f32_16x16x32_bf16 v[220:223], v[44:47], v[232:235], v[220:223]\n"
  "ds_read_u16 v211, v211\n"
  "v_mfma_f32_16x16x32_bf16 v[168:171], v[96:99], v[192:195], v[168:171]\n"
  "v_lshlrev_b32_sdwa v212, 1, v212 dst_sel:DWORD dst_unused:UNUSED_PAD src0_sel:DWORD src1_sel:WORD_1\n"
  "v_lshlrev_b32_sdwa v213, 1, v213 dst_sel:DWORD dst_unused:UNUSED_PAD src0_sel:DWORD src1_sel:WORD_1\n"
  "v_mfma_f32_16x16x32_bf16 v[172:175], v[100:103], v[192:195], v[172:175]\n"
  "v_cvt_pk_bf16_f32 v152, v152, v153\n"
  "v_cvt_pk_bf16_f32 v153, v154, v155\n"
  "v_cvt_pk_bf16_f32 v154, v156, v157\n"
  "v_cvt_pk_bf16_f32 v155, v158, v159\n"
  "v_mfma_f32_16x16x32_bf16 v[176:179], v[104:107], v[192:195], v[176:179]\n"
  "s_setprio 0\n"
  "ds_write_b128 v249, v[152:155] offset:64\n"
  "v_mfma_f32_16x16x32_bf16 v[180:183], v[108:111], v[192:195], v[180:183]\n"
  "s_setprio 2\n"
  "ds_read_u16 v212, v212\n"
  "v_lshlrev_b32_sdwa v214, 1, v214 dst_sel:DWORD dst_unused:UNUSED_PAD src0_sel:DWORD src1_sel:WORD_1\n"
  "v_mfma_f32_16x16x32_bf16 v[184:187], v[112:115], v[192:195], v[184:187]\n"
  "s_waitcnt lgkmcnt(7)\n"
  "v_cvt_pk_u16_u32 v201, v202, v203\n"
  "v_cvt_pk_u16_u32 v202, v204, v205\n"
  "v_cvt_pk_u16_u32 v203, v206, v207\n"
  "v_mfma_f32_16x16x32_bf16 v[188:191], v[116:119], v[192:195], v[188:191]\n"
  "ds_read_u16 v213, v213\n"
  "v_lshlrev_b32_sdwa v215, 1, v215 dst_sel:DWORD dst_unused:UNUSED_PAD src0_sel:DWORD src1_sel:WORD_1\n"
  "v_mfma_f32_16x16x32_bf16 v[192:195], v[48:51], v[224:227], 0\n"
  "ds_read_b128 v[236:239], v248 offset:9216\n"
  "v_cvt_pk_bf16_f32 v160, v160, v161\n"
  "v_cvt_pk_bf16_f32 v161, v162, v163\n"
  "v_cvt_pk_bf16_f32 v162, v164, v165\n"
  "v_cvt_pk_bf16_f32 v163, v166, v167\n"
  "v_mfma_f32_16x16x32_bf16 v[196:199], v[60:63], v[224:227], 0\n"
  "ds_write_b128 v249, v[160:163] offset:128\n"
  "v_mfma_f32_16x16x32_bf16 v[192:195], v[52:55], v[228:231], v[192:195]\n"
  "ds_read_u16 v214, v214\n"
  "v_mfma_f32_16x16x32_bf16 v[196:199], v[64:67], v[228:231], v[196:199]\n"
  "s_setprio 0\n"
  "s_nop 1\n"
  "v_mfma_f32_16x16x32_bf16 v[192:195], v[56:59], v[232:235], v[192:195]\n"
  "s_setprio 2\n"
  "s_nop 3\n"
  "v_mfma_f32_16x16x32_bf16 v[196:199], v[68:71], v[232:235], v[196:199]\n"
  "v_lshlrev_b32_sdwa v216, 1, v216 dst_sel:DWORD dst_unused:UNUSED_PAD src0_sel:DWORD src1_sel:WORD_1\n"
  "v_lshlrev_b32_sdwa v217, 1, v217 dst_sel:DWORD dst_unused:UNUSED_PAD src0_sel:DWORD src1_sel:WORD_1\n"
  "v_mfma_f32_16x16x32_bf16 v[168:171], v[120:123], v[200:203], v[168:171]\n"
  "ds_read_b128 v[240:243], v248 offset:10240\n"
  "v_lshlrev_b32_sdwa v218, 1, v218 dst_sel:DWORD dst_unused:UNUSED_PAD src0_sel:DWORD src1_sel:WORD_1\n"
  "v_lshlrev_b32_sdwa v219, 1, v219 dst_sel:DWORD dst_unused:UNUSED_PAD src0_sel:DWORD src1_sel:WORD_1\n"
  "v_mfma_f32_16x16x32_bf16 v[172:175], v[124:127], v[200:203], v[172:175]\n"
  "ds_read_b128 v[244:247], v248 offset:11264\n"
  "v_lshlrev_b32_sdwa v220, 1, v220 dst_sel:DWORD dst_unused:UNUSED_PAD src0_sel:DWORD src1_sel:WORD_1\n"
  "v_mfma_f32_16x16x32_bf16 v[176:179], v[128:131], v[200:203], v[176:179]\n"
  "s_setprio 0\n"
  "ds_read_u16 v215, v215\n"
  "v_lshlrev_b32_sdwa v221, 1, v221 dst_sel:DWORD dst_unused:UNUSED_PAD src0_sel:DWORD src1_sel:WORD_1\n"
  "v_mfma_f32_16x16x32_bf16 v[180:183], v[132:135], v[200:203], v[180:183]\n"
  "s_setprio 2\n"
  "ds_read_u16 v216, v216\n"
  "v_lshlrev_b32_sdwa v222, 1, v222 dst_sel:DWORD dst_unused:UNUSED_PAD src0_sel:DWORD src1_sel:WORD_1\n"
  "v_mfma_f32_16x16x32_bf16 v[184:187], v[136:139], v[200:203], v[184:187]\n"
  "s_waitcnt lgkmcnt(1)\n"
  "v_cvt_pk_u16_u32 v208, v208, v209\n"
  "v_cvt_pk_u16_u32 v209, v210, v211\n"
  "v_cvt_pk_u16_u32 v210, v212, v213\n"
  "v_cvt_pk_u16_u32 v211, v214, v215\n"
  "v_mfma_f32_16x16x32_bf16 v[188:191], v[140:143], v[200:203], v[188:191]\n"
  "ds_read_u16 v217, v217\n"
  "v_lshlrev_b32_sdwa v223, 1, v223 dst_sel:DWORD dst_unused:UNUSED_PAD src0_sel:DWORD src1_sel:WORD_1\n"
  "v_mfma_f32_16x16x32_bf16 v[200:203], v[0:3], v[236:239], 0\n"
  "ds_read_u16 v218, v218\n"
  "v_lshlrev_b32_sdwa v194, 1, v194 dst_sel:DWORD dst_unused:UNUSED_PAD src0_sel:DWORD src1_sel:WORD_1\n"
  "v_lshlrev_b32_sdwa v195, 1, v195 dst_sel:DWORD dst_unused:UNUSED_PAD src0_sel:DWORD src1_sel:WORD_1\n"
  "v_fma_f32 v251, -v192, v192, s46\n"
  "v_mfma_f32_16x16x32_bf16 v[204:207], v[12:15], v[236:239], 0\n"
  "s_add_u32 s23, s23, 512\n"
  "ds_read_u16 v219, v219\n"
  "v_fma_f32 v252, -v193, v193, s46\n"
  "v_lshlrev_b32_sdwa v196, 1, v196 dst_sel:DWORD dst_unused:UNUSED_PAD src0_sel:DWORD src1_sel:WORD_1\n"
  "v_mul_f32 v251, v192, v251\n"
  "v_mfma_f32_16x16x32_bf16 v[200:203], v[4:7], v[240:243], v[200:203]\n"
  "ds_read_u16 v220, v220\n"
  "v_mul_f32 v252, v193, v252\n"
  "v_lshlrev_b32_sdwa v197, 1, v197 dst_sel:DWORD dst_unused:UNUSED_PAD src0_sel:DWORD src1_sel:WORD_1\n"
  "v_exp_f32 v251, v251\n"
  "v_mfma_f32_16x16x32_bf16 v[204:207], v[16:19], v[240:243], v[204:207]\n"
  "s_setprio 0\n"
  "ds_read_u16 v221, v221\n"
  "ds_read_u16 v222, v222\n"
  "v_mfma_f32_16x16x32_bf16 v[200:203], v[8:11], v[244:247], v[200:203]\n"
  "s_setprio 2\n"
  "ds_read_u16 v223, v223\n"
  "v_exp_f32 v252, v252\n"
  "v_lshlrev_b32_sdwa v198, 1, v198 dst_sel:DWORD dst_unused:UNUSED_PAD src0_sel:DWORD src1_sel:WORD_1\n"
  "v_add_u32 v248, s54, v248\n"
  "v_mfma_f32_16x16x32_bf16 v[204:207], v[20:23], v[244:247], v[204:207]\n"
  "s_sub_u32 s54, 0, s54\n"
  "ds_read_addtid_b32 v254 offset:62464\n"
  "ds_read_u16 v194, v194\n"
  "ds_read_u16 v195, v195\n"
  "v_mfma_f32_16x16x32_bf16 v[144:147], v[72:75], v[208:211], 0\n"
  "ds_read_u16 v196, v196\n"
  "v_add_f32 v251, 1.0, v251\n"
  "v_add_f32 v252, 1.0, v252\n"
  "v_lshlrev_b32_sdwa v199, 1, v199 dst_sel:DWORD dst_unused:UNUSED_PAD src0_sel:DWORD src1_sel:WORD_1\n"
  "v_rcp_f32 v251, v251\n"
  "v_mfma_f32_16x16x32_bf16 v[148:151], v[76:79], v[208:211], 0\n"
  "ds_read_u16 v197, v197\n"
  "v_rcp_f32 v252, v252\n"
  "v_mfma_f32_16x16x32_bf16 v[152:155], v[80:83], v[208:211], 0\n"
  "s_sub_u32 s55, s55, 1\n"
  "s_setprio 0\n"
  "ds_read_u16 v198, v198\n"
  "ds_read_u16 v199, v199\n"
  "v_mfma_f32_16x16x32_bf16 v[156:159], v[84:87], v[208:211], 0\n"
  "s_setprio 2\n"
  "v_cvt_pk_bf16_f32 v168, v168, v169\n"
  "v_cvt_pk_bf16_f32 v169, v170, v171\n"
  "v_cvt_pk_bf16_f32 v170, v172, v173\n"
  "v_cvt_pk_bf16_f32 v171, v174, v175\n"
  "v_mfma_f32_16x16x32_bf16 v[160:163], v[88:91], v[208:211], 0\n"
  "s_waitcnt lgkmcnt(6)\n"
  "v_readfirstlane_b32 s22, v254\n"
  "v_cvt_pk_u16_u32 v216, v216, v217\n"
  "v_cvt_pk_u16_u32 v217, v218, v219\n"
  "v_cvt_pk_u16_u32 v218, v220, v221\n"
  "v_cvt_pk_u16_u32 v219, v222, v223\n"
  "v_mfma_f32_16x16x32_bf16 v[164:167], v[92:95], v[208:211], 0\n"
  "ds_write_b128 v249, v[168:171] offset:3072\n"
  "v_mul_f32 v192, v192, v251\n"
  "v_mul_f32 v193, v193, v252\n"
  "v_cvt_pk_bf16_f32 v192, v192, v193\n"
  "s_cbranch_scc0 G_ev_%=\n"
  "v_mfma_f32_16x16x32_bf16 v[208:211], v[24:27], v[236:239], 0\n"
  "s_cmp_ge_u32 s22, s23\n"
  "s_cbranch_scc0 Ge_slow_%=\n"
  "Ge_back_%=:\n"
  "v_mfma_f32_16x16x32_bf16 v[212:215], v[36:39], v[236:239], 0\n"
  "v_lshlrev_b32_sdwa v200, 1, v200 dst_sel:DWORD dst_unused:UNUSED_PAD src0_sel:DWORD src1_sel:WORD_1\n"
  "v_lshlrev_b32_sdwa v201, 1, v201 dst_sel:DWORD dst_unused:UNUSED_PAD src0_sel:DWORD src1_sel:WORD_1\n"
  "v_lshlrev_b32_sdwa v202, 1, v202 dst_sel:DWORD dst_unused:UNUSED_PAD src0_sel:DWORD src1_sel:WORD_1\n"
  "v_mfma_f32_16x16x32_bf16 v[208:211], v[28:31], v[240:243], v[208:211]\n"
  "ds_read_u16 v200, v200\n"
  "ds_read_u16 v201, v201\n"
  "ds_read_u16 v202, v202\n"
  "v_mfma_f32_16x16x32_bf16 v[212:215], v[40:43], v[240:243], v[212:215]\n"
  "v_mfma_f32_16x16x32_bf16 v[208:211], v[32:35], v[244:247], v[208:211]\n"
  "s_setprio 2\n"
  "v_lshlrev_b32_sdwa v203, 1, v203 dst_sel:DWORD dst_unused:UNUSED_PAD src0_sel:DWORD src1_sel:WORD_1\n"
  "v_mfma_f32_16x16x32_bf16 v[212:215], v[44:47], v[244:247], v[212:215]\n"
  "ds_read_u16 v203, v203\n"
  "v_mfma_f32_16x16x32_bf16 v[144:147], v[96:99], v[216:219], v[144:147]\n"
  "v_lshlrev_b32_sdwa v204, 1, v204 dst_sel:DWORD dst_unused:UNUSED_PAD src0_sel:DWORD src1_sel:WORD_1\n"
  "v_lshlrev_b32_sdwa v205, 1, v205 dst_sel:DWORD dst_unused:UNUSED_PAD src0_sel:DWORD src1_sel:WORD_1\n"
  "v_mfma_f32_16x16x32_bf16 v[148:151], v[100:103], v[216:219], v[148:151]\n"
  "ds_read_u16 v204, v204\n"
  "v_cvt_pk_bf16_f32 v176, v176, v177\n"
  "v_cvt_pk_bf16_f32 v177, v178, v179\n"
  "v_cvt_pk_bf16_f32 v178, v180, v181\n"
  "v_cvt_pk_bf16_f32 v179, v182, v183\n"
  "v_mfma_f32_16x16x32_bf16 v[152:155], v[104:107], v[216:219], v[152:155]\n"
  "s_setprio 0\n"
  "ds_write_b128 v249, v[176:179] offset:3136\n"
  "v_mfma_f32_16x16x32_bf16 v[156:159], v[108:111], v[216:219], v[156:159]\n"
  "s_setprio 2\n"
  "ds_read_u16 v205, v205\n"
  "v_mfma_f32_16x16x32_bf16 v[160:163], v[112:115], v[216:219], v[160:163]\n"
  "s_waitcnt lgkmcnt(8)\n"
  "v_cvt_pk_u16_u32 v193, v194, v195\n"
  "v_cvt_pk_u16_u32 v194, v196, v197\n"
  "v_cvt_pk_u16_u32 v195, v198, v199\n"
  "v_mfma_f32_16x16x32_bf16 v[164:167], v[116:119], v[216:219], v[164:167]\n"
  "v_lshlrev_b32_sdwa v206, 1, v206 dst_sel:DWORD dst_unused:UNUSED_PAD src0_sel:DWORD src1_sel:WORD_1\n"
  "v_lshlrev_b32_sdwa v207, 1, v207 dst_sel:DWORD dst_unused:UNUSED_PAD src0_sel:DWORD src1_sel:WORD_1\n"
  "v_mfma_f32_16x16x32_bf16 v[216:219], v[48:51], v[236:239], 0\n"
  "ds_read_u16 v206, v206\n"
  "v_cvt_pk_bf16_f32 v184, v184, v185\n"
  "v_cvt_pk_bf16_f32 v185, v186, v187\n"
  "v_cvt_pk_bf16_f32 v186, v188, v189\n"
  "v_cvt_pk_bf16_f32 v187, v190, v191\n"
  "v_mfma_f32_16x16x32_bf16 v[220:223], v[60:63], v[236:239], 0\n"
  "ds_write_b128 v249, v[184:187] offset:3200\n"
  "v_mfma_f32_16x16x32_bf16 v[216:219], v[52:55], v[240:243], v[216:219]\n"
  "ds_read_u16 v207, v207\n"
  "v_mfma_f32_16x16x32_bf16 v[220:223], v[64:67], v[240:243], v[220:223]\n"
  "s_setprio 0\n"
  "ds_append v254 offset:62464\n"
  "v_mfma_f32_16x16x32_bf16 v[216:219], v[56:59], v[244:247], v[216:219]\n"
  "s_setprio 2\n"
  "v_lshlrev_b32_sdwa v208, 1, v208 dst_sel:DWORD dst_unused:UNUSED_PAD src0_sel:DWORD src1_sel:WORD_1\n"
  "v_mfma_f32_16x16x32_bf16 v[220:223], v[68:71], v[244:247], v[220:223]\n"
  "ds_read_u16 v208, v208\n"
  "v_lshlrev_b32_sdwa v209, 1, v209 dst_sel:DWORD dst_unused:UNUSED_PAD src0_sel:DWORD src1_sel:WORD_1\n"
  "v_mfma_f32_16x16x32_bf16 v[144:147], v[120:123], v[192:195], v[144:147]\n"
  "ds_read_u16 v209, v209\n"
  "v_lshlrev_b32_sdwa v210, 1, v210 dst_sel:DWORD dst_unused:UNUSED_PAD src0_sel:DWORD src1_sel:WORD_1\n"
  "v_lshlrev_b32_sdwa v211, 1, v211 dst_sel:DWORD dst_unused:UNUSED_PAD src0_sel:DWORD src1_sel:WORD_1\n"
  "v_mfma_f32_16x16x32_bf16 v[148:151], v[124:127], v[192:195], v[148:151]\n"
  "ds_read_u16 v210, v210\n"
  "v_lshlrev_b32_sdwa v212, 1, v212 dst_sel:DWORD dst_unused:UNUSED_PAD src0_sel:DWORD src1_sel:WORD_1\n"
  "v_mfma_f32_16x16x32_bf16 v[152:155], v[128:131], v[192:195], v[152:155]\n"
  "s_setprio 0\n"
  "ds_read_u16 v211, v211\n"
  "ds_read_u16 v212, v212\n"
  "v_mfma_f32_16x16x32_bf16 v[156:159], v[132:135], v[192:195], v[156:159]\n"
  "s_setprio 2\n"
  "v_lshlrev_b32_sdwa v213, 1, v213 dst_sel:DWORD dst_unused:UNUSED_PAD src0_sel:DWORD src1_sel:WORD_1\n"
  "v_mfma_f32_16x16x32_bf16 v[160:163], v[136:139], v[192:195], v[160:163]\n"
  "s_waitcnt lgkmcnt(6)\n"
  "v_cvt_pk_u16_u32 v200, v200, v201\n"
  "v_cvt_pk_u16_u32 v201, v202, v203\n"
  "v_cvt_pk_u16_u32 v202, v204, v205\n"
  "v_cvt_pk_u16_u32 v203, v206, v207\n"
  "v_mfma_f32_16x16x32_bf16 v[164:167], v[140:143], v[192:195], v[164:167]\n"
  "ds_read_u16 v213, v213\n"
  "v_lshlrev_b32_sdwa v214, 1, v214 dst_sel:DWORD dst_unused:UNUSED_PAD src0_sel:DWORD src1_sel:WORD_1\n"
  "v_lshlrev_b32_sdwa v215, 1, v215 dst_sel:DWORD dst_unused:UNUSED_PAD src0_sel:DWORD src1_sel:WORD_1\n"
  "v_mfma_f32_16x16x32_bf16 v[168:171], v[72:75], v[200:203], 0\n"
  "ds_read_u16 v214, v214\n"
  "ds_read_u16 v215, v215\n"
  "v_lshlrev_b32_sdwa v218, 1, v218 dst_sel:DWORD dst_unused:UNUSED_PAD src0_sel:DWORD src1_sel:WORD_1\n"
  "v_lshlrev_b32_sdwa v219, 1, v219 dst_sel:DWORD dst_unused:UNUSED_PAD src0_sel:DWORD src1_sel:WORD_1\n"
  "v_fma_f32 v251, -v216, v216, s46\n"
  "v_fma_f32 v252, -v217, v217, s46\n"
  "v_lshlrev_b32_sdwa v220, 1, v220 dst_sel:DWORD dst_unused:UNUSED_PAD src0_sel:DWORD src1_sel:WORD_1\n"
  "v_mfma_f32_16x16x32_bf16 v[172:175], v[76:79], v[200:203], 0\n"
  "ds_read_u16 v218, v218\n"
  "ds_read_u16 v219, v219\n"
  "v_mul_f32 v251, v216, v251\n"
  "v_mul_f32 v252, v217, v252\n"
  "v_lshlrev_b32_sdwa v221, 1, v221 dst_sel:DWORD dst_unused:UNUSED_PAD src0_sel:DWORD src1_sel:WORD_1\n"
  "v_exp_f32 v251, v251\n"
  "v_mfma_f32_16x16x32_bf16 v[176:179], v[80:83], v[200:203], 0\n"
  "ds_read_u16 v220, v220\n"
  "ds_read_u16 v221, v221\n"
  "v_exp_f32 v252, v252\n"
  "v_lshlrev_b32_sdwa v222, 1, v222 dst_sel:DWORD dst_unused:UNUSED_PAD src0_sel:DWORD src1_sel:WORD_1\n"
  "v_add_f32 v251, 1.0, v251\n"
  "v_add_f32 v252, 1.0, v252\n"
  "v_mfma_f32_16x16x32_bf16 v[180:183], v[84:87], v[200:203], 0\n"
  "ds_read_u16 v222, v222\n"
  "v_lshlrev_b32_sdwa v223, 1, v223 dst_sel:DWORD dst_unused:UNUSED_PAD src0_sel:DWORD src1_sel:WORD_1\n"
  "v_rcp_f32 v251, v251\n"
  "v_rcp_f32 v252, v252\n"
  "v_mul_f32 v216, v216, v251\n"
  "v_mfma_f32_16x16x32_bf16 v[184:187], v[88:91], v[200:203], 0\n"
  "ds_read_u16 v223, v223\n"
  "v_cvt_pk_bf16_f32 v144, v144, v145\n"
  "v_cvt_pk_bf16_f32 v145, v146, v147\n"
  "v_cvt_pk_bf16_f32 v146, v148, v149\n"
  "v_cvt_pk_bf16_f32 v147, v150, v151\n"
  "v_mul_f32 v217, v217, v252\n"
  "v_mfma_f32_16x16x32_bf16 v[188:191], v[92:95], v[200:203], 0\n"
  "s_waitcnt lgkmcnt(6)\n"
  "ds_write_b128 v249, v[144:147] offset:24576\n"
  "v_cvt_pk_u16_u32 v208, v208, v209\n"
  "v_cvt_pk_u16_u32 v209, v210, v211\n"
  "v_cvt_pk_u16_u32 v210, v212, v213\n"
  "v_cvt_pk_u16_u32 v211, v214, v215\n"
  "v_cvt_pk_bf16_f32 v216, v216, v217\n"
  "s_nop 0\n"
  "v_mfma_f32_16x16x32_bf16 v[168:171], v[96:99], v[208:211], v[168:171]\n"
  "v_cvt_pk_bf16_f32 v152, v152, v153\n"
  "v_cvt_pk_bf16_f32 v153, v154, v155\n"
  "v_cvt_pk_bf16_f32 v154, v156, v157\n"
  "v_cvt_pk_bf16_f32 v155, v158, v159\n"
  "v_mfma_f32_16x16x32_bf16 v[172:175], v[100:103], v[208:211], v[172:175]\n"
  "ds_write_b128 v249, v[152:155] offset:24640\n"
  "v_mfma_f32_16x16x32_bf16 v[176:179], v[104:107], v[208:211], v[176:179]\n"
  "v_mfma_f32_16x16x32_bf16 v[180:183], v[108:111], v[208:211], v[180:183]\n"
  "v_cvt_pk_bf16_f32 v160, v160, v161\n"
  "v_cvt_pk_bf16_f32 v161, v162, v163\n"
  "v_cvt_pk_bf16_f32 v162, v164, v165\n"
  "v_cvt_pk_bf16_f32 v163, v166, v167\n"
  "v_mfma_f32_16x16x32_bf16 v[184:187], v[112:115], v[208:211], v[184:187]\n"
  "ds_write_b128 v249, v[160:163] offset:24704\n"
  "v_mfma_f32_16x16x32_bf16 v[188:191], v[116:119], v[208:211], v[188:191]\n"
  "s_waitcnt lgkmcnt(3)\n"
  "v_cvt_pk_u16_u32 v217, v218, v219\n"
  "v_cvt_pk_u16_u32 v218, v220, v221\n"
  "v_cvt_pk_u16_u32 v219, v222, v223\n"
  "s_nop 1\n"
  "v_mfma_f32_16x16x32_bf16 v[168:171], v[120:123], v[216:219], v[168:171]\n"
  "v_mfma_f32_16x16x32_bf16 v[172:175], v[124:127], v[216:219], v[172:175]\n"
  "v_mfma_f32_16x16x32_bf16 v[176:179], v[128:131], v[216:219], v[176:179]\n"
  "v_mfma_f32_16x16x32_bf16 v[180:183], v[132:135], v[216:219], v[180:183]\n"
  "s_setprio 0\n"
  "v_mfma_f32_16x16x32_bf16 v[184:187], v[136:139], v[216:219], v[184:187]\n"
  "v_mfma_f32_16x16x32_bf16 v[188:191], v[140:143], v[216:219], v[188:191]\n"
  "s_nop 1\n"
  "v_cvt_pk_bf16_f32 v168, v168, v169\n"
  "v_cvt_pk_bf16_f32 v169, v170, v171\n"
  "v_cvt_pk_bf16_f32 v170, v172, v173\n"
  "v_cvt_pk_bf16_f32 v171, v174, v175\n"
  "ds_write_b128 v249, v[168:171] offset:27648\n"
  "v_cvt_pk_bf16_f32 v176, v176, v177\n"
  "v_cvt_pk_bf16_f32 v177, v178, v179\n"
  "v_cvt_pk_bf16_f32 v178, v180, v181\n"
  "v_cvt_pk_bf16_f32 v179, v182, v183\n"
  "ds_write_b128 v249, v[176:179] offset:27712\n"
  "v_cvt_pk_bf16_f32 v184, v184, v185\n"
  "v_cvt_pk_bf16_f32 v185, v186, v187\n"
  "v_cvt_pk_bf16_f32 v186, v188, v189\n"
  "v_cvt_pk_bf16_f32 v187, v190, v191\n"
  "ds_write_b128 v249, v[184:187] offset:27776\n"
  "s_waitcnt lgkmcnt(0)\n"
  "s_barrier\n"
  "s_endpgm\n"
  "Ga_slow_%=:\n"
  "s_mov_b32 s48, 0x40000\n"
  "Ga_spin_%=:\n"
  "s_sleep 0\n"
  "ds_read_addtid_b32 v254 offset:62464\n"
  "s_waitcnt lgkmcnt(0)\n"
  "v_readfirstlane_b32 s22, v254\n"
  "s_nop 3\n"
  "s_cmp_ge_u32 s22, s23\n"
  "s_cbranch_scc1 Ga_back_%=\n"
  "s_sub_u32 s48, s48, 1\n"
  "s_cbranch_scc0 Ga_spin_%=\n"
  "s_branch Ga_back_%=\n"
  "Gb_slow_%=:\n"
  "s_mov_b32 s48, 0x40000\n"
  "Gb_spin_%=:\n"
  "s_sleep 0\n"
  "ds_read_addtid_b32 v254 offset:62464\n"
  "s_waitcnt lgkmcnt(0)\n"
  "v_readfirstlane_b32 s22, v254\n"
  "s_nop 3\n"
  "s_cmp_ge_u32 s22, s23\n"
  "s_cbranch_scc1 Gb_back_%=\n"
  "s_sub_u32 s48, s48, 1\n"
  "s_cbranch_scc0 Gb_spin_%=\n"
  "s_branch Gb_back_%=\n"
  "Ge_slow_%=:\n"
  "s_mov_b32 s48, 0x40000\n"
  "Ge_spin_%=:\n"
  "s_sleep 0\n"
  "ds_read_addtid_b32 v254 offset:62464\n"
  "s_waitcnt lgkmcnt(0)\n"
  "v_readfirstlane_b32 s22, v254\n"
  "s_nop 3\n"
  "s_cmp_ge_u32 s22, s23\n"
  "s_cbranch_scc1 Ge_back_%=\n"
  "s_sub_u32 s48, s48, 1\n"
  "s_cbranch_scc0 Ge_spin_%=\n"
  "s_branch Ge_back_%=\n"
  "H_cont_%=:\n"
  "s_nop 0\n"
  "s_add_u32 m0, s64, 33216\n"
  "s_nop 0\n"
  "buffer_load_dwordx4 v238, s[24:27], s73 offen lds\n"
  "s_cmp_eq_u32 s56, 3\ns_cselect_b32 s59, -1, 0\ns_mov_b32 s58, 0\n"
  "v_mov_b32 v237, 61440\n"
  "v_cndmask_b32_e64 v237, v236, v237, s[58:59]\n"
  "buffer_load_dwordx4 v239, s[24:27], s73 offen offset:1040 lds\n"
  "buffer_load_dwordx4 v240, s[24:27], s73 offen offset:2080 lds\n"
  "buffer_load_dwordx4 v241, s[24:27], s73 offen offset:3120 lds\n"
  "buffer_load_dwordx2 v[196:197], v247, s[28:31], s79 offen nt\n"
  "buffer_load_dwordx2 v[198:199], v247, s[28:31], s79 offen offset:512 nt\n"
  "buffer_load_dwordx2 v[200:201], v247, s[28:31], s79 offen offset:1024 nt\n"
  "ds_read_b64 v[144:145], v237\n"
  "ds_read_b64 v[146:147], v237 offset:208\n"
  "ds_read_b64 v[148:149], v237 offset:416\n"
  "ds_read_b64 v[150:151], v237 offset:624\n"
  "ds_read_b64 v[152:153], v237 offset:832\n"
  "ds_read_b64 v[154:155], v237 offset:8\n"
  "ds_read_b64 v[156:157], v237 offset:216\n"
  "ds_read_b64 v[158:159], v237 offset:424\n"
  "ds_read_b64 v[160:161], v237 offset:632\n"
  "ds_read_b64 v[162:163], v237 offset:840\n"
  "ds_read_b64 v[164:165], v237 offset:16\n"
  "ds_read_b64 v[166:167], v237 offset:224\n"
  "ds_read_b64 v[168:169], v237 offset:432\n"
  "ds_read_b64 v[170:171], v237 offset:640\n"
  "ds_read_b64 v[172:173], v237 offset:848\n"
  "s_waitcnt lgkmcnt(2)\n"
  "v_smfmac_f32_16x16x64_bf16 v[120:123], v[4:7], v[144:151], v234\n"
  "v_smfmac_f32_16x16x64_bf16 v[124:127], v[16:19], v[154:161], v234\n"
  "s_waitcnt lgkmcnt(0)\n"
  "v_smfmac_f32_16x16x64_bf16 v[128:131], v[28:31], v[164:171], v234\n"
  "s_nop 2\n"
  "v_mfma_f32_16x16x16_bf16 v[120:123], v[10:11], v[152:153], v[120:123]\n"
  "s_cmp_eq_u32 s56, 3\ns_cbranch_scc0 Hs6_%=\nds_read_b64 v[144:145], v255 offset:0\nds_read_b64 v[146:147], v255 offset:208\nds_read_b64 v[148:149], v255 offset:416\nds_read_b64 v[150:151], v255 offset:624\nds_read_b64 v[152:153], v255 offset:832\nHs6_%=:\n"
  "v_mfma_f32_16x16x16_bf16 v[124:127], v[22:23], v[162:163], v[124:127]\n"
  "s_cmp_eq_u32 s56, 3\ns_cbranch_scc0 Hs8_%=\nds_read_b64 v[154:155], v255 offset:8\nds_read_b64 v[156:157], v255 offset:216\nds_read_b64 v[158:159], v255 offset:424\nds_read_b64 v[160:161], v255 offset:632\nds_read_b64 v[162:163], v255 offset:840\nHs8_%=:\n"
  "v_mfma_f32_16x16x16_bf16 v[128:131], v[34:35], v[172:173], v[128:131]\n"
  "s_nop 3\n"
  "v_mul_f32 v223, v120, v120\n"
  "v_fmac_f32 v223, v121, v121\n"
  "v_add_f32 v222, v120, v121\n"
  "v_fmac_f32 v223, v122, v122\n"
  "v_add_f32 v222, v222, v122\n"
  "v_fmac_f32 v223, v123, v123\n"
  "v_add_f32 v222, v222, v123\n"
  "v_fmac_f32 v223, v124, v124\n"
  "v_add_f32 v222, v222, v124\n"
  "s_cmp_eq_u32 s56, 3\ns_cbranch_scc0 Hs5_%=\ns_waitcnt lgkmcnt(0)\nHs5_%=:\n"
  "s_cmp_eq_u32 s56, 3\ns_cbranch_scc0 Hs7_%=\ns_waitcnt lgkmcnt(0)\nHs7_%=:\n"
  "v_mfma_f32_16x16x16_bf16 v[72:75], v[8:9], v[152:153], 0\n"
  "v_fmac_f32 v223, v125, v125\n"
  "v_add_f32 v222, v222, v125\n"
  "v_fmac_f32 v223, v126, v126\n"
  "v_mfma_f32_16x16x16_bf16 v[76:79], v[20:21], v[162:163], 0\n"
  "v_add_f32 v222, v222, v126\n"
  "v_fmac_f32 v223, v127, v127\n"
  "v_add_f32 v222, v222, v127\n"
  "v_smfmac_f32_16x16x64_bf16 v[72:75], v[0:3], v[144:151], v234\n"
  "ds_read_b64 v[144:145], v237 offset:24\n"
  "ds_read_b64 v[146:147], v237 offset:232\n"
  "ds_read_b64 v[148:149], v237 offset:440\n"
  "ds_read_b64 v[150:151], v237 offset:648\n"
  "ds_read_b64 v[152:153], v237 offset:856\n"
  "s_cmp_eq_u32 s56, 3\ns_cbranch_scc0 Hs10_%=\nds_read_b64 v[164:165], v255 offset:16\nds_read_b64 v[166:167], v255 offset:224\nds_read_b64 v[168:169], v255 offset:432\nds_read_b64 v[170:171], v255 offset:640\nds_read_b64 v[172:173], v255 offset:848\nHs10_%=:\n"
  "ds_read_b64 v[162:163], v237 offset:864\n"
  "v_smfmac_f32_16x16x64_bf16 v[76:79], v[12:15], v[154:161], v234\n"
  "v_fmac_f32 v223, v128, v128\n"
  "v_add_f32 v222, v222, v128\n"
  "v_fmac_f32 v223, v129, v129\n"
  "v_add_f32 v222, v222, v129\n"
  "v_fmac_f32 v223, v130, v130\n"
  "v_add_f32 v222, v222, v130\n"
  "ds_read_b64 v[154:155], v237 offset:32\n"
  "ds_read_b64 v[156:157], v237 offset:240\n"
  "ds_read_b64 v[158:159], v237 offset:448\n"
  "ds_read_b64 v[160:161], v237 offset:656\n"
  "s_cmp_eq_u32 s56, 3\ns_cbranch_scc0 Hs9_%=\ns_waitcnt lgkmcnt(0)\nHs9_%=:\n"
  "v_mfma_f32_16x16x16_bf16 v[80:83], v[32:33], v[172:173], 0\n"
  "v_fmac_f32 v223, v131, v131\n"
  "v_add_f32 v222, v222, v131\n"
  "s_waitcnt vmcnt(9)\n"
  "v_lshlrev_b32 v202, 16, v190\n"
  "v_and_b32 v203, s49, v190\n"
  "v_lshlrev_b32 v204, 16, v191\n"
  "v_and_b32 v205, s49, v191\n"
  "v_smfmac_f32_16x16x64_bf16 v[80:83], v[24:27], v[164:171], v234\n"
  "ds_read_b64 v[164:165], v237 offset:40\n"
  "ds_read_b64 v[166:167], v237 offset:248\n"
  "ds_read_b64 v[168:169], v237 offset:456\n"
  "ds_read_b64 v[170:171], v237 offset:664\n"
  "ds_read_b64 v[172:173], v237 offset:872\n"
  "s_waitcnt lgkmcnt(5)\n"
  "v_smfmac_f32_16x16x64_bf16 v[132:135], v[40:43], v[144:151], v234\n"
  "v_smfmac_f32_16x16x64_bf16 v[136:139], v[52:55], v[154:161], v234\n"
  "s_nop 4\n"
  "v_mfma_f32_16x16x16_bf16 v[132:135], v[46:47], v[152:153], v[132:135]\n"
  "s_cmp_eq_u32 s56, 3\ns_cbranch_scc0 Hs12_%=\nds_read_b64 v[144:145], v255 offset:24\nds_read_b64 v[146:147], v255 offset:232\nds_read_b64 v[148:149], v255 offset:440\nds_read_b64 v[150:151], v255 offset:648\nds_read_b64 v[152:153], v255 offset:856\nHs12_%=:\n"
  "v_mfma_f32_16x16x16_bf16 v[136:139], v[58:59], v[162:163], v[136:139]\n"
  "s_cmp_eq_u32 s56, 3\ns_cbranch_scc0 Hs14_%=\nds_read_b64 v[154:155], v255 offset:32\nds_read_b64 v[156:157], v255 offset:240\nds_read_b64 v[158:159], v255 offset:448\nds_read_b64 v[160:161], v255 offset:656\nds_read_b64 v[162:163], v255 offset:864\nHs14_%=:\n"
  "s_waitcnt lgkmcnt(0)\n"
  "v_smfmac_f32_16x16x64_bf16 v[140:143], v[64:67], v[164:171], v234\n"
  "ds_read_b32 v253, v229\n"
  "s_nop 1\n"
  "v_add_f32 v222, v222, v132\n"
  "v_fmac_f32 v223, v132, v132\n"
  "v_fmac_f32 v223, v133, v133\n"
  "v_add_f32 v222, v222, v133\n"
  "v_fmac_f32 v223, v134, v134\n"
  "v_add_f32 v222, v222, v134\n"
  "v_fmac_f32 v223, v135, v135\n"
  "v_add_f32 v222, v222, v135\n"
  "v_fmac_f32 v223, v136, v136\n"
  "s_cmp_eq_u32 s56, 3\ns_cbranch_scc0 Hs11_%=\ns_waitcnt lgkmcnt(0)\nHs11_%=:\n"
  "s_cmp_eq_u32 s56, 3\ns_cbranch_scc0 Hs13_%=\ns_waitcnt lgkmcnt(0)\nHs13_%=:\n"
  "buffer_load_dwordx2 v[190:191], v247, s[28:31], s80 offen nt\n"
  "s_waitcnt lgkmcnt(0)\n"
  "v_readfirstlane_b32 s22, v253\n"
  "v_add_f32 v222, v222, v136\n"
  "v_fmac_f32 v223, v137, v137\n"
  "v_add_f32 v222, v222, v137\n"
  "v_mfma_f32_16x16x16_bf16 v[140:143], v[70:71], v[172:173], v[140:143]\n"
  "v_fmac_f32 v223, v138, v138\n"
  "v_add_f32 v222, v222, v138\n"
  "v_fmac_f32 v223, v139, v139\n"
  "v_add_f32 v222, v222, v139\n"
  "s_cmp_ge_u32 s22, 768\ns_cbranch_scc0 HB2_slow_%=\nHB2_back_%=:\ns_setprio 1\n"
  "ds_read_b64 v[174:175], v244\n"
  "ds_read_b64 v[176:177], v244 offset:6144\n"
  "ds_read_b64 v[178:179], v244 offset:12288\n"
  "ds_read_b64 v[180:181], v244 offset:18432\n"
  "ds_read_b64 v[182:183], v245\n"
  "ds_read_b64 v[184:185], v245 offset:6144\n"
  "ds_read_b64 v[186:187], v245 offset:12288\n"
  "ds_read_b64 v[188:189], v245 offset:18432\n"
  "s_cmp_eq_u32 s56, 3\ns_cbranch_scc0 Hs16_%=\nds_read_b64 v[164:165], v255 offset:40\nds_read_b64 v[166:167], v255 offset:248\nds_read_b64 v[168:169], v255 offset:456\nds_read_b64 v[170:171], v255 offset:664\nds_read_b64 v[172:173], v255 offset:872\nHs16_%=:\n"
  "v_fmac_f32 v223, v140, v140\n"
  "v_add_f32 v222, v222, v140\n"
  "v_fmac_f32 v223, v141, v141\n"
  "v_add_f32 v222, v222, v141\n"
  "v_fmac_f32 v223, v142, v142\n"
  "v_add_f32 v222, v222, v142\n"
  "v_fmac_f32 v223, v143, v143\n"
  "v_add_f32 v222, v222, v143\n"
  "v_mfma_f32_16x16x16_bf16 v[84:87], v[44:45], v[152:153], 0\n"
  "ds_write_b64 v249, v[222:223]\n"
  "s_cmp_eq_u32 s56, 3\ns_cbranch_scc0 Hs15_%=\ns_waitcnt lgkmcnt(0)\nHs15_%=:\n"
  "s_waitcnt lgkmcnt(1)\n"
  "v_smfmac_f32_16x16x64_bf16 v[202:205], v[230:233], v[174:181], v234\n"
  "s_waitcnt vmcnt(9)\n"
  "v_lshlrev_b32 v206, 16, v192\n"
  "v_mfma_f32_16x16x16_bf16 v[88:91], v[56:57], v[162:163], 0\n"
  "v_and_b32 v207, s49, v192\n"
  "ds_read_b64 v[174:175], v246\n"
  "ds_read_b64 v[176:177], v246 offset:6144\n"
  "ds_read_b64 v[178:179], v246 offset:12288\n"
  "ds_read_b64 v[180:181], v246 offset:18432\n"
  "ds_append v253 offset:62464\n"
  "v_mfma_f32_16x16x16_bf16 v[92:95], v[68:69], v[172:173], 0\n"
  "v_lshlrev_b32 v208, 16, v193\n"
  "v_and_b32 v209, s49, v193\n"
  "s_waitcnt vmcnt(8)\n"
  "v_lshlrev_b32 v210, 16, v194\n"
  "v_and_b32 v211, s49, v194\n"
  "v_lshlrev_b32 v212, 16, v195\n"
  "v_and_b32 v213, s49, v195\n"
  "v_smfmac_f32_16x16x64_bf16 v[84:87], v[36:39], v[144:151], v234\n"
  "buffer_load_dwordx2 v[192:193], v247, s[28:31], s80 offen offset:512 nt\n"
  "v_smfmac_f32_16x16x64_bf16 v[88:91], v[48:51], v[154:161], v234\n"
  "buffer_load_dwordx2 v[194:195], v247, s[28:31], s80 offen offset:1024 nt\n"
  "v_smfmac_f32_16x16x64_bf16 v[92:95], v[60:63], v[164:171], v234\n"
  "ds_read_b32 v253, v229\n"
  "ds_read_b64 v[214:215], v250\n"
  "ds_read_b64 v[216:217], v250 offset:512\n"
  "ds_read_b64 v[218:219], v250 offset:1024\n"
  "ds_read_b64 v[220:221], v250 offset:1536\n"
  "s_waitcnt lgkmcnt(0)\n"
  "v_readfirstlane_b32 s22, v253\n"
  "v_smfmac_f32_16x16x64_bf16 v[206:209], v[230:233], v[182:189], v234\n"
  "s_cmp_ge_u32 s22, 1024\ns_cbranch_scc0 HA3_slow_%=\nHA3_back_%=:\n"
  "v_add_f32 v214, v214, v216\n"
  "v_add_f32 v215, v215, v217\n"
  "v_add_f32 v218, v218, v220\n"
  "v_add_f32 v219, v219, v221\n"
  "v_add_f32 v214, v214, v218\n"
  "v_add_f32 v215, v215, v219\n"
  "v_fmaak_f32 v252, v235, v215, 0x3727c5ac\n"
  "v_mul_f32 v251, v235, v214\n"
  "v_fma_f32 v252, -v251, v251, v252\n"
  "v_max_f32 v252, 0x3727c5ac, v252\n"
  "v_rsq_f32 v252, v252\n"
  "s_nop 0\n"
  "v_mul_f32 v226, s47, v252\n"
  "v_mul_f32_e64 v227, -v251, v226\n"
  "v_fma_f32 v120, v120, v226, v227\n"
  "v_fma_f32 v121, v121, v226, v227\n"
  "v_fma_f32 v122, v122, v226, v227\n"
  "v_fma_f32 v123, v123, v226, v227\n"
  "v_fma_f32 v124, v124, v226, v227\n"
  "v_smfmac_f32_16x16x64_bf16 v[210:213], v[230:233], v[174:181], v234\n"
  "v_fma_f32 v125, v125, v226, v227\n"
  "v_fma_f32 v126, v126, v226, v227\n"
  "v_fma_f32 v127, v127, v226, v227\n"
  "v_cvt_pk_bf16_f32 v202, v202, v203\n"
  "v_cvt_pk_bf16_f32 v203, v204, v205\n"
  "v_cvt_pk_bf16_f32 v214, v120, v121\n"
  "v_cvt_pk_bf16_f32 v215, v122, v123\n"
  "v_cvt_pk_bf16_f32 v216, v124, v125\n"
  "v_cvt_pk_bf16_f32 v217, v126, v127\n"
  "buffer_store_dwordx2 v[202:203], v247, s[36:39], s78 offen sc1\n"
  "ds_write_b128 v248, v[214:217]\n"
  "s_waitcnt vmcnt(6)\n"
  "v_lshlrev_b32 v204, 16, v197\n"
  "v_and_b32 v205, s49, v197\n"
  "v_lshlrev_b32 v202, 16, v196\n"
  "v_and_b32 v203, s49, v196\n"
  "v_fma_f32 v128, v128, v226, v227\n"
  "v_fma_f32 v129, v129, v226, v227\n"
  "v_fma_f32 v130, v130, v226, v227\n"
  "v_fma_f32 v131, v131, v226, v227\n"
  "v_fma_f32 v132, v132, v226, v227\n"
  "v_fma_f32 v133, v133, v226, v227\n"
  "v_fma_f32 v134, v134, v226, v227\n"
  "v_fma_f32 v135, v135, v226, v227\n"
  "v_fma_f32 v136, v136, v226, v227\n"
  "v_fma_f32 v137, v137, v226, v227\n"
  "v_fma_f32 v138, v138, v226, v227\n"
  "v_fma_f32 v139, v139, v226, v227\n"
  "v_fma_f32 v140, v140, v226, v227\n"
  "v_fma_f32 v141, v141, v226, v227\n"
  "v_fma_f32 v142, v142, v226, v227\n"
  "v_fma_f32 v143, v143, v226, v227\n"
  "v_cvt_pk_bf16_f32 v218, v128, v129\n"
  "v_cvt_pk_bf16_f32 v219, v130, v131\n"
  "v_cvt_pk_bf16_f32 v220, v132, v133\n"
  "v_cvt_pk_bf16_f32 v221, v134, v135\n"
  "v_cvt_pk_bf16_f32 v214, v136, v137\n"
  "v_cvt_pk_bf16_f32 v215, v138, v139\n"
  "v_cvt_pk_bf16_f32 v216, v140, v141\n"
  "ds_read_b32 v253, v229\n"
  "ds_write_b128 v248, v[218:221] offset:256\n"
  "v_cvt_pk_bf16_f32 v217, v142, v143\n"
  "v_cvt_pk_bf16_f32 v206, v206, v207\n"
  "v_cvt_pk_bf16_f32 v210, v210, v211\n"
  "v_cvt_pk_bf16_f32 v207, v208, v209\n"
  "v_cvt_pk_bf16_f32 v211, v212, v213\n"
  "s_waitcnt vmcnt(5)\n"
  "v_lshlrev_b32 v208, 16, v199\n"
  "v_and_b32 v209, s49, v199\n"
  "buffer_store_dwordx2 v[206:207], v247, s[36:39], s78 offen offset:512 sc1\n"
  "ds_write_b128 v248, v[214:217] offset:512\n"
  "s_cmp_eq_u32 s57, 6\ns_cbranch_scc0 Hs17_%=\nv_mov_b32 v238, v242\nv_mov_b32 v241, v243\nHs17_%=:\n"
  "s_add_u32 m0, s64, 0\n"
  "s_waitcnt lgkmcnt(1)\n"
  "v_readfirstlane_b32 s22, v253\n"
  "v_lshlrev_b32 v206, 16, v198\n"
  "v_and_b32 v207, s49, v198\n"
  "s_waitcnt vmcnt(5)\n"
  "v_lshlrev_b32 v212, 16, v201\n"
  "v_and_b32 v213, s49, v201\n"
  "s_cmp_ge_u32 s22, 1280\ns_cbranch_scc0 HB3_slow_%=\nHB3_back_%=:\n"
  "buffer_store_dwordx2 v[210:211], v247, s[36:39], s78 offen offset:1024 sc1\n"
  "ds_read_b64 v[182:183], v244 offset:24576\n"
  "ds_read_b64 v[184:185], v244 offset:30720\n"
  "ds_read_b64 v[186:187], v244 offset:36864\n"
  "ds_read_b64 v[188:189], v244 offset:43008\n"
  "ds_read_b64 v[174:175], v245 offset:24576\n"
  "ds_read_b64 v[176:177], v245 offset:30720\n"
  "ds_read_b64 v[178:179], v245 offset:36864\n"
  "ds_read_b64 v[180:181], v245 offset:43008\n"
  "v_lshlrev_b32 v210, 16, v200\n"
  "v_and_b32 v211, s49, v200\n"
  "buffer_load_dwordx4 v238, s[24:27], s74 offen lds\n"
  "s_waitcnt lgkmcnt(0)\n"
  "v_smfmac_f32_16x16x64_bf16 v[202:205], v[230:233], v[182:189], v234\n"
  "ds_read_b64 v[182:183], v246 offset:24576\n"
  "ds_read_b64 v[184:185], v246 offset:30720\n"
  "ds_read_b64 v[186:187], v246 offset:36864\n"
  "ds_read_b64 v[188:189], v246 offset:43008\n"
  "ds_append v253 offset:62464\n"
  "buffer_load_dwordx4 v239, s[24:27], s74 offen offset:1040 lds\n"
  "buffer_load_dwordx4 v240, s[24:27], s74 offen offset:2080 lds\n"
  "buffer_load_dwordx4 v241, s[24:27], s74 offen offset:3120 lds\n"
  "buffer_load_dwordx2 v[196:197], v247, s[28:31], s81 offen nt\n"
  "buffer_load_dwordx2 v[198:199], v247, s[28:31], s81 offen offset:512 nt\n"
  "buffer_load_dwordx2 v[200:201], v247, s[28:31], s81 offen offset:1024 nt\n"
  "ds_read_b64 v[152:153], v236 offset:17440\n"
  "ds_read_b64 v[144:145], v236 offset:16608\n"
  "ds_read_b64 v[146:147], v236 offset:16816\n"
  "ds_read_b64 v[148:149], v236 offset:17024\n"
  "ds_read_b64 v[150:151], v236 offset:17232\n"
  "ds_read_b64 v[162:163], v236 offset:17448\n"
  "ds_read_b64 v[154:155], v236 offset:16616\n"
  "ds_read_b64 v[156:157], v236 offset:16824\n"
  "ds_read_b64 v[158:159], v236 offset:17032\n"
  "ds_read_b64 v[160:161], v236 offset:17240\n"
  "s_waitcnt lgkmcnt(2)\n"
  "v_mfma_f32_16x16x16_bf16 v[96:99], v[8:9], v[152:153], 0\n"
  "ds_read_b64 v[172:173], v236 offset:17456\n"
  "ds_read_b64 v[164:165], v236 offset:16624\n"
  "ds_read_b64 v[166:167], v236 offset:16832\n"
  "v_mfma_f32_16x16x16_bf16 v[100:103], v[20:21], v[162:163], 0\n"
  "ds_read_b64 v[168:169], v236 offset:17040\n"
  "ds_read_b64 v[170:171], v236 offset:17248\n"
  "v_smfmac_f32_16x16x64_bf16 v[72:75], v[4:7], v[144:151], v234\n"
  "v_smfmac_f32_16x16x64_bf16 v[96:99], v[0:3], v[144:151], v234\n"
  "ds_read_b64 v[144:145], v236 offset:16632\n"
  "ds_read_b64 v[146:147], v236 offset:16840\n"
  "ds_read_b64 v[148:149], v236 offset:17048\n"
  "ds_read_b64 v[150:151], v236 offset:17256\n"
  "s_nop 0\n"
  "v_mfma_f32_16x16x16_bf16 v[72:75], v[10:11], v[152:153], v[72:75]\n"
  "ds_read_b64 v[152:153], v236 offset:17464\n"
  "s_waitcnt lgkmcnt(5)\n"
  "v_mfma_f32_16x16x16_bf16 v[104:107], v[32:33], v[172:173], 0\n"
  "v_smfmac_f32_16x16x64_bf16 v[76:79], v[16:19], v[154:161], v234\n"
  "v_smfmac_f32_16x16x64_bf16 v[100:103], v[12:15], v[154:161], v234\n"
  "ds_read_b64 v[154:155], v236 offset:16640\n"
  "ds_read_b64 v[156:157], v236 offset:16848\n"
  "ds_read_b64 v[158:159], v236 offset:17056\n"
  "ds_read_b64 v[160:161], v236 offset:17264\n"
  "v_mul_f32 v223, v72, v72\n"
  "v_add_f32 v222, v72, v73\n"
  "v_fmac_f32 v223, v73, v73\n"
  "v_mfma_f32_16x16x16_bf16 v[76:79], v[22:23], v[162:163], v[76:79]\n"
  "v_fmac_f32 v223, v74, v74\n"
  "v_add_f32 v222, v222, v74\n"
  "v_fmac_f32 v223, v75, v75\n"
  "v_smfmac_f32_16x16x64_bf16 v[80:83], v[28:31], v[164:171], v234\n"
  "ds_read_b64 v[162:163], v236 offset:17472\n"
  "v_add_f32 v222, v222, v75\n"
  "v_smfmac_f32_16x16x64_bf16 v[104:107], v[24:27], v[164:171], v234\n"
  "v_cvt_pk_bf16_f32 v202, v202, v203\n"
  "v_fmac_f32 v223, v76, v76\n"
  "v_add_f32 v222, v222, v76\n"
  "v_fmac_f32 v223, v77, v77\n"
  "v_add_f32 v222, v222, v77\n"
  "ds_read_b64 v[164:165], v236 offset:16648\n"
  "ds_read_b64 v[166:167], v236 offset:16856\n"
  "ds_read_b64 v[168:169], v236 offset:17064\n"
  "ds_read_b64 v[170:171], v236 offset:17272\n"
  "v_mfma_f32_16x16x16_bf16 v[80:83], v[34:35], v[172:173], v[80:83]\n"
  "v_fmac_f32 v223, v78, v78\n"
  "v_add_f32 v222, v222, v78\n"
  "v_fmac_f32 v223, v79, v79\n"
  "v_add_f32 v222, v222, v79\n"
  "v_cvt_pk_bf16_f32 v203, v204, v205\n"
  "s_waitcnt lgkmcnt(4)\n"
  "v_smfmac_f32_16x16x64_bf16 v[84:87], v[40:43], v[144:151], v234\n"
  "ds_read_b64 v[172:173], v236 offset:17480\n"
  "buffer_store_dwordx2 v[202:203], v247, s[36:39], s79 offen sc1\n"
  "v_fmac_f32 v223, v80, v80\n"
  "v_add_f32 v222, v222, v80\n"
  "v_fmac_f32 v223, v81, v81\n"
  "v_add_f32 v222, v222, v81\n"
  "v_mfma_f32_16x16x16_bf16 v[84:87], v[46:47], v[152:153], v[84:87]\n"
  "v_fmac_f32 v223, v82, v82\n"
  "v_add_f32 v222, v222, v82\n"
  "v_fmac_f32 v223, v83, v83\n"
  "v_smfmac_f32_16x16x64_bf16 v[88:91], v[52:55], v[154:161], v234\n"
  "v_add_f32 v222, v222, v83\n"
  "v_smfmac_f32_16x16x64_bf16 v[206:209], v[230:233], v[174:181], v234\n"
  "s_waitcnt lgkmcnt(0)\n"
  "v_smfmac_f32_16x16x64_bf16 v[92:95], v[64:67], v[164:171], v234\n"
  "v_fmac_f32 v223, v84, v84\n"
  "v_add_f32 v222, v222, v84\n"
  "v_fmac_f32 v223, v85, v85\n"
  "v_add_f32 v222, v222, v85\n"
  "v_mfma_f32_16x16x16_bf16 v[88:91], v[58:59], v[162:163], v[88:91]\n"
  "v_fmac_f32 v223, v86, v86\n"
  "v_add_f32 v222, v222, v86\n"
  "v_fmac_f32 v223, v87, v87\n"
  "v_add_f32 v222, v222, v87\n"
  "v_mfma_f32_16x16x16_bf16 v[92:95], v[70:71], v[172:173], v[92:95]\n"
  "ds_read_b32 v253, v229\n"
  "s_nop 1\n"
  "v_fmac_f32 v223, v88, v88\n"
  "v_add_f32 v222, v222, v88\n"
  "v_fmac_f32 v223, v89, v89\n"
  "v_add_f32 v222, v222, v89\n"
  "v_fmac_f32 v223, v90, v90\n"
  "v_add_f32 v222, v222, v90\n"
  "v_fmac_f32 v223, v91, v91\n"
  "v_smfmac_f32_16x16x64_bf16 v[210:213], v[230:233], v[182:189], v234\n"
  "s_waitcnt lgkmcnt(0)\n"
  "v_readfirstlane_b32 s22, v253\n"
  "v_add_f32 v222, v222, v91\n"
  "v_fmac_f32 v223, v92, v92\n"
  "v_add_f32 v222, v222, v92\n"
  "v_fmac_f32 v223, v93, v93\n"
  "v_add_f32 v222, v222, v93\n"
  "v_fmac_f32 v223, v94, v94\n"
  "v_add_f32 v222, v222, v94\n"
  "v_fmac_f32 v223, v95, v95\n"
  "s_cmp_ge_u32 s22, 1792\ns_cbranch_scc0 HB4_slow_%=\nHB4_back_%=:\n"
  "ds_read_b64 v[174:175], v244\n"
  "ds_read_b64 v[176:177], v244 offset:6144\n"
  "ds_read_b64 v[178:179], v244 offset:12288\n"
  "ds_read_b64 v[180:181], v244 offset:18432\n"
  "ds_read_b64 v[182:183], v245\n"
  "ds_read_b64 v[184:185], v245 offset:6144\n"
  "ds_read_b64 v[186:187], v245 offset:12288\n"
  "ds_read_b64 v[188:189], v245 offset:18432\n"
  "s_waitcnt vmcnt(13)\n"
  "v_lshlrev_b32 v204, 16, v191\n"
  "v_and_b32 v205, s49, v191\n"
  "v_lshlrev_b32 v202, 16, v190\n"
  "v_and_b32 v203, s49, v190\n"
  "v_add_f32 v222, v222, v95\n"
  "v_mfma_f32_16x16x16_bf16 v[108:111], v[44:45], v[152:153], 0\n"
  "v_cvt_pk_bf16_f32 v206, v206, v207\n"
  "v_cvt_pk_bf16_f32 v210, v210, v211\n"
  "v_cvt_pk_bf16_f32 v207, v208, v209\n"
  "ds_write_b64 v249, v[222:223]\n"
  "buffer_store_dwordx2 v[206:207], v247, s[36:39], s79 offen offset:512 sc1\n"
  "s_waitcnt lgkmcnt(1)\n"
  "v_smfmac_f32_16x16x64_bf16 v[202:205], v[230:233], v[174:181], v234\n"
  "v_cvt_pk_bf16_f32 v211, v212, v213\n"
  "v_mfma_f32_16x16x16_bf16 v[112:115], v[56:57], v[162:163], 0\n"
  "ds_read_b64 v[174:175], v246\n"
  "ds_read_b64 v[176:177], v246 offset:6144\n"
  "ds_read_b64 v[178:179], v246 offset:12288\n"
  "ds_read_b64 v[180:181], v246 offset:18432\n"
  "ds_append v253 offset:62464\n"
  "buffer_store_dwordx2 v[210:211], v247, s[36:39], s79 offen offset:1024 sc1\n"
  "v_mfma_f32_16x16x16_bf16 v[116:119], v[68:69], v[172:173], 0\n"
  "s_waitcnt vmcnt(14)\n"
  "v_lshlrev_b32 v208, 16, v193\n"
  "v_and_b32 v209, s49, v193\n"
  "v_lshlrev_b32 v206, 16, v192\n"
  "v_and_b32 v207, s49, v192\n"
  "v_smfmac_f32_16x16x64_bf16 v[108:111], v[36:39], v[144:151], v234\n"
  "buffer_load_dwordx2 v[190:191], v247, s[28:31], s82 offen nt\n"
  "s_waitcnt vmcnt(14)\n"
  "v_lshlrev_b32 v212, 16, v195\n"
  "v_and_b32 v213, s49, v195\n"
  "v_lshlrev_b32 v210, 16, v194\n"
  "v_and_b32 v211, s49, v194\n"
  "v_smfmac_f32_16x16x64_bf16 v[112:115], v[48:51], v[154:161], v234\n"
  "buffer_load_dwordx2 v[192:193], v247, s[28:31], s82 offen offset:512 nt\n"
  "v_smfmac_f32_16x16x64_bf16 v[116:119], v[60:63], v[164:171], v234\n"
  "buffer_load_dwordx2 v[194:195], v247, s[28:31], s82 offen offset:1024 nt\n"
  "ds_read_b32 v253, v229\n"
  "ds_read_b64 v[214:215], v250\n"
  "ds_read_b64 v[216:217], v250 offset:512\n"
  "ds_read_b64 v[218:219], v250 offset:1024\n"
  "ds_read_b64 v[220:221], v250 offset:1536\n"
  "s_waitcnt lgkmcnt(0)\n"
  "v_readfirstlane_b32 s22, v253\n"
  "v_smfmac_f32_16x16x64_bf16 v[206:209], v[230:233], v[182:189], v234\n"
  "s_cmp_ge_u32 s22, 2048\ns_cbranch_scc0 HA5_slow_%=\nHA5_back_%=:\n"
  "v_add_f32 v214, v214, v216\n"
  "v_add_f32 v215, v215, v217\n"
  "v_add_f32 v218, v218, v220\n"
  "v_add_f32 v219, v219, v221\n"
  "v_add_f32 v214, v214, v218\n"
  "v_add_f32 v215, v215, v219\n"
  "v_fmaak_f32 v252, v235, v215, 0x3727c5ac\n"
  "v_mul_f32 v251, v235, v214\n"
  "v_fma_f32 v252, -v251, v251, v252\n"
  "v_max_f32 v252, 0x3727c5ac, v252\n"
  "v_rsq_f32 v252, v252\n"
  "s_nop 0\n"
  "v_mul_f32 v226, s47, v252\n"
  "v_mul_f32_e64 v227, -v251, v226\n"
  "v_fma_f32 v72, v72, v226, v227\n"
  "v_fma_f32 v73, v73, v226, v227\n"
  "v_fma_f32 v74, v74, v226, v227\n"
  "v_fma_f32 v75, v75, v226, v227\n"
  "v_fma_f32 v76, v76, v226, v227\n"
  "v_smfmac_f32_16x16x64_bf16 v[210:213], v[230:233], v[174:181], v234\n"
  "v_fma_f32 v77, v77, v226, v227\n"
  "v_fma_f32 v78, v78, v226, v227\n"
  "v_fma_f32 v79, v79, v226, v227\n"
  "v_cvt_pk_bf16_f32 v202, v202, v203\n"
  "v_cvt_pk_bf16_f32 v203, v204, v205\n"
  "v_cvt_pk_bf16_f32 v214, v72, v73\n"
  "v_cvt_pk_bf16_f32 v215, v74, v75\n"
  "v_cvt_pk_bf16_f32 v216, v76, v77\n"
  "v_cvt_pk_bf16_f32 v217, v78, v79\n"
  "buffer_store_dwordx2 v[202:203], v247, s[36:39], s80 offen sc1\n"
  "ds_write_b128 v248, v[214:217] offset:12288\n"
  "s_waitcnt vmcnt(9)\n"
  "v_lshlrev_b32 v204, 16, v197\n"
  "v_and_b32 v205, s49, v197\n"
  "v_lshlrev_b32 v202, 16, v196\n"
  "v_and_b32 v203, s49, v196\n"
  "v_fma_f32 v80, v80, v226, v227\n"
  "v_fma_f32 v81, v81, v226, v227\n"
  "v_fma_f32 v82, v82, v226, v227\n"
  "v_fma_f32 v83, v83, v226, v227\n"
  "v_fma_f32 v84, v84, v226, v227\n"
  "v_fma_f32 v85, v85, v226, v227\n"
  "v_fma_f32 v86, v86, v226, v227\n"
  "v_fma_f32 v87, v87, v226, v227\n"
  "v_fma_f32 v88, v88, v226, v227\n"
  "v_fma_f32 v89, v89, v226, v227\n"
  "v_fma_f32 v90, v90, v226, v227\n"
  "v_fma_f32 v91, v91, v226, v227\n"
  "v_fma_f32 v92, v92, v226, v227\n"
  "v_fma_f32 v93, v93, v226, v227\n"
  "v_fma_f32 v94, v94, v226, v227\n"
  "v_fma_f32 v95, v95, v226, v227\n"
  "v_cvt_pk_bf16_f32 v218, v80, v81\n"
  "v_cvt_pk_bf16_f32 v219, v82, v83\n"
  "v_cvt_pk_bf16_f32 v220, v84, v85\n"
  "v_cvt_pk_bf16_f32 v221, v86, v87\n"
  "v_cvt_pk_bf16_f32 v214, v88, v89\n"
  "v_cvt_pk_bf16_f32 v215, v90, v91\n"
  "v_cvt_pk_bf16_f32 v216, v92, v93\n"
  "ds_read_b32 v253, v229\n"
  "ds_write_b128 v248, v[218:221] offset:12544\n"
  "v_cvt_pk_bf16_f32 v217, v94, v95\n"
  "v_cvt_pk_bf16_f32 v206, v206, v207\n"
  "v_cvt_pk_bf16_f32 v210, v210, v211\n"
  "v_cvt_pk_bf16_f32 v207, v208, v209\n"
  "v_cvt_pk_bf16_f32 v211, v212, v213\n"
  "s_waitcnt vmcnt(8)\n"
  "v_lshlrev_b32 v208, 16, v199\n"
  "v_and_b32 v209, s49, v199\n"
  "buffer_store_dwordx2 v[206:207], v247, s[36:39], s80 offen offset:512 sc1\n"
  "ds_write_b128 v248, v[214:217] offset:12800\n"
  "s_cmp_eq_u32 s57, 7\ns_cbranch_scc0 Hs18_%=\nv_mov_b32 v238, v242\nv_mov_b32 v241, v243\nHs18_%=:\n"
  "s_add_u32 m0, s64, 16608\n"
  "s_waitcnt lgkmcnt(1)\n"
  "v_readfirstlane_b32 s22, v253\n"
  "v_lshlrev_b32 v206, 16, v198\n"
  "v_and_b32 v207, s49, v198\n"
  "s_waitcnt vmcnt(8)\n"
  "v_lshlrev_b32 v212, 16, v201\n"
  "v_and_b32 v213, s49, v201\n"
  "s_cmp_ge_u32 s22, 2304\ns_cbranch_scc0 HB5_slow_%=\nHB5_back_%=:\n"
  "buffer_store_dwordx2 v[210:211], v247, s[36:39], s80 offen offset:1024 sc1\n"
  "ds_read_b64 v[182:183], v244 offset:24576\n"
  "ds_read_b64 v[184:185], v244 offset:30720\n"
  "ds_read_b64 v[186:187], v244 offset:36864\n"
  "ds_read_b64 v[188:189], v244 offset:43008\n"
  "ds_read_b64 v[174:175], v245 offset:24576\n"
  "ds_read_b64 v[176:177], v245 offset:30720\n"
  "ds_read_b64 v[178:179], v245 offset:36864\n"
  "ds_read_b64 v[180:181], v245 offset:43008\n"
  "v_lshlrev_b32 v210, 16, v200\n"
  "v_and_b32 v211, s49, v200\n"
  "buffer_load_dwordx4 v238, s[24:27], s75 offen lds\n"
  "s_waitcnt lgkmcnt(0)\n"
  "v_smfmac_f32_16x16x64_bf16 v[202:205], v[230:233], v[182:189], v234\n"
  "ds_read_b64 v[182:183], v246 offset:24576\n"
  "ds_read_b64 v[184:185], v246 offset:30720\n"
  "ds_read_b64 v[186:187], v246 offset:36864\n"
  "ds_read_b64 v[188:189], v246 offset:43008\n"
  "ds_append v253 offset:62464\n"
  "buffer_load_dwordx4 v239, s[24:27], s75 offen offset:1040 lds\n"
  "buffer_load_dwordx4 v240, s[24:27], s75 offen offset:2080 lds\n"
  "buffer_load_dwordx4 v241, s[24:27], s75 offen offset:3120 lds\n"
  "buffer_load_dwordx2 v[196:197], v247, s[28:31], s83 offen nt\n"
  "buffer_load_dwordx2 v[198:199], v247, s[28:31], s83 offen offset:512 nt\n"
  "buffer_load_dwordx2 v[200:201], v247, s[28:31], s83 offen offset:1024 nt\n"
  "ds_read_b64 v[152:153], v236 offset:34048\n"
  "ds_read_b64 v[144:145], v236 offset:33216\n"
  "ds_read_b64 v[146:147], v236 offset:33424\n"
  "ds_read_b64 v[148:149], v236 offset:33632\n"
  "ds_read_b64 v[150:151], v236 offset:33840\n"
  "ds_read_b64 v[162:163], v236 offset:34056\n"
  "ds_read_b64 v[154:155], v236 offset:33224\n"
  "ds_read_b64 v[156:157], v236 offset:33432\n"
  "ds_read_b64 v[158:159], v236 offset:33640\n"
  "ds_read_b64 v[160:161], v236 offset:33848\n"
  "s_waitcnt lgkmcnt(2)\n"
  "v_mfma_f32_16x16x16_bf16 v[120:123], v[8:9], v[152:153], 0\n"
  "ds_read_b64 v[172:173], v236 offset:34064\n"
  "ds_read_b64 v[164:165], v236 offset:33232\n"
  "ds_read_b64 v[166:167], v236 offset:33440\n"
  "v_mfma_f32_16x16x16_bf16 v[124:127], v[20:21], v[162:163], 0\n"
  "ds_read_b64 v[168:169], v236 offset:33648\n"
  "ds_read_b64 v[170:171], v236 offset:33856\n"
  "v_smfmac_f32_16x16x64_bf16 v[96:99], v[4:7], v[144:151], v234\n"
  "v_smfmac_f32_16x16x64_bf16 v[120:123], v[0:3], v[144:151], v234\n"
  "ds_read_b64 v[144:145], v236 offset:33240\n"
  "ds_read_b64 v[146:147], v236 offset:33448\n"
  "ds_read_b64 v[148:149], v236 offset:33656\n"
  "ds_read_b64 v[150:151], v236 offset:33864\n"
  "s_nop 0\n"
  "v_mfma_f32_16x16x16_bf16 v[96:99], v[10:11], v[152:153], v[96:99]\n"
  "ds_read_b64 v[152:153], v236 offset:34072\n"
  "s_waitcnt lgkmcnt(5)\n"
  "v_mfma_f32_16x16x16_bf16 v[128:131], v[32:33], v[172:173], 0\n"
  "v_smfmac_f32_16x16x64_bf16 v[100:103], v[16:19], v[154:161], v234\n"
  "v_smfmac_f32_16x16x64_bf16 v[124:127], v[12:15], v[154:161], v234\n"
  "ds_read_b64 v[154:155], v236 offset:33248\n"
  "ds_read_b64 v[156:157], v236 offset:33456\n"
  "ds_read_b64 v[158:159], v236 offset:33664\n"
  "ds_read_b64 v[160:161], v236 offset:33872\n"
  "v_mul_f32 v223, v96, v96\n"
  "v_add_f32 v222, v96, v97\n"
  "v_fmac_f32 v223, v97, v97\n"
  "v_mfma_f32_16x16x16_bf16 v[100:103], v[22:23], v[162:163], v[100:103]\n"
  "v_fmac_f32 v223, v98, v98\n"
  "v_add_f32 v222, v222, v98\n"
  "v_fmac_f32 v223, v99, v99\n"
  "v_smfmac_f32_16x16x64_bf16 v[104:107], v[28:31], v[164:171], v234\n"
  "ds_read_b64 v[162:163], v236 offset:34080\n"
  "v_add_f32 v222, v222, v99\n"
  "v_smfmac_f32_16x16x64_bf16 v[128:131], v[24:27], v[164:171], v234\n"
  "v_cvt_pk_bf16_f32 v202, v202, v203\n"
  "v_fmac_f32 v223, v100, v100\n"
  "v_add_f32 v222, v222, v100\n"
  "v_fmac_f32 v223, v101, v101\n"
  "v_add_f32 v222, v222, v101\n"
  "ds_read_b64 v[164:165], v236 offset:33256\n"
  "ds_read_b64 v[166:167], v236 offset:33464\n"
  "ds_read_b64 v[168:169], v236 offset:33672\n"
  "ds_read_b64 v[170:171], v236 offset:33880\n"
  "v_mfma_f32_16x16x16_bf16 v[104:107], v[34:35], v[172:173], v[104:107]\n"
  "v_fmac_f32 v223, v102, v102\n"
  "v_add_f32 v222, v222, v102\n"
  "v_fmac_f32 v223, v103, v103\n"
  "v_add_f32 v222, v222, v103\n"
  "v_cvt_pk_bf16_f32 v203, v204, v205\n"
  "s_waitcnt lgkmcnt(4)\n"
  "v_smfmac_f32_16x16x64_bf16 v[108:111], v[40:43], v[144:151], v234\n"
  "ds_read_b64 v[172:173], v236 offset:34088\n"
  "buffer_store_dwordx2 v[202:203], v247, s[36:39], s81 offen sc1\n"
  "v_fmac_f32 v223, v104, v104\n"
  "v_add_f32 v222, v222, v104\n"
  "v_fmac_f32 v223, v105, v105\n"
  "v_add_f32 v222, v222, v105\n"
  "v_mfma_f32_16x16x16_bf16 v[108:111], v[46:47], v[152:153], v[108:111]\n"
  "v_fmac_f32 v223, v106, v106\n"
  "v_add_f32 v222, v222, v106\n"
  "v_fmac_f32 v223, v107, v107\n"
  "v_smfmac_f32_16x16x64_bf16 v[112:115], v[52:55], v[154:161], v234\n"
  "v_add_f32 v222, v222, v107\n"
  "v_smfmac_f32_16x16x64_bf16 v[206:209], v[230:233], v[174:181], v234\n"
  "s_waitcnt lgkmcnt(0)\n"
  "v_smfmac_f32_16x16x64_bf16 v[116:119], v[64:67], v[164:171], v234\n"
  "v_fmac_f32 v223, v108, v108\n"
  "v_add_f32 v222, v222, v108\n"
  "v_fmac_f32 v223, v109, v109\n"
  "v_add_f32 v222, v222, v109\n"
  "v_mfma_f32_16x16x16_bf16 v[112:115], v[58:59], v[162:163], v[112:115]\n"
  "v_fmac_f32 v223, v110, v110\n"
  "v_add_f32 v222, v222, v110\n"
  "v_fmac_f32 v223, v111, v111\n"
  "v_add_f32 v222, v222, v111\n"
  "v_mfma_f32_16x16x16_bf16 v[116:119], v[70:71], v[172:173], v[116:119]\n"
  "ds_read_b32 v253, v229\n"
  "s_nop 1\n"
  "v_fmac_f32 v223, v112, v112\n"
  "v_add_f32 v222, v222, v112\n"
  "v_fmac_f32 v223, v113, v113\n"
  "v_add_f32 v222, v222, v113\n"
  "v_fmac_f32 v223, v114, v114\n"
  "v_add_f32 v222, v222, v114\n"
  "v_fmac_f32 v223, v115, v115\n"
  "v_smfmac_f32_16x16x64_bf16 v[210:213], v[230:233], v[182:189], v234\n"
  "s_waitcnt lgkmcnt(0)\n"
  "v_readfirstlane_b32 s22, v253\n"
  "v_add_f32 v222, v222, v115\n"
  "v_fmac_f32 v223, v116, v116\n"
  "v_add_f32 v222, v222, v116\n"
  "v_fmac_f32 v223, v117, v117\n"
  "v_add_f32 v222, v222, v117\n"
  "v_fmac_f32 v223, v118, v118\n"
  "v_add_f32 v222, v222, v118\n"
  "v_fmac_f32 v223, v119, v119\n"
  "s_cmp_ge_u32 s22, 2816\ns_cbranch_scc0 HB6_slow_%=\nHB6_back_%=:\n"
  "ds_read_b64 v[174:175], v244\n"
  "ds_read_b64 v[176:177], v244 offset:6144\n"
  "ds_read_b64 v[178:179], v244 offset:12288\n"
  "ds_read_b64 v[180:181], v244 offset:18432\n"
  "ds_read_b64 v[182:183], v245\n"
  "ds_read_b64 v[184:185], v245 offset:6144\n"
  "ds_read_b64 v[186:187], v245 offset:12288\n"
  "ds_read_b64 v[188:189], v245 offset:18432\n"
  "s_waitcnt vmcnt(13)\n"
  "v_lshlrev_b32 v204, 16, v191\n"
  "v_and_b32 v205, s49, v191\n"
  "v_lshlrev_b32 v202, 16, v190\n"
  "v_and_b32 v203, s49, v190\n"
  "v_add_f32 v222, v222, v119\n"
  "v_mfma_f32_16x16x16_bf16 v[132:135], v[44:45], v[152:153], 0\n"
  "v_cvt_pk_bf16_f32 v206, v206, v207\n"
  "v_cvt_pk_bf16_f32 v210, v210, v211\n"
  "v_cvt_pk_bf16_f32 v207, v208, v209\n"
  "ds_write_b64 v249, v[222:223]\n"
  "buffer_store_dwordx2 v[206:207], v247, s[36:39], s81 offen offset:512 sc1\n"
  "s_waitcnt lgkmcnt(1)\n"
  "v_smfmac_f32_16x16x64_bf16 v[202:205], v[230:233], v[174:181], v234\n"
  "v_cvt_pk_bf16_f32 v211, v212, v213\n"
  "v_mfma_f32_16x16x16_bf16 v[136:139], v[56:57], v[162:163], 0\n"
  "ds_read_b64 v[174:175], v246\n"
  "ds_read_b64 v[176:177], v246 offset:6144\n"
  "ds_read_b64 v[178:179], v246 offset:12288\n"
  "ds_read_b64 v[180:181], v246 offset:18432\n"
  "ds_append v253 offset:62464\n"
  "buffer_store_dwordx2 v[210:211], v247, s[36:39], s81 offen offset:1024 sc1\n"
  "v_mfma_f32_16x16x16_bf16 v[140:143], v[68:69], v[172:173], 0\n"
  "s_waitcnt vmcnt(14)\n"
  "v_lshlrev_b32 v208, 16, v193\n"
  "v_and_b32 v209, s49, v193\n"
  "v_lshlrev_b32 v206, 16, v192\n"
  "v_and_b32 v207, s49, v192\n"
  "v_smfmac_f32_16x16x64_bf16 v[132:135], v[36:39], v[144:151], v234\n"
  "buffer_load_dwordx2 v[190:191], v247, s[28:31], s84 offen nt\n"
  "s_waitcnt vmcnt(14)\n"
  "v_lshlrev_b32 v212, 16, v195\n"
  "v_and_b32 v213, s49, v195\n"
  "v_lshlrev_b32 v210, 16, v194\n"
  "v_and_b32 v211, s49, v194\n"
  "v_smfmac_f32_16x16x64_bf16 v[136:139], v[48:51], v[154:161], v234\n"
  "buffer_load_dwordx2 v[192:193], v247, s[28:31], s84 offen offset:512 nt\n"
  "v_smfmac_f32_16x16x64_bf16 v[140:143], v[60:63], v[164:171], v234\n"
  "buffer_load_dwordx2 v[194:195], v247, s[28:31], s84 offen offset:1024 nt\n"
  "ds_read_b32 v253, v229\n"
  "ds_read_b64 v[214:215], v250\n"
  "ds_read_b64 v[216:217], v250 offset:512\n"
  "ds_read_b64 v[218:219], v250 offset:1024\n"
  "ds_read_b64 v[220:221], v250 offset:1536\n"
  "s_waitcnt lgkmcnt(0)\n"
  "v_readfirstlane_b32 s22, v253\n"
  "v_smfmac_f32_16x16x64_bf16 v[206:209], v[230:233], v[182:189], v234\n"
  "s_cmp_ge_u32 s22, 3072\ns_cbranch_scc0 HA7_slow_%=\nHA7_back_%=:\n"
  "v_add_f32 v214, v214, v216\n"
  "v_add_f32 v215, v215, v217\n"
  "v_add_f32 v218, v218, v220\n"
  "v_add_f32 v219, v219, v221\n"
  "v_add_f32 v214, v214, v218\n"
  "v_add_f32 v215, v215, v219\n"
  "v_fmaak_f32 v252, v235, v215, 0x3727c5ac\n"
  "v_mul_f32 v251, v235, v214\n"
  "v_fma_f32 v252, -v251, v251, v252\n"
  "v_max_f32 v252, 0x3727c5ac, v252\n"
  "v_rsq_f32 v252, v252\n"
  "s_nop 0\n"
  "v_mul_f32 v226, s47, v252\n"
  "v_mul_f32_e64 v227, -v251, v226\n"
  "v_fma_f32 v96, v96, v226, v227\n"
  "v_fma_f32 v97, v97, v226, v227\n"
  "v_fma_f32 v98, v98, v226, v227\n"
  "v_fma_f32 v99, v99, v226, v227\n"
  "v_fma_f32 v100, v100, v226, v227\n"
  "v_smfmac_f32_16x16x64_bf16 v[210:213], v[230:233], v[174:181], v234\n"
  "v_fma_f32 v101, v101, v226, v227\n"
  "v_fma_f32 v102, v102, v226, v227\n"
  "v_fma_f32 v103, v103, v226, v227\n"
  "v_cvt_pk_bf16_f32 v202, v202, v203\n"
  "v_cvt_pk_bf16_f32 v203, v204, v205\n"
  "v_cvt_pk_bf16_f32 v214, v96, v97\n"
  "v_cvt_pk_bf16_f32 v215, v98, v99\n"
  "v_cvt_pk_bf16_f32 v216, v100, v101\n"
  "v_cvt_pk_bf16_f32 v217, v102, v103\n"
  "buffer_store_dwordx2 v[202:203], v247, s[36:39], s82 offen sc1\n"
  "ds_write_b128 v248, v[214:217]\n"
  "s_waitcnt vmcnt(9)\n"
  "v_lshlrev_b32 v204, 16, v197\n"
  "v_and_b32 v205, s49, v197\n"
  "v_lshlrev_b32 v202, 16, v196\n"
  "v_and_b32 v203, s49, v196\n"
  "v_fma_f32 v104, v104, v226, v227\n"
  "v_fma_f32 v105, v105, v226, v227\n"
  "v_fma_f32 v106, v106, v226, v227\n"
  "v_fma_f32 v107, v107, v226, v227\n"
  "v_fma_f32 v108, v108, v226, v227\n"
  "v_fma_f32 v109, v109, v226, v227\n"
  "v_fma_f32 v110, v110, v226, v227\n"
  "v_fma_f32 v111, v111, v226, v227\n"
  "v_fma_f32 v112, v112, v226, v227\n"
  "v_fma_f32 v113, v113, v226, v227\n"
  "v_fma_f32 v114, v114, v226, v227\n"
  "v_fma_f32 v115, v115, v226, v227\n"
  "v_fma_f32 v116, v116, v226, v227\n"
  "v_fma_f32 v117, v117, v226, v227\n"
  "v_fma_f32 v118, v118, v226, v227\n"
  "v_fma_f32 v119, v119, v226, v227\n"
  "v_cvt_pk_bf16_f32 v218, v104, v105\n"
  "v_cvt_pk_bf16_f32 v219, v106, v107\n"
  "v_cvt_pk_bf16_f32 v220, v108, v109\n"
  "v_cvt_pk_bf16_f32 v221, v110, v111\n"
  "v_cvt_pk_bf16_f32 v214, v112, v113\n"
  "v_cvt_pk_bf16_f32 v215, v114, v115\n"
  "v_cvt_pk_bf16_f32 v216, v116, v117\n"
  "ds_read_b32 v253, v229\n"
  "ds_write_b128 v248, v[218:221] offset:256\n"
  "v_cvt_pk_bf16_f32 v217, v118, v119\n"
  "v_cvt_pk_bf16_f32 v206, v206, v207\n"
  "v_cvt_pk_bf16_f32 v210, v210, v211\n"
  "v_cvt_pk_bf16_f32 v207, v208, v209\n"
  "v_cvt_pk_bf16_f32 v211, v212, v213\n"
  "s_waitcnt vmcnt(8)\n"
  "v_lshlrev_b32 v208, 16, v199\n"
  "v_and_b32 v209, s49, v199\n"
  "buffer_store_dwordx2 v[206:207], v247, s[36:39], s82 offen offset:512 sc1\n"
  "ds_write_b128 v248, v[214:217] offset:512\n"
  "s_add_u32 m0, s64, 33216\n"
  "s_waitcnt lgkmcnt(1)\n"
  "v_readfirstlane_b32 s22, v253\n"
  "v_lshlrev_b32 v206, 16, v198\n"
  "v_and_b32 v207, s49, v198\n"
  "s_waitcnt vmcnt(8)\n"
  "v_lshlrev_b32 v212, 16, v201\n"
  "v_and_b32 v213, s49, v201\n"
  "s_cmp_ge_u32 s22, 3328\ns_cbranch_scc0 HB7_slow_%=\nHB7_back_%=:\n"
  "buffer_store_dwordx2 v[210:211], v247, s[36:39], s82 offen offset:1024 sc1\n"
  "ds_read_b64 v[182:183], v244 offset:24576\n"
  "ds_read_b64 v[184:185], v244 offset:30720\n"
  "ds_read_b64 v[186:187], v244 offset:36864\n"
  "ds_read_b64 v[188:189], v244 offset:43008\n"
  "ds_read_b64 v[174:175], v245 offset:24576\n"
  "ds_read_b64 v[176:177], v245 offset:30720\n"
  "ds_read_b64 v[178:179], v245 offset:36864\n"
  "ds_read_b64 v[180:181], v245 offset:43008\n"
  "v_lshlrev_b32 v210, 16, v200\n"
  "v_and_b32 v211, s49, v200\n"
  "v_mov_b32 v237, 61440\n"
  "buffer_load_dwordx4 v238, s[24:27], s76 offen lds\n"
  "s_waitcnt lgkmcnt(0)\n"
  "v_smfmac_f32_16x16x64_bf16 v[202:205], v[230:233], v[182:189], v234\n"
  "ds_read_b64 v[182:183], v246 offset:24576\n"
  "ds_read_b64 v[184:185], v246 offset:30720\n"
  "ds_read_b64 v[186:187], v246 offset:36864\n"
  "ds_read_b64 v[188:189], v246 offset:43008\n"
  "ds_append v253 offset:62464\n"
  "buffer_load_dwordx4 v239, s[24:27], s76 offen offset:1040 lds\n"
  "buffer_load_dwordx4 v240, s[24:27], s76 offen offset:2080 lds\n"
  "buffer_load_dwordx4 v241, s[24:27], s76 offen offset:3120 lds\n"
  "buffer_load_dwordx2 v[196:197], v247, s[28:31], s85 offen nt\n"
  "buffer_load_dwordx2 v[198:199], v247, s[28:31], s85 offen offset:512 nt\n"
  "buffer_load_dwordx2 v[200:201], v247, s[28:31], s85 offen offset:1024 nt\n"
  "s_cmp_eq_u32 s56, 6\ns_cselect_b32 s59, -1, 0\ns_mov_b32 s58, 0\n"
  "v_cndmask_b32_e64 v237, v236, v237, s[58:59]\n"
  "ds_read_b64 v[144:145], v237\n"
  "ds_read_b64 v[146:147], v237 offset:208\n"
  "ds_read_b64 v[148:149], v237 offset:416\n"
  "ds_read_b64 v[150:151], v237 offset:624\n"
  "ds_read_b64 v[152:153], v237 offset:832\n"
  "ds_read_b64 v[154:155], v237 offset:8\n"
  "ds_read_b64 v[156:157], v237 offset:216\n"
  "ds_read_b64 v[158:159], v237 offset:424\n"
  "ds_read_b64 v[160:161], v237 offset:632\n"
  "ds_read_b64 v[162:163], v237 offset:840\n"
  "ds_read_b64 v[164:165], v237 offset:16\n"
  "ds_read_b64 v[166:167], v237 offset:224\n"
  "ds_read_b64 v[168:169], v237 offset:432\n"
  "ds_read_b64 v[170:171], v237 offset:640\n"
  "ds_read_b64 v[172:173], v237 offset:848\n"
  "s_waitcnt lgkmcnt(0)\n"
  "v_smfmac_f32_16x16x64_bf16 v[120:123], v[4:7], v[144:151], v234\n"
  "v_smfmac_f32_16x16x64_bf16 v[124:127], v[16:19], v[154:161], v234\n"
  "v_smfmac_f32_16x16x64_bf16 v[128:131], v[28:31], v[164:171], v234\n"
  "s_nop 3\n"
  "v_mfma_f32_16x16x16_bf16 v[120:123], v[10:11], v[152:153], v[120:123]\n"
  "s_cmp_eq_u32 s56, 6\ns_cbranch_scc0 Hs20_%=\nds_read_b64 v[144:145], v255 offset:0\nds_read_b64 v[146:147], v255 offset:208\nds_read_b64 v[148:149], v255 offset:416\nds_read_b64 v[150:151], v255 offset:624\nds_read_b64 v[152:153], v255 offset:832\nHs20_%=:\n"
  "v_mfma_f32_16x16x16_bf16 v[124:127], v[22:23], v[162:163], v[124:127]\n"
  "s_cmp_eq_u32 s56, 6\ns_cbranch_scc0 Hs22_%=\nds_read_b64 v[154:155], v255 offset:8\nds_read_b64 v[156:157], v255 offset:216\nds_read_b64 v[158:159], v255 offset:424\nds_read_b64 v[160:161], v255 offset:632\nds_read_b64 v[162:163], v255 offset:840\nHs22_%=:\n"
  "v_mfma_f32_16x16x16_bf16 v[128:131], v[34:35], v[172:173], v[128:131]\n"
  "s_nop 3\n"
  "v_mul_f32 v223, v120, v120\n"
  "v_add_f32 v222, v120, v121\n"
  "v_fmac_f32 v223, v121, v121\n"
  "v_add_f32 v222, v222, v122\n"
  "v_fmac_f32 v223, v122, v122\n"
  "v_add_f32 v222, v222, v123\n"
  "v_fmac_f32 v223, v123, v123\n"
  "v_smfmac_f32_16x16x64_bf16 v[206:209], v[230:233], v[174:181], v234\n"
  "s_cmp_eq_u32 s56, 6\ns_cbranch_scc0 Hs19_%=\ns_waitcnt lgkmcnt(0)\nHs19_%=:\n"
  "s_cmp_eq_u32 s56, 6\ns_cbranch_scc0 Hs21_%=\ns_waitcnt lgkmcnt(0)\nHs21_%=:\n"
  "v_mfma_f32_16x16x16_bf16 v[72:75], v[8:9], v[152:153], 0\n"
  "v_fmac_f32 v223, v124, v124\n"
  "v_add_f32 v222, v222, v124\n"
  "v_fmac_f32 v223, v125, v125\n"
  "v_mfma_f32_16x16x16_bf16 v[76:79], v[20:21], v[162:163], 0\n"
  "v_add_f32 v222, v222, v125\n"
  "v_fmac_f32 v223, v126, v126\n"
  "v_add_f32 v222, v222, v126\n"
  "v_smfmac_f32_16x16x64_bf16 v[72:75], v[0:3], v[144:151], v234\n"
  "ds_read_b64 v[144:145], v237 offset:24\n"
  "ds_read_b64 v[146:147], v237 offset:232\n"
  "ds_read_b64 v[148:149], v237 offset:440\n"
  "ds_read_b64 v[150:151], v237 offset:648\n"
  "ds_read_b64 v[152:153], v237 offset:856\n"
  "s_cmp_eq_u32 s56, 6\ns_cbranch_scc0 Hs24_%=\nds_read_b64 v[164:165], v255 offset:16\nds_read_b64 v[166:167], v255 offset:224\nds_read_b64 v[168:169], v255 offset:432\nds_read_b64 v[170:171], v255 offset:640\nds_read_b64 v[172:173], v255 offset:848\nHs24_%=:\n"
  "ds_read_b64 v[162:163], v237 offset:864\n"
  "v_smfmac_f32_16x16x64_bf16 v[76:79], v[12:15], v[154:161], v234\n"
  "v_fmac_f32 v223, v127, v127\n"
  "v_add_f32 v222, v222, v127\n"
  "v_fmac_f32 v223, v128, v128\n"
  "v_add_f32 v222, v222, v128\n"
  "v_smfmac_f32_16x16x64_bf16 v[210:213], v[230:233], v[182:189], v234\n"
  "ds_read_b64 v[154:155], v237 offset:32\n"
  "ds_read_b64 v[156:157], v237 offset:240\n"
  "ds_read_b64 v[158:159], v237 offset:448\n"
  "ds_read_b64 v[160:161], v237 offset:656\n"
  "s_cmp_eq_u32 s56, 6\ns_cbranch_scc0 Hs23_%=\ns_waitcnt lgkmcnt(0)\nHs23_%=:\n"
  "v_mfma_f32_16x16x16_bf16 v[80:83], v[32:33], v[172:173], 0\n"
  "v_fmac_f32 v223, v129, v129\n"
  "v_add_f32 v222, v222, v129\n"
  "v_fmac_f32 v223, v130, v130\n"
  "v_add_f32 v222, v222, v130\n"
  "v_fmac_f32 v223, v131, v131\n"
  "v_add_f32 v222, v222, v131\n"
  "v_smfmac_f32_16x16x64_bf16 v[80:83], v[24:27], v[164:171], v234\n"
  "ds_read_b64 v[164:165], v237 offset:40\n"
  "ds_read_b64 v[166:167], v237 offset:248\n"
  "ds_read_b64 v[168:169], v237 offset:456\n"
  "ds_read_b64 v[170:171], v237 offset:664\n"
  "ds_read_b64 v[172:173], v237 offset:872\n"
  "s_waitcnt lgkmcnt(5)\n"
  "v_smfmac_f32_16x16x64_bf16 v[132:135], v[40:43], v[144:151], v234\n"
  "v_cvt_pk_bf16_f32 v202, v202, v203\n"
  "v_smfmac_f32_16x16x64_bf16 v[136:139], v[52:55], v[154:161], v234\n"
  "v_cvt_pk_bf16_f32 v203, v204, v205\n"
  "buffer_store_dwordx2 v[202:203], v247, s[36:39], s83 offen sc1\n"
  "s_nop 1\n"
  "v_mfma_f32_16x16x16_bf16 v[132:135], v[46:47], v[152:153], v[132:135]\n"
  "s_waitcnt vmcnt(13)\n"
  "v_lshlrev_b32 v204, 16, v191\n"
  "v_and_b32 v205, s49, v191\n"
  "v_mfma_f32_16x16x16_bf16 v[136:139], v[58:59], v[162:163], v[136:139]\n"
  "v_lshlrev_b32 v202, 16, v190\n"
  "v_and_b32 v203, s49, v190\n"
  "s_waitcnt lgkmcnt(0)\n"
  "v_smfmac_f32_16x16x64_bf16 v[140:143], v[64:67], v[164:171], v234\n"
  "ds_read_b32 v253, v229\n"
  "s_cmp_eq_u32 s56, 6\ns_cbranch_scc0 Hs26_%=\nds_read_b64 v[144:145], v255 offset:24\nds_read_b64 v[146:147], v255 offset:232\nds_read_b64 v[148:149], v255 offset:440\nds_read_b64 v[150:151], v255 offset:648\nds_read_b64 v[152:153], v255 offset:856\nHs26_%=:\n"
  "s_cmp_eq_u32 s56, 6\ns_cbranch_scc0 Hs28_%=\nds_read_b64 v[154:155], v255 offset:32\nds_read_b64 v[156:157], v255 offset:240\nds_read_b64 v[158:159], v255 offset:448\nds_read_b64 v[160:161], v255 offset:656\nds_read_b64 v[162:163], v255 offset:864\nHs28_%=:\n"
  "v_add_f32 v222, v222, v132\n"
  "v_fmac_f32 v223, v132, v132\n"
  "v_fmac_f32 v223, v133, v133\n"
  "v_add_f32 v222, v222, v133\n"
  "v_fmac_f32 v223, v134, v134\n"
  "v_add_f32 v222, v222, v134\n"
  "v_fmac_f32 v223, v135, v135\n"
  "v_add_f32 v222, v222, v135\n"
  "v_fmac_f32 v223, v136, v136\n"
  "s_cmp_eq_u32 s56, 6\ns_cbranch_scc0 Hs25_%=\ns_waitcnt lgkmcnt(0)\nHs25_%=:\n"
  "s_cmp_eq_u32 s56, 6\ns_cbranch_scc0 Hs27_%=\ns_waitcnt lgkmcnt(0)\nHs27_%=:\n"
  "buffer_load_dwordx2 v[190:191], v247, s[28:31], s86 offen nt\n"
  "s_waitcnt lgkmcnt(0)\n"
  "v_readfirstlane_b32 s22, v253\n"
  "v_add_f32 v222, v222, v136\n"
  "v_fmac_f32 v223, v137, v137\n"
  "v_add_f32 v222, v222, v137\n"
  "v_mfma_f32_16x16x16_bf16 v[140:143], v[70:71], v[172:173], v[140:143]\n"
  "v_fmac_f32 v223, v138, v138\n"
  "v_add_f32 v222, v222, v138\n"
  "v_fmac_f32 v223, v139, v139\n"
  "v_add_f32 v222, v222, v139\n"
  "s_cmp_ge_u32 s22, 3840\ns_cbranch_scc0 HB8_slow_%=\nHB8_back_%=:\n"
  "ds_read_b64 v[174:175], v244\n"
  "ds_read_b64 v[176:177], v244 offset:6144\n"
  "ds_read_b64 v[178:179], v244 offset:12288\n"
  "ds_read_b64 v[180:181], v244 offset:18432\n"
  "ds_read_b64 v[182:183], v245\n"
  "ds_read_b64 v[184:185], v245 offset:6144\n"
  "ds_read_b64 v[186:187], v245 offset:12288\n"
  "ds_read_b64 v[188:189], v245 offset:18432\n"
  "s_cmp_eq_u32 s56, 6\ns_cbranch_scc0 Hs30_%=\nds_read_b64 v[164:165], v255 offset:40\nds_read_b64 v[166:167], v255 offset:248\nds_read_b64 v[168:169], v255 offset:456\nds_read_b64 v[170:171], v255 offset:664\nds_read_b64 v[172:173], v255 offset:872\nHs30_%=:\n"
  "v_fmac_f32 v223, v140, v140\n"
  "v_add_f32 v222, v222, v140\n"
  "v_fmac_f32 v223, v141, v141\n"
  "v_add_f32 v222, v222, v141\n"
  "v_fmac_f32 v223, v142, v142\n"
  "v_add_f32 v222, v222, v142\n"
  "v_fmac_f32 v223, v143, v143\n"
  "v_add_f32 v222, v222, v143\n"
  "v_mfma_f32_16x16x16_bf16 v[84:87], v[44:45], v[152:153], 0\n"
  "ds_write_b64 v249, v[222:223]\n"
  "s_cmp_eq_u32 s56, 6\ns_cbranch_scc0 Hs29_%=\ns_waitcnt lgkmcnt(0)\nHs29_%=:\n"
  "s_waitcnt lgkmcnt(1)\n"
  "v_smfmac_f32_16x16x64_bf16 v[202:205], v[230:233], v[174:181], v234\n"
  "v_cvt_pk_bf16_f32 v206, v206, v207\n"
  "v_mfma_f32_16x16x16_bf16 v[88:91], v[56:57], v[162:163], 0\n"
  "v_cvt_pk_bf16_f32 v210, v210, v211\n"
  "ds_read_b64 v[174:175], v246\n"
  "ds_read_b64 v[176:177], v246 offset:6144\n"
  "ds_read_b64 v[178:179], v246 offset:12288\n"
  "ds_read_b64 v[180:181], v246 offset:18432\n"
  "ds_append v253 offset:62464\n"
  "v_mfma_f32_16x16x16_bf16 v[92:95], v[68:69], v[172:173], 0\n"
  "v_cvt_pk_bf16_f32 v207, v208, v209\n"
  "v_cvt_pk_bf16_f32 v211, v212, v213\n"
  "s_waitcnt vmcnt(13)\n"
  "v_lshlrev_b32 v208, 16, v193\n"
  "v_and_b32 v209, s49, v193\n"
  "v_smfmac_f32_16x16x64_bf16 v[84:87], v[36:39], v[144:151], v234\n"
  "buffer_store_dwordx2 v[206:207], v247, s[36:39], s83 offen offset:512 sc1\n"
  "s_waitcnt vmcnt(13)\n"
  "v_lshlrev_b32 v212, 16, v195\n"
  "v_and_b32 v213, s49, v195\n"
  "v_lshlrev_b32 v206, 16, v192\n"
  "v_and_b32 v207, s49, v192\n"
  "v_smfmac_f32_16x16x64_bf16 v[88:91], v[48:51], v[154:161], v234\n"
  "buffer_load_dwordx2 v[192:193], v247, s[28:31], s86 offen offset:512 nt\n"
  "v_smfmac_f32_16x16x64_bf16 v[92:95], v[60:63], v[164:171], v234\n"
  "buffer_store_dwordx2 v[210:211], v247, s[36:39], s83 offen offset:1024 sc1\n"
  "ds_read_b32 v253, v229\n"
  "ds_read_b64 v[214:215], v250\n"
  "ds_read_b64 v[216:217], v250 offset:512\n"
  "ds_read_b64 v[218:219], v250 offset:1024\n"
  "ds_read_b64 v[220:221], v250 offset:1536\n"
  "v_lshlrev_b32 v210, 16, v194\n"
  "v_and_b32 v211, s49, v194\n"
  "s_waitcnt lgkmcnt(0)\n"
  "v_readfirstlane_b32 s22, v253\n"
  "v_smfmac_f32_16x16x64_bf16 v[206:209], v[230:233], v[182:189], v234\n"
  "buffer_load_dwordx2 v[194:195], v247, s[28:31], s86 offen offset:1024 nt\n"
  "s_cmp_ge_u32 s22, 4096\ns_cbranch_scc0 HA9_slow_%=\nHA9_back_%=:\n"
  "v_add_f32 v214, v214, v216\n"
  "v_add_f32 v215, v215, v217\n"
  "v_add_f32 v218, v218, v220\n"
  "v_add_f32 v219, v219, v221\n"
  "v_add_f32 v214, v214, v218\n"
  "v_add_f32 v215, v215, v219\n"
  "v_fmaak_f32 v252, v235, v215, 0x3727c5ac\n"
  "v_mul_f32 v251, v235, v214\n"
  "v_fma_f32 v252, -v251, v251, v252\n"
  "v_max_f32 v252, 0x3727c5ac, v252\n"
  "v_rsq_f32 v252, v252\n"
  "s_nop 0\n"
  "v_mul_f32 v226, s47, v252\n"
  "v_mul_f32_e64 v227, -v251, v226\n"
  "v_fma_f32 v120, v120, v226, v227\n"
  "v_fma_f32 v121, v121, v226, v227\n"
  "v_fma_f32 v122, v122, v226, v227\n"
  "v_fma_f32 v123, v123, v226, v227\n"
  "v_fma_f32 v124, v124, v226, v227\n"
  "v_smfmac_f32_16x16x64_bf16 v[210:213], v[230:233], v[174:181], v234\n"
  "v_fma_f32 v125, v125, v226, v227\n"
  "v_fma_f32 v126, v126, v226, v227\n"
  "v_fma_f32 v127, v127, v226, v227\n"
  "v_cvt_pk_bf16_f32 v202, v202, v203\n"
  "v_cvt_pk_bf16_f32 v203, v204, v205\n"
  "v_cvt_pk_bf16_f32 v214, v120, v121\n"
  "v_cvt_pk_bf16_f32 v215, v122, v123\n"
  "v_cvt_pk_bf16_f32 v216, v124, v125\n"
  "v_cvt_pk_bf16_f32 v217, v126, v127\n"
  "buffer_store_dwordx2 v[202:203], v247, s[36:39], s84 offen sc1\n"
  "ds_write_b128 v248, v[214:217] offset:12288\n"
  "s_waitcnt vmcnt(9)\n"
  "v_lshlrev_b32 v204, 16, v197\n"
  "v_and_b32 v205, s49, v197\n"
  "v_lshlrev_b32 v202, 16, v196\n"
  "v_and_b32 v203, s49, v196\n"
  "v_fma_f32 v128, v128, v226, v227\n"
  "v_fma_f32 v129, v129, v226, v227\n"
  "v_fma_f32 v130, v130, v226, v227\n"
  "v_fma_f32 v131, v131, v226, v227\n"
  "v_fma_f32 v132, v132, v226, v227\n"
  "v_fma_f32 v133, v133, v226, v227\n"
  "v_fma_f32 v134, v134, v226, v227\n"
  "v_fma_f32 v135, v135, v226, v227\n"
  "v_fma_f32 v136, v136, v226, v227\n"
  "v_fma_f32 v137, v137, v226, v227\n"
  "v_fma_f32 v138, v138, v226, v227\n"
  "v_fma_f32 v139, v139, v226, v227\n"
  "v_fma_f32 v140, v140, v226, v227\n"
  "v_fma_f32 v141, v141, v226, v227\n"
  "v_fma_f32 v142, v142, v226, v227\n"
  "v_fma_f32 v143, v143, v226, v227\n"
  "v_cvt_pk_bf16_f32 v218, v128, v129\n"
  "v_cvt_pk_bf16_f32 v219, v130, v131\n"
  "v_cvt_pk_bf16_f32 v220, v132, v133\n"
  "v_cvt_pk_bf16_f32 v221, v134, v135\n"
  "v_cvt_pk_bf16_f32 v214, v136, v137\n"
  "v_cvt_pk_bf16_f32 v215, v138, v139\n"
  "v_cvt_pk_bf16_f32 v216, v140, v141\n"
  "ds_read_b32 v253, v229\n"
  "ds_write_b128 v248, v[218:221] offset:12544\n"
  "v_cvt_pk_bf16_f32 v217, v142, v143\n"
  "v_cvt_pk_bf16_f32 v206, v206, v207\n"
  "v_cvt_pk_bf16_f32 v210, v210, v211\n"
  "v_cvt_pk_bf16_f32 v207, v208, v209\n"
  "v_cvt_pk_bf16_f32 v211, v212, v213\n"
  "s_waitcnt vmcnt(8)\n"
  "v_lshlrev_b32 v208, 16, v199\n"
  "v_and_b32 v209, s49, v199\n"
  "buffer_store_dwordx2 v[206:207], v247, s[36:39], s84 offen offset:512 sc1\n"
  "ds_write_b128 v248, v[214:217] offset:12800\n"
  "s_cmp_eq_u32 s57, 9\ns_cbranch_scc0 Hs31_%=\nv_mov_b32 v238, v242\nv_mov_b32 v241, v243\nHs31_%=:\n"
  "s_add_u32 m0, s64, 0\n"
  "s_waitcnt lgkmcnt(1)\n"
  "v_readfirstlane_b32 s22, v253\n"
  "v_lshlrev_b32 v206, 16, v198\n"
  "v_and_b32 v207, s49, v198\n"
  "s_waitcnt vmcnt(8)\n"
  "v_lshlrev_b32 v212, 16, v201\n"
  "v_and_b32 v213, s49, v201\n"
  "s_cmp_ge_u32 s22, 4352\ns_cbranch_scc0 HB9_slow_%=\nHB9_back_%=:\n"
  "buffer_store_dwordx2 v[210:211], v247, s[36:39], s84 offen offset:1024 sc1\n"
  "ds_read_b64 v[182:183], v244 offset:24576\n"
  "ds_read_b64 v[184:185], v244 offset:30720\n"
  "ds_read_b64 v[186:187], v244 offset:36864\n"
  "ds_read_b64 v[188:189], v244 offset:43008\n"
  "ds_read_b64 v[174:175], v245 offset:24576\n"
  "ds_read_b64 v[176:177], v245 offset:30720\n"
  "ds_read_b64 v[178:179], v245 offset:36864\n"
  "ds_read_b64 v[180:181], v245 offset:43008\n"
  "v_lshlrev_b32 v210, 16, v200\n"
  "v_and_b32 v211, s49, v200\n"
  "buffer_load_dwordx4 v238, s[24:27], s77 offen lds\n"
  "s_waitcnt lgkmcnt(0)\n"
  "v_smfmac_f32_16x16x64_bf16 v[202:205], v[230:233], v[182:189], v234\n"
  "ds_read_b64 v[182:183], v246 offset:24576\n"
  "ds_read_b64 v[184:185], v246 offset:30720\n"
  "ds_read_b64 v[186:187], v246 offset:36864\n"
  "ds_read_b64 v[188:189], v246 offset:43008\n"
  "ds_append v253 offset:62464\n"
  "buffer_load_dwordx4 v239, s[24:27], s77 offen offset:1040 lds\n"
  "buffer_load_dwordx4 v240, s[24:27], s77 offen offset:2080 lds\n"
  "buffer_load_dwordx4 v241, s[24:27], s77 offen offset:3120 lds\n"
  "buffer_load_dwordx2 v[196:197], v247, s[28:31], s87 offen nt\n"
  "buffer_load_dwordx2 v[198:199], v247, s[28:31], s87 offen offset:512 nt\n"
  "buffer_load_dwordx2 v[200:201], v247, s[28:31], s87 offen offset:1024 nt\n"
  "ds_read_b64 v[152:153], v236 offset:17440\n"
  "ds_read_b64 v[144:145], v236 offset:16608\n"
  "ds_read_b64 v[146:147], v236 offset:16816\n"
  "ds_read_b64 v[148:149], v236 offset:17024\n"
  "ds_read_b64 v[150:151], v236 offset:17232\n"
  "ds_read_b64 v[162:163], v236 offset:17448\n"
  "ds_read_b64 v[154:155], v236 offset:16616\n"
  "ds_read_b64 v[156:157], v236 offset:16824\n"
  "ds_read_b64 v[158:159], v236 offset:17032\n"
  "ds_read_b64 v[160:161], v236 offset:17240\n"
  "s_waitcnt lgkmcnt(2)\n"
  "v_mfma_f32_16x16x16_bf16 v[96:99], v[8:9], v[152:153], 0\n"
  "ds_read_b64 v[172:173], v236 offset:17456\n"
  "ds_read_b64 v[164:165], v236 offset:16624\n"
  "ds_read_b64 v[166:167], v236 offset:16832\n"
  "v_mfma_f32_16x16x16_bf16 v[100:103], v[20:21], v[162:163], 0\n"
  "ds_read_b64 v[168:169], v236 offset:17040\n"
  "ds_read_b64 v[170:171], v236 offset:17248\n"
  "v_smfmac_f32_16x16x64_bf16 v[72:75], v[4:7], v[144:151], v234\n"
  "v_smfmac_f32_16x16x64_bf16 v[96:99], v[0:3], v[144:151], v234\n"
  "ds_read_b64 v[144:145], v236 offset:16632\n"
  "ds_read_b64 v[146:147], v236 offset:16840\n"
  "ds_read_b64 v[148:149], v236 offset:17048\n"
  "ds_read_b64 v[150:151], v236 offset:17256\n"
  "s_nop 0\n"
  "v_mfma_f32_16x16x16_bf16 v[72:75], v[10:11], v[152:153], v[72:75]\n"
  "ds_read_b64 v[152:153], v236 offset:17464\n"
  "s_waitcnt lgkmcnt(5)\n"
  "v_mfma_f32_16x16x16_bf16 v[104:107], v[32:33], v[172:173], 0\n"
  "v_smfmac_f32_16x16x64_bf16 v[76:79], v[16:19], v[154:161], v234\n"
  "v_smfmac_f32_16x16x64_bf16 v[100:103], v[12:15], v[154:161], v234\n"
  "ds_read_b64 v[154:155], v236 offset:16640\n"
  "ds_read_b64 v[156:157], v236 offset:16848\n"
  "ds_read_b64 v[158:159], v236 offset:17056\n"
  "ds_read_b64 v[160:161], v236 offset:17264\n"
  "v_mul_f32 v223, v72, v72\n"
  "v_add_f32 v222, v72, v73\n"
  "v_fmac_f32 v223, v73, v73\n"
  "v_mfma_f32_16x16x16_bf16 v[76:79], v[22:23], v[162:163], v[76:79]\n"
  "v_fmac_f32 v223, v74, v74\n"
  "v_add_f32 v222, v222, v74\n"
  "v_fmac_f32 v223, v75, v75\n"
  "v_smfmac_f32_16x16x64_bf16 v[80:83], v[28:31], v[164:171], v234\n"
  "ds_read_b64 v[162:163], v236 offset:17472\n"
  "v_add_f32 v222, v222, v75\n"
  "v_smfmac_f32_16x16x64_bf16 v[104:107], v[24:27], v[164:171], v234\n"
  "v_cvt_pk_bf16_f32 v202, v202, v203\n"
  "v_fmac_f32 v223, v76, v76\n"
  "v_add_f32 v222, v222, v76\n"
  "v_fmac_f32 v223, v77, v77\n"
  "v_add_f32 v222, v222, v77\n"
  "ds_read_b64 v[164:165], v236 offset:16648\n"
  "ds_read_b64 v[166:167], v236 offset:16856\n"
  "ds_read_b64 v[168:169], v236 offset:17064\n"
  "ds_read_b64 v[170:171], v236 offset:17272\n"
  "v_mfma_f32_16x16x16_bf16 v[80:83], v[34:35], v[172:173], v[80:83]\n"
  "v_fmac_f32 v223, v78, v78\n"
  "v_add_f32 v222, v222, v78\n"
  "v_fmac_f32 v223, v79, v79\n"
  "v_add_f32 v222, v222, v79\n"
  "v_cvt_pk_bf16_f32 v203, v204, v205\n"
  "s_waitcnt lgkmcnt(4)\n"
  "v_smfmac_f32_16x16x64_bf16 v[84:87], v[40:43], v[144:151], v234\n"
  "ds_read_b64 v[172:173], v236 offset:17480\n"
  "buffer_store_dwordx2 v[202:203], v247, s[36:39], s85 offen sc1\n"
  "v_fmac_f32 v223, v80, v80\n"
  "v_add_f32 v222, v222, v80\n"
  "v_fmac_f32 v223, v81, v81\n"
  "v_add_f32 v222, v222, v81\n"
  "v_mfma_f32_16x16x16_bf16 v[84:87], v[46:47], v[152:153], v[84:87]\n"
  "v_fmac_f32 v223, v82, v82\n"
  "v_add_f32 v222, v222, v82\n"
  "v_fmac_f32 v223, v83, v83\n"
  "v_smfmac_f32_16x16x64_bf16 v[88:91], v[52:55], v[154:161], v234\n"
  "v_add_f32 v222, v222, v83\n"
  "v_smfmac_f32_16x16x64_bf16 v[206:209], v[230:233], v[174:181], v234\n"
  "s_waitcnt lgkmcnt(0)\n"
  "v_smfmac_f32_16x16x64_bf16 v[92:95], v[64:67], v[164:171], v234\n"
  "v_fmac_f32 v223, v84, v84\n"
  "v_add_f32 v222, v222, v84\n"
  "v_fmac_f32 v223, v85, v85\n"
  "v_add_f32 v222, v222, v85\n"
  "v_mfma_f32_16x16x16_bf16 v[88:91], v[58:59], v[162:163], v[88:91]\n"
  "v_fmac_f32 v223, v86, v86\n"
  "v_add_f32 v222, v222, v86\n"
  "v_fmac_f32 v223, v87, v87\n"
  "v_add_f32 v222, v222, v87\n"
  "v_mfma_f32_16x16x16_bf16 v[92:95], v[70:71], v[172:173], v[92:95]\n"
  "ds_read_b32 v253, v229\n"
  "s_nop 1\n"
  "v_fmac_f32 v223, v88, v88\n"
  "v_add_f32 v222, v222, v88\n"
  "v_fmac_f32 v223, v89, v89\n"
  "v_add_f32 v222, v222, v89\n"
  "v_fmac_f32 v223, v90, v90\n"
  "v_add_f32 v222, v222, v90\n"
  "v_fmac_f32 v223, v91, v91\n"
  "v_smfmac_f32_16x16x64_bf16 v[210:213], v[230:233], v[182:189], v234\n"
  "s_waitcnt lgkmcnt(0)\n"
  "v_readfirstlane_b32 s22, v253\n"
  "v_add_f32 v222, v222, v91\n"
  "v_fmac_f32 v223, v92, v92\n"
  "v_add_f32 v222, v222, v92\n"
  "v_fmac_f32 v223, v93, v93\n"
  "v_add_f32 v222, v222, v93\n"
  "v_fmac_f32 v223, v94, v94\n"
  "v_add_f32 v222, v222, v94\n"
  "v_fmac_f32 v223, v95, v95\n"
  "s_cmp_ge_u32 s22, 4864\ns_cbranch_scc0 HB10_slow_%=\nHB10_back_%=:\n"
  "ds_read_b64 v[174:175], v244\n"
  "ds_read_b64 v[176:177], v244 offset:6144\n"
  "ds_read_b64 v[178:179], v244 offset:12288\n"
  "ds_read_b64 v[180:181], v244 offset:18432\n"
  "ds_read_b64 v[182:183], v245\n"
  "ds_read_b64 v[184:185], v245 offset:6144\n"
  "ds_read_b64 v[186:187], v245 offset:12288\n"
  "ds_read_b64 v[188:189], v245 offset:18432\n"
  "s_waitcnt vmcnt(15)\n"
  "v_lshlrev_b32 v204, 16, v191\n"
  "v_and_b32 v205, s49, v191\n"
  "v_lshlrev_b32 v202, 16, v190\n"
  "v_and_b32 v203, s49, v190\n"
  "v_add_f32 v222, v222, v95\n"
  "v_mfma_f32_16x16x16_bf16 v[108:111], v[44:45], v[152:153], 0\n"
  "v_cvt_pk_bf16_f32 v206, v206, v207\n"
  "v_cvt_pk_bf16_f32 v210, v210, v211\n"
  "v_cvt_pk_bf16_f32 v207, v208, v209\n"
  "ds_write_b64 v249, v[222:223]\n"
  "buffer_store_dwordx2 v[206:207], v247, s[36:39], s85 offen offset:512 sc1\n"
  "s_waitcnt lgkmcnt(1)\n"
  "v_smfmac_f32_16x16x64_bf16 v[202:205], v[230:233], v[174:181], v234\n"
  "v_cvt_pk_bf16_f32 v211, v212, v213\n"
  "v_mfma_f32_16x16x16_bf16 v[112:115], v[56:57], v[162:163], 0\n"
  "ds_read_b64 v[174:175], v246\n"
  "ds_read_b64 v[176:177], v246 offset:6144\n"
  "ds_read_b64 v[178:179], v246 offset:12288\n"
  "ds_read_b64 v[180:181], v246 offset:18432\n"
  "ds_append v253 offset:62464\n"
  "buffer_store_dwordx2 v[210:211], v247, s[36:39], s85 offen offset:1024 sc1\n"
  "v_mfma_f32_16x16x16_bf16 v[116:119], v[68:69], v[172:173], 0\n"
  "s_waitcnt vmcnt(15)\n"
  "v_lshlrev_b32 v208, 16, v193\n"
  "v_and_b32 v209, s49, v193\n"
  "v_lshlrev_b32 v206, 16, v192\n"
  "v_and_b32 v207, s49, v192\n"
  "v_smfmac_f32_16x16x64_bf16 v[108:111], v[36:39], v[144:151], v234\n"
  "buffer_load_dwordx2 v[190:191], v247, s[28:31], s88 offen nt\n"
  "s_waitcnt vmcnt(14)\n"
  "v_lshlrev_b32 v212, 16, v195\n"
  "v_and_b32 v213, s49, v195\n"
  "v_lshlrev_b32 v210, 16, v194\n"
  "v_and_b32 v211, s49, v194\n"
  "v_smfmac_f32_16x16x64_bf16 v[112:115], v[48:51], v[154:161], v234\n"
  "buffer_load_dwordx2 v[192:193], v247, s[28:31], s88 offen offset:512 nt\n"
  "v_smfmac_f32_16x16x64_bf16 v[116:119], v[60:63], v[164:171], v234\n"
  "buffer_load_dwordx2 v[194:195], v247, s[28:31], s88 offen offset:1024 nt\n"
  "ds_read_b32 v253, v229\n"
  "ds_read_b64 v[214:215], v250\n"
  "ds_read_b64 v[216:217], v250 offset:512\n"
  "ds_read_b64 v[218:219], v250 offset:1024\n"
  "ds_read_b64 v[220:221], v250 offset:1536\n"
  "s_waitcnt lgkmcnt(0)\n"
  "v_readfirstlane_b32 s22, v253\n"
  "v_smfmac_f32_16x16x64_bf16 v[206:209], v[230:233], v[182:189], v234\n"
  "s_cmp_ge_u32 s22, 5120\ns_cbranch_scc0 HA11_slow_%=\nHA11_back_%=:\n"
  "v_add_f32 v214, v214, v216\n"
  "v_add_f32 v215, v215, v217\n"
  "v_add_f32 v218, v218, v220\n"
  "v_add_f32 v219, v219, v221\n"
  "v_add_f32 v214, v214, v218\n"
  "v_add_f32 v215, v215, v219\n"
  "v_fmaak_f32 v252, v235, v215, 0x3727c5ac\n"
  "v_mul_f32 v251, v235, v214\n"
  "v_fma_f32 v252, -v251, v251, v252\n"
  "v_max_f32 v252, 0x3727c5ac, v252\n"
  "v_rsq_f32 v252, v252\n"
  "s_nop 0\n"
  "v_mul_f32 v226, s47, v252\n"
  "v_mul_f32_e64 v227, -v251, v226\n"
  "v_fma_f32 v72, v72, v226, v227\n"
  "v_fma_f32 v73, v73, v226, v227\n"
  "v_fma_f32 v74, v74, v226, v227\n"
  "v_fma_f32 v75, v75, v226, v227\n"
  "v_fma_f32 v76, v76, v226, v227\n"
  "v_smfmac_f32_16x16x64_bf16 v[210:213], v[230:233], v[174:181], v234\n"
  "v_fma_f32 v77, v77, v226, v227\n"
  "v_fma_f32 v78, v78, v226, v227\n"
  "v_fma_f32 v79, v79, v226, v227\n"
  "v_cvt_pk_bf16_f32 v202, v202, v203\n"
  "v_cvt_pk_bf16_f32 v203, v204, v205\n"
  "v_cvt_pk_bf16_f32 v214, v72, v73\n"
  "v_cvt_pk_bf16_f32 v215, v74, v75\n"
  "v_cvt_pk_bf16_f32 v216, v76, v77\n"
  "v_cvt_pk_bf16_f32 v217, v78, v79\n"
  "buffer_store_dwordx2 v[202:203], v247, s[36:39], s86 offen sc1\n"
  "ds_write_b128 v248, v[214:217]\n"
  "s_waitcnt vmcnt(9)\n"
  "v_lshlrev_b32 v204, 16, v197\n"
  "v_and_b32 v205, s49, v197\n"
  "v_lshlrev_b32 v202, 16, v196\n"
  "v_and_b32 v203, s49, v196\n"
  "v_fma_f32 v80, v80, v226, v227\n"
  "v_fma_f32 v81, v81, v226, v227\n"
  "v_fma_f32 v82, v82, v226, v227\n"
  "v_fma_f32 v83, v83, v226, v227\n"
  "v_fma_f32 v84, v84, v226, v227\n"
  "v_fma_f32 v85, v85, v226, v227\n"
  "v_fma_f32 v86, v86, v226, v227\n"
  "v_fma_f32 v87, v87, v226, v227\n"
  "v_fma_f32 v88, v88, v226, v227\n"
  "v_fma_f32 v89, v89, v226, v227\n"
  "v_fma_f32 v90, v90, v226, v227\n"
  "v_fma_f32 v91, v91, v226, v227\n"
  "v_fma_f32 v92, v92, v226, v227\n"
  "v_fma_f32 v93, v93, v226, v227\n"
  "v_fma_f32 v94, v94, v226, v227\n"
  "v_fma_f32 v95, v95, v226, v227\n"
  "v_cvt_pk_bf16_f32 v218, v80, v81\n"
  "v_cvt_pk_bf16_f32 v219, v82, v83\n"
  "v_cvt_pk_bf16_f32 v220, v84, v85\n"
  "v_cvt_pk_bf16_f32 v221, v86, v87\n"
  "v_cvt_pk_bf16_f32 v214, v88, v89\n"
  "v_cvt_pk_bf16_f32 v215, v90, v91\n"
  "v_cvt_pk_bf16_f32 v216, v92, v93\n"
  "ds_read_b32 v253, v229\n"
  "ds_write_b128 v248, v[218:221] offset:256\n"
  "v_cvt_pk_bf16_f32 v217, v94, v95\n"
  "v_cvt_pk_bf16_f32 v206, v206, v207\n"
  "v_cvt_pk_bf16_f32 v210, v210, v211\n"
  "v_cvt_pk_bf16_f32 v207, v208, v209\n"
  "v_cvt_pk_bf16_f32 v211, v212, v213\n"
  "s_waitcnt vmcnt(8)\n"
  "v_lshlrev_b32 v208, 16, v199\n"
  "v_and_b32 v209, s49, v199\n"
  "buffer_store_dwordx2 v[206:207], v247, s[36:39], s86 offen offset:512 sc1\n"
  "ds_write_b128 v248, v[214:217] offset:512\n"
  "s_waitcnt lgkmcnt(1)\n"
  "v_readfirstlane_b32 s22, v253\n"
  "v_lshlrev_b32 v206, 16, v198\n"
  "v_and_b32 v207, s49, v198\n"
  "s_waitcnt vmcnt(8)\n"
  "v_lshlrev_b32 v212, 16, v201\n"
  "v_and_b32 v213, s49, v201\n"
  "s_cmp_ge_u32 s22, 5376\ns_cbranch_scc0 HB11_slow_%=\nHB11_back_%=:\n"
  "buffer_store_dwordx2 v[210:211], v247, s[36:39], s86 offen offset:1024 sc1\n"
  "ds_read_b64 v[182:183], v244 offset:24576\n"
  "ds_read_b64 v[184:185], v244 offset:30720\n"
  "ds_read_b64 v[186:187], v244 offset:36864\n"
  "ds_read_b64 v[188:189], v244 offset:43008\n"
  "ds_read_b64 v[174:175], v245 offset:24576\n"
  "ds_read_b64 v[176:177], v245 offset:30720\n"
  "ds_read_b64 v[178:179], v245 offset:36864\n"
  "ds_read_b64 v[180:181], v245 offset:43008\n"
  "v_lshlrev_b32 v210, 16, v200\n"
  "v_and_b32 v211, s49, v200\n"
  "buffer_load_dwordx2 v[196:197], v247, s[28:31], s89 offen nt\n"
  "s_waitcnt lgkmcnt(0)\n"
  "v_smfmac_f32_16x16x64_bf16 v[202:205], v[230:233], v[182:189], v234\n"
  "ds_read_b64 v[182:183], v246 offset:24576\n"
  "ds_read_b64 v[184:185], v246 offset:30720\n"
  "ds_read_b64 v[186:187], v246 offset:36864\n"
  "ds_read_b64 v[188:189], v246 offset:43008\n"
  "ds_append v253 offset:62464\n"
  "buffer_load_dwordx2 v[198:199], v247, s[28:31], s89 offen offset:512 nt\n"
  "buffer_load_dwordx2 v[200:201], v247, s[28:31], s89 offen offset:1024 nt\n"
  "ds_read_b64 v[152:153], v236 offset:34048\n"
  "ds_read_b64 v[144:145], v236 offset:33216\n"
  "ds_read_b64 v[146:147], v236 offset:33424\n"
  "ds_read_b64 v[148:149], v236 offset:33632\n"
  "ds_read_b64 v[150:151], v236 offset:33840\n"
  "ds_read_b64 v[162:163], v236 offset:34056\n"
  "ds_read_b64 v[154:155], v236 offset:33224\n"
  "ds_read_b64 v[156:157], v236 offset:33432\n"
  "ds_read_b64 v[158:159], v236 offset:33640\n"
  "ds_read_b64 v[160:161], v236 offset:33848\n"
  "s_waitcnt lgkmcnt(0)\n"
  "v_mfma_f32_16x16x16_bf16 v[120:123], v[8:9], v[152:153], 0\n"
  "ds_read_b64 v[172:173], v236 offset:34064\n"
  "ds_read_b64 v[164:165], v236 offset:33232\n"
  "ds_read_b64 v[166:167], v236 offset:33440\n"
  "v_mfma_f32_16x16x16_bf16 v[124:127], v[20:21], v[162:163], 0\n"
  "ds_read_b64 v[168:169], v236 offset:33648\n"
  "ds_read_b64 v[170:171], v236 offset:33856\n"
  "v_smfmac_f32_16x16x64_bf16 v[96:99], v[4:7], v[144:151], v234\n"
  "v_smfmac_f32_16x16x64_bf16 v[120:123], v[0:3], v[144:151], v234\n"
  "ds_read_b64 v[144:145], v236 offset:33240\n"
  "v_smfmac_f32_16x16x64_bf16 v[100:103], v[16:19], v[154:161], v234\n"
  "ds_read_b64 v[146:147], v236 offset:33448\n"
  "v_smfmac_f32_16x16x64_bf16 v[124:127], v[12:15], v[154:161], v234\n"
  "ds_read_b64 v[148:149], v236 offset:33656\n"
  "ds_read_b64 v[150:151], v236 offset:33864\n"
  "ds_read_b64 v[154:155], v236 offset:33248\n"
  "ds_read_b64 v[156:157], v236 offset:33456\n"
  "ds_read_b64 v[158:159], v236 offset:33664\n"
  "ds_read_b64 v[160:161], v236 offset:33872\n"
  "v_mfma_f32_16x16x16_bf16 v[96:99], v[10:11], v[152:153], v[96:99]\n"
  "ds_read_b64 v[152:153], v236 offset:34072\n"
  "s_waitcnt lgkmcnt(9)\n"
  "v_mfma_f32_16x16x16_bf16 v[128:131], v[32:33], v[172:173], 0\n"
  "v_mfma_f32_16x16x16_bf16 v[100:103], v[22:23], v[162:163], v[100:103]\n"
  "ds_read_b64 v[162:163], v236 offset:34080\n"
  "s_nop 2\n"
  "v_mul_f32 v223, v96, v96\n"
  "v_add_f32 v222, v96, v97\n"
  "v_fmac_f32 v223, v97, v97\n"
  "v_add_f32 v222, v222, v98\n"
  "v_fmac_f32 v223, v98, v98\n"
  "v_add_f32 v222, v222, v99\n"
  "v_fmac_f32 v223, v99, v99\n"
  "v_smfmac_f32_16x16x64_bf16 v[104:107], v[28:31], v[164:171], v234\n"
  "v_add_f32 v222, v222, v100\n"
  "v_fmac_f32 v223, v100, v100\n"
  "v_smfmac_f32_16x16x64_bf16 v[128:131], v[24:27], v[164:171], v234\n"
  "v_fmac_f32 v223, v101, v101\n"
  "v_add_f32 v222, v222, v101\n"
  "v_fmac_f32 v223, v102, v102\n"
  "v_mfma_f32_16x16x16_bf16 v[104:107], v[34:35], v[172:173], v[104:107]\n"
  "ds_read_b64 v[164:165], v236 offset:33256\n"
  "ds_read_b64 v[166:167], v236 offset:33464\n"
  "ds_read_b64 v[168:169], v236 offset:33672\n"
  "ds_read_b64 v[170:171], v236 offset:33880\n"
  "ds_read_b64 v[172:173], v236 offset:34088\n"
  "v_add_f32 v222, v222, v102\n"
  "v_fmac_f32 v223, v103, v103\n"
  "v_add_f32 v222, v222, v103\n"
  "v_fmac_f32 v223, v104, v104\n"
  "v_add_f32 v222, v222, v104\n"
  "v_fmac_f32 v223, v105, v105\n"
  "v_add_f32 v222, v222, v105\n"
  "v_fmac_f32 v223, v106, v106\n"
  "v_add_f32 v222, v222, v106\n"
  "s_waitcnt lgkmcnt(0)\n"
  "v_smfmac_f32_16x16x64_bf16 v[108:111], v[40:43], v[144:151], v234\n"
  "v_fmac_f32 v223, v107, v107\n"
  "v_add_f32 v222, v222, v107\n"
  "v_smfmac_f32_16x16x64_bf16 v[112:115], v[52:55], v[154:161], v234\n"
  "v_cvt_pk_bf16_f32 v202, v202, v203\n"
  "s_nop 1\n"
  "v_mfma_f32_16x16x16_bf16 v[108:111], v[46:47], v[152:153], v[108:111]\n"
  "v_cvt_pk_bf16_f32 v203, v204, v205\n"
  "s_waitcnt vmcnt(8)\n"
  "v_lshlrev_b32 v204, 16, v191\n"
  "v_smfmac_f32_16x16x64_bf16 v[206:209], v[230:233], v[174:181], v234\n"
  "v_and_b32 v205, s49, v191\n"
  "v_mfma_f32_16x16x16_bf16 v[112:115], v[58:59], v[162:163], v[112:115]\n"
  "buffer_store_dwordx2 v[202:203], v247, s[36:39], s87 offen sc1\n"
  "s_nop 0\n"
  "v_add_f32 v222, v222, v108\n"
  "v_fmac_f32 v223, v108, v108\n"
  "v_add_f32 v222, v222, v109\n"
  "v_fmac_f32 v223, v109, v109\n"
  "v_add_f32 v222, v222, v110\n"
  "v_fmac_f32 v223, v110, v110\n"
  "v_add_f32 v222, v222, v111\n"
  "v_smfmac_f32_16x16x64_bf16 v[116:119], v[64:67], v[164:171], v234\n"
  "ds_read_b32 v253, v229\n"
  "v_fmac_f32 v223, v111, v111\n"
  "v_add_f32 v222, v222, v112\n"
  "v_fmac_f32 v223, v112, v112\n"
  "v_add_f32 v222, v222, v113\n"
  "v_fmac_f32 v223, v113, v113\n"
  "v_mfma_f32_16x16x16_bf16 v[116:119], v[70:71], v[172:173], v[116:119]\n"
  "v_fmac_f32 v223, v114, v114\n"
  "v_add_f32 v222, v222, v114\n"
  "v_fmac_f32 v223, v115, v115\n"
  "s_waitcnt lgkmcnt(0)\n"
  "v_readfirstlane_b32 s22, v253\n"
  "v_add_f32 v222, v222, v115\n"
  "v_lshlrev_b32 v202, 16, v190\n"
  "v_and_b32 v203, s49, v190\n"
  "v_fmac_f32 v223, v116, v116\n"
  "v_add_f32 v222, v222, v116\n"
  "v_fmac_f32 v223, v117, v117\n"
  "v_smfmac_f32_16x16x64_bf16 v[210:213], v[230:233], v[182:189], v234\n"
  "s_cmp_ge_u32 s22, 5888\ns_cbranch_scc0 HB12_slow_%=\nHB12_back_%=:\n"
  "ds_read_b64 v[174:175], v244\n"
  "ds_read_b64 v[176:177], v244 offset:6144\n"
  "ds_read_b64 v[178:179], v244 offset:12288\n"
  "ds_read_b64 v[180:181], v244 offset:18432\n"
  "ds_read_b64 v[182:183], v245\n"
  "ds_read_b64 v[184:185], v245 offset:6144\n"
  "ds_read_b64 v[186:187], v245 offset:12288\n"
  "ds_read_b64 v[188:189], v245 offset:18432\n"
  "buffer_load_dwordx2 v[190:191], v247, s[28:31], s90 offen nt\n"
  "v_add_f32 v222, v222, v117\n"
  "v_fmac_f32 v223, v118, v118\n"
  "v_add_f32 v222, v222, v118\n"
  "v_fmac_f32 v223, v119, v119\n"
  "v_add_f32 v222, v222, v119\n"
  "v_mfma_f32_16x16x16_bf16 v[132:135], v[44:45], v[152:153], 0\n"
  "v_cvt_pk_bf16_f32 v206, v206, v207\n"
  "v_cvt_pk_bf16_f32 v210, v210, v211\n"
  "v_cvt_pk_bf16_f32 v207, v208, v209\n"
  "ds_write_b64 v249, v[222:223]\n"
  "buffer_store_dwordx2 v[206:207], v247, s[36:39], s87 offen offset:512 sc1\n"
  "s_waitcnt lgkmcnt(1)\n"
  "v_smfmac_f32_16x16x64_bf16 v[202:205], v[230:233], v[174:181], v234\n"
  "v_cvt_pk_bf16_f32 v211, v212, v213\n"
  "v_mfma_f32_16x16x16_bf16 v[136:139], v[56:57], v[162:163], 0\n"
  "ds_read_b64 v[174:175], v246\n"
  "ds_read_b64 v[176:177], v246 offset:6144\n"
  "ds_read_b64 v[178:179], v246 offset:12288\n"
  "ds_read_b64 v[180:181], v246 offset:18432\n"
  "ds_append v253 offset:62464\n"
  "buffer_store_dwordx2 v[210:211], v247, s[36:39], s87 offen offset:1024 sc1\n"
  "v_mfma_f32_16x16x16_bf16 v[140:143], v[68:69], v[172:173], 0\n"
  "s_waitcnt vmcnt(11)\n"
  "v_lshlrev_b32 v208, 16, v193\n"
  "v_and_b32 v209, s49, v193\n"
  "v_lshlrev_b32 v206, 16, v192\n"
  "v_and_b32 v207, s49, v192\n"
  "v_smfmac_f32_16x16x64_bf16 v[132:135], v[36:39], v[144:151], v234\n"
  "buffer_load_dwordx2 v[192:193], v247, s[28:31], s90 offen offset:512 nt\n"
  "s_waitcnt vmcnt(11)\n"
  "v_lshlrev_b32 v212, 16, v195\n"
  "v_and_b32 v213, s49, v195\n"
  "v_lshlrev_b32 v210, 16, v194\n"
  "v_and_b32 v211, s49, v194\n"
  "v_smfmac_f32_16x16x64_bf16 v[136:139], v[48:51], v[154:161], v234\n"
  "buffer_load_dwordx2 v[194:195], v247, s[28:31], s90 offen offset:1024 nt\n"
  "v_smfmac_f32_16x16x64_bf16 v[140:143], v[60:63], v[164:171], v234\n"
  "ds_read_b32 v253, v229\n"
  "ds_read_b64 v[214:215], v250\n"
  "ds_read_b64 v[216:217], v250 offset:512\n"
  "ds_read_b64 v[218:219], v250 offset:1024\n"
  "ds_read_b64 v[220:221], v250 offset:1536\n"
  "s_waitcnt lgkmcnt(0)\n"
  "v_readfirstlane_b32 s22, v253\n"
  "v_smfmac_f32_16x16x64_bf16 v[206:209], v[230:233], v[182:189], v234\n"
  "s_cmp_ge_u32 s22, 6144\ns_cbranch_scc0 HA13_slow_%=\nHA13_back_%=:\n"
  "v_add_f32 v214, v214, v216\n"
  "v_add_f32 v215, v215, v217\n"
  "v_add_f32 v218, v218, v220\n"
  "v_add_f32 v219, v219, v221\n"
  "v_add_f32 v214, v214, v218\n"
  "v_add_f32 v215, v215, v219\n"
  "v_fmaak_f32 v252, v235, v215, 0x3727c5ac\n"
  "v_mul_f32 v251, v235, v214\n"
  "v_fma_f32 v252, -v251, v251, v252\n"
  "v_max_f32 v252, 0x3727c5ac, v252\n"
  "v_rsq_f32 v252, v252\n"
  "s_nop 0\n"
  "v_mul_f32 v226, s47, v252\n"
  "v_mul_f32_e64 v227, -v251, v226\n"
  "v_fma_f32 v96, v96, v226, v227\n"
  "v_fma_f32 v97, v97, v226, v227\n"
  "v_fma_f32 v98, v98, v226, v227\n"
  "v_fma_f32 v99, v99, v226, v227\n"
  "v_fma_f32 v100, v100, v226, v227\n"
  "v_smfmac_f32_16x16x64_bf16 v[210:213], v[230:233], v[174:181], v234\n"
  "v_fma_f32 v101, v101, v226, v227\n"
  "v_fma_f32 v102, v102, v226, v227\n"
  "v_fma_f32 v103, v103, v226, v227\n"
  "v_cvt_pk_bf16_f32 v202, v202, v203\n"
  "v_cvt_pk_bf16_f32 v203, v204, v205\n"
  "v_cvt_pk_bf16_f32 v214, v96, v97\n"
  "v_cvt_pk_bf16_f32 v215, v98, v99\n"
  "v_cvt_pk_bf16_f32 v216, v100, v101\n"
  "v_cvt_pk_bf16_f32 v217, v102, v103\n"
  "buffer_store_dwordx2 v[202:203], v247, s[36:39], s88 offen sc1\n"
  "ds_write_b128 v248, v[214:217] offset:12288\n"
  "s_waitcnt vmcnt(9)\n"
  "v_lshlrev_b32 v204, 16, v197\n"
  "v_and_b32 v205, s49, v197\n"
  "v_lshlrev_b32 v202, 16, v196\n"
  "v_and_b32 v203, s49, v196\n"
  "v_fma_f32 v104, v104, v226, v227\n"
  "v_fma_f32 v105, v105, v226, v227\n"
  "v_fma_f32 v106, v106, v226, v227\n"
  "v_fma_f32 v107, v107, v226, v227\n"
  "v_fma_f32 v108, v108, v226, v227\n"
  "v_fma_f32 v109, v109, v226, v227\n"
  "v_fma_f32 v110, v110, v226, v227\n"
  "v_fma_f32 v111, v111, v226, v227\n"
  "v_fma_f32 v112, v112, v226, v227\n"
  "v_fma_f32 v113, v113, v226, v227\n"
  "v_fma_f32 v114, v114, v226, v227\n"
  "v_fma_f32 v115, v115, v226, v227\n"
  "v_fma_f32 v116, v116, v226, v227\n"
  "v_fma_f32 v117, v117, v226, v227\n"
  "v_fma_f32 v118, v118, v226, v227\n"
  "v_fma_f32 v119, v119, v226, v227\n"
  "v_cvt_pk_bf16_f32 v218, v104, v105\n"
  "v_cvt_pk_bf16_f32 v219, v106, v107\n"
  "v_cvt_pk_bf16_f32 v220, v108, v109\n"
  "v_cvt_pk_bf16_f32 v221, v110, v111\n"
  "v_cvt_pk_bf16_f32 v214, v112, v113\n"
  "v_cvt_pk_bf16_f32 v215, v114, v115\n"
  "v_cvt_pk_bf16_f32 v216, v116, v117\n"
  "ds_read_b32 v253, v229\n"
  "ds_write_b128 v248, v[218:221] offset:12544\n"
  "v_cvt_pk_bf16_f32 v217, v118, v119\n"
  "v_cvt_pk_bf16_f32 v206, v206, v207\n"
  "v_cvt_pk_bf16_f32 v210, v210, v211\n"
  "v_cvt_pk_bf16_f32 v207, v208, v209\n"
  "v_cvt_pk_bf16_f32 v211, v212, v213\n"
  "s_waitcnt vmcnt(8)\n"
  "v_lshlrev_b32 v208, 16, v199\n"
  "v_and_b32 v209, s49, v199\n"
  "buffer_store_dwordx2 v[206:207], v247, s[36:39], s88 offen offset:512 sc1\n"
  "ds_write_b128 v248, v[214:217] offset:12800\n"
  "s_waitcnt lgkmcnt(1)\n"
  "v_readfirstlane_b32 s22, v253\n"
  "v_lshlrev_b32 v206, 16, v198\n"
  "v_and_b32 v207, s49, v198\n"
  "s_waitcnt vmcnt(8)\n"
  "v_lshlrev_b32 v212, 16, v201\n"
  "v_and_b32 v213, s49, v201\n"
  "s_cmp_ge_u32 s22, 6400\ns_cbranch_scc0 HB13_slow_%=\nHB13_back_%=:\n"
  "buffer_store_dwordx2 v[210:211], v247, s[36:39], s88 offen offset:1024 sc1\n"
  "ds_read_b64 v[182:183], v244 offset:24576\n"
  "ds_read_b64 v[184:185], v244 offset:30720\n"
  "ds_read_b64 v[186:187], v244 offset:36864\n"
  "ds_read_b64 v[188:189], v244 offset:43008\n"
  "ds_read_b64 v[174:175], v245 offset:24576\n"
  "ds_read_b64 v[176:177], v245 offset:30720\n"
  "ds_read_b64 v[178:179], v245 offset:36864\n"
  "ds_read_b64 v[180:181], v245 offset:43008\n"
  "v_lshlrev_b32 v210, 16, v200\n"
  "v_and_b32 v211, s49, v200\n"
  "v_mov_b32 v237, 61440\n"
  "buffer_load_dwordx2 v[196:197], v247, s[28:31], s91 offen nt\n"
  "s_waitcnt lgkmcnt(0)\n"
  "v_smfmac_f32_16x16x64_bf16 v[202:205], v[230:233], v[182:189], v234\n"
  "ds_read_b64 v[182:183], v246 offset:24576\n"
  "ds_read_b64 v[184:185], v246 offset:30720\n"
  "ds_read_b64 v[186:187], v246 offset:36864\n"
  "ds_read_b64 v[188:189], v246 offset:43008\n"
  "ds_append v253 offset:62464\n"
  "buffer_load_dwordx2 v[198:199], v247, s[28:31], s91 offen offset:512 nt\n"
  "buffer_load_dwordx2 v[200:201], v247, s[28:31], s91 offen offset:1024 nt\n"
  "s_cmp_eq_u32 s56, 9\ns_cselect_b32 s59, -1, 0\ns_mov_b32 s58, 0\n"
  "v_cndmask_b32_e64 v237, v236, v237, s[58:59]\n"
  "ds_read_b64 v[144:145], v237\n"
  "ds_read_b64 v[146:147], v237 offset:208\n"
  "ds_read_b64 v[148:149], v237 offset:416\n"
  "ds_read_b64 v[150:151], v237 offset:624\n"
  "ds_read_b64 v[152:153], v237 offset:832\n"
  "ds_read_b64 v[154:155], v237 offset:8\n"
  "ds_read_b64 v[156:157], v237 offset:216\n"
  "ds_read_b64 v[158:159], v237 offset:424\n"
  "ds_read_b64 v[160:161], v237 offset:632\n"
  "ds_read_b64 v[162:163], v237 offset:840\n"
  "ds_read_b64 v[164:165], v237 offset:16\n"
  "ds_read_b64 v[166:167], v237 offset:224\n"
  "ds_read_b64 v[168:169], v237 offset:432\n"
  "ds_read_b64 v[170:171], v237 offset:640\n"
  "s_waitcnt lgkmcnt(0)\n"
  "v_smfmac_f32_16x16x64_bf16 v[120:123], v[4:7], v[144:151], v234\n"
  "ds_read_b64 v[172:173], v237 offset:848\n"
  "v_smfmac_f32_16x16x64_bf16 v[124:127], v[16:19], v[154:161], v234\n"
  "ds_read_b64 v[144:145], v237 offset:24\n"
  "v_smfmac_f32_16x16x64_bf16 v[128:131], v[28:31], v[164:171], v234\n"
  "ds_read_b64 v[146:147], v237 offset:232\n"
  "ds_read_b64 v[148:149], v237 offset:440\n"
  "ds_read_b64 v[150:151], v237 offset:648\n"
  "ds_read_b64 v[154:155], v237 offset:32\n"
  "ds_read_b64 v[156:157], v237 offset:240\n"
  "ds_read_b64 v[158:159], v237 offset:448\n"
  "ds_read_b64 v[160:161], v237 offset:656\n"
  "ds_read_b64 v[164:165], v237 offset:40\n"
  "ds_read_b64 v[166:167], v237 offset:248\n"
  "ds_read_b64 v[168:169], v237 offset:456\n"
  "ds_read_b64 v[170:171], v237 offset:664\n"
  "v_mfma_f32_16x16x16_bf16 v[120:123], v[10:11], v[152:153], v[120:123]\n"
  "ds_read_b64 v[152:153], v237 offset:856\n"
  "v_mfma_f32_16x16x16_bf16 v[124:127], v[22:23], v[162:163], v[124:127]\n"
  "ds_read_b64 v[162:163], v237 offset:864\n"
  "s_nop 4\n"
  "v_mul_f32 v223, v120, v120\n"
  "v_fmac_f32 v223, v121, v121\n"
  "v_add_f32 v222, v120, v121\n"
  "v_fmac_f32 v223, v122, v122\n"
  "v_add_f32 v222, v222, v122\n"
  "v_fmac_f32 v223, v123, v123\n"
  "v_add_f32 v222, v222, v123\n"
  "v_fmac_f32 v223, v124, v124\n"
  "v_add_f32 v222, v222, v124\n"
  "v_fmac_f32 v223, v125, v125\n"
  "v_add_f32 v222, v222, v125\n"
  "v_fmac_f32 v223, v126, v126\n"
  "v_add_f32 v222, v222, v126\n"
  "v_fmac_f32 v223, v127, v127\n"
  "v_add_f32 v222, v222, v127\n"
  "v_cvt_pk_bf16_f32 v202, v202, v203\n"
  "v_cvt_pk_bf16_f32 v203, v204, v205\n"
  "buffer_store_dwordx2 v[202:203], v247, s[36:39], s89 offen sc1\n"
  "s_waitcnt lgkmcnt(0)\n"
  "v_mfma_f32_16x16x16_bf16 v[128:131], v[34:35], v[172:173], v[128:131]\n"
  "ds_read_b64 v[172:173], v237 offset:872\n"
  "s_waitcnt vmcnt(11)\n"
  "v_lshlrev_b32 v204, 16, v191\n"
  "v_smfmac_f32_16x16x64_bf16 v[132:135], v[40:43], v[144:151], v234\n"
  "v_and_b32 v205, s49, v191\n"
  "v_smfmac_f32_16x16x64_bf16 v[136:139], v[52:55], v[154:161], v234\n"
  "s_nop 1\n"
  "v_add_f32 v222, v222, v128\n"
  "v_fmac_f32 v223, v128, v128\n"
  "v_add_f32 v222, v222, v129\n"
  "v_fmac_f32 v223, v129, v129\n"
  "v_mfma_f32_16x16x16_bf16 v[132:135], v[46:47], v[152:153], v[132:135]\n"
  "v_fmac_f32 v223, v130, v130\n"
  "v_add_f32 v222, v222, v130\n"
  "v_fmac_f32 v223, v131, v131\n"
  "v_smfmac_f32_16x16x64_bf16 v[206:209], v[230:233], v[174:181], v234\n"
  "v_add_f32 v222, v222, v131\n"
  "v_mfma_f32_16x16x16_bf16 v[136:139], v[58:59], v[162:163], v[136:139]\n"
  "v_lshlrev_b32 v202, 16, v190\n"
  "v_and_b32 v203, s49, v190\n"
  "v_fmac_f32 v223, v132, v132\n"
  "v_add_f32 v222, v222, v132\n"
  "v_fmac_f32 v223, v133, v133\n"
  "v_smfmac_f32_16x16x64_bf16 v[140:143], v[64:67], v[164:171], v234\n"
  "v_add_f32 v222, v222, v133\n"
  "v_fmac_f32 v223, v134, v134\n"
  "v_add_f32 v222, v222, v134\n"
  "v_fmac_f32 v223, v135, v135\n"
  "v_add_f32 v222, v222, v135\n"
  "v_fmac_f32 v223, v136, v136\n"
  "v_add_f32 v222, v222, v136\n"
  "v_smfmac_f32_16x16x64_bf16 v[210:213], v[230:233], v[182:189], v234\n"
  "ds_read_b32 v253, v229\n"
  "v_fmac_f32 v223, v137, v137\n"
  "v_add_f32 v222, v222, v137\n"
  "v_fmac_f32 v223, v138, v138\n"
  "v_add_f32 v222, v222, v138\n"
  "v_fmac_f32 v223, v139, v139\n"
  "v_add_f32 v222, v222, v139\n"
  "s_waitcnt lgkmcnt(1)\n"
  "v_mfma_f32_16x16x16_bf16 v[140:143], v[70:71], v[172:173], v[140:143]\n"
  "s_waitcnt lgkmcnt(0)\n"
  "v_readfirstlane_b32 s22, v253\n"
  "v_cvt_pk_bf16_f32 v206, v206, v207\n"
  "v_cvt_pk_bf16_f32 v210, v210, v211\n"
  "v_cvt_pk_bf16_f32 v207, v208, v209\n"
  "v_cvt_pk_bf16_f32 v211, v212, v213\n"
  "s_waitcnt vmcnt(8)\n"
  "v_lshlrev_b32 v208, 16, v193\n"
  "v_fmac_f32 v223, v140, v140\n"
  "v_add_f32 v222, v222, v140\n"
  "s_cmp_ge_u32 s22, 6912\ns_cbranch_scc0 HB14_slow_%=\nHB14_back_%=:\n"
  "ds_read_b64 v[174:175], v244\n"
  "ds_read_b64 v[176:177], v244 offset:6144\n"
  "ds_read_b64 v[178:179], v244 offset:12288\n"
  "ds_read_b64 v[180:181], v244 offset:18432\n"
  "ds_read_b64 v[182:183], v245\n"
  "ds_read_b64 v[184:185], v245 offset:6144\n"
  "ds_read_b64 v[186:187], v245 offset:12288\n"
  "ds_read_b64 v[188:189], v245 offset:18432\n"
  "buffer_store_dwordx2 v[206:207], v247, s[36:39], s89 offen offset:512 sc1\n"
  "v_fmac_f32 v223, v141, v141\n"
  "v_add_f32 v222, v222, v141\n"
  "v_fmac_f32 v223, v142, v142\n"
  "v_add_f32 v222, v222, v142\n"
  "v_fmac_f32 v223, v143, v143\n"
  "v_add_f32 v222, v222, v143\n"
  "v_and_b32 v209, s49, v193\n"
  "v_lshlrev_b32 v206, 16, v192\n"
  "v_and_b32 v207, s49, v192\n"
  "ds_write_b64 v249, v[222:223]\n"
  "s_waitcnt lgkmcnt(1)\n"
  "v_smfmac_f32_16x16x64_bf16 v[202:205], v[230:233], v[174:181], v234\n"
  "ds_read_b64 v[174:175], v246\n"
  "ds_read_b64 v[176:177], v246 offset:6144\n"
  "ds_read_b64 v[178:179], v246 offset:12288\n"
  "ds_read_b64 v[180:181], v246 offset:18432\n"
  "ds_append v253 offset:62464\n"
  "buffer_store_dwordx2 v[210:211], v247, s[36:39], s89 offen offset:1024 sc1\n"
  "s_waitcnt vmcnt(9)\n"
  "v_lshlrev_b32 v212, 16, v195\n"
  "v_and_b32 v213, s49, v195\n"
  "v_lshlrev_b32 v210, 16, v194\n"
  "v_and_b32 v211, s49, v194\n"
  "buffer_load_dwordx2 v[190:191], v247, s[28:31], s92 offen nt\n"
  "buffer_load_dwordx2 v[192:193], v247, s[28:31], s92 offen offset:512 nt\n"
  "buffer_load_dwordx2 v[194:195], v247, s[28:31], s92 offen offset:1024 nt\n"
  "s_nop 1\n"
  "ds_read_b32 v253, v229\n"
  "ds_read_b64 v[214:215], v250\n"
  "ds_read_b64 v[216:217], v250 offset:512\n"
  "ds_read_b64 v[218:219], v250 offset:1024\n"
  "ds_read_b64 v[220:221], v250 offset:1536\n"
  "s_waitcnt lgkmcnt(0)\n"
  "v_readfirstlane_b32 s22, v253\n"
  "s_cmp_ge_u32 s22, 7168\ns_cbranch_scc0 HA15_slow_%=\nHA15_back_%=:\n"
  "s_nop 15\n"
  "s_nop 15\n"
  "v_add_f32 v214, v214, v216\n"
  "v_add_f32 v215, v215, v217\n"
  "v_add_f32 v218, v218, v220\n"
  "v_add_f32 v219, v219, v221\n"
  "v_add_f32 v214, v214, v218\n"
  "v_add_f32 v215, v215, v219\n"
  "v_smfmac_f32_16x16x64_bf16 v[206:209], v[230:233], v[182:189], v234\n"
  "v_fmaak_f32 v252, v235, v215, 0x3727c5ac\n"
  "v_mul_f32 v251, v235, v214\n"
  "v_fma_f32 v252, -v251, v251, v252\n"
  "v_max_f32 v252, 0x3727c5ac, v252\n"
  "v_rsq_f32 v252, v252\n"
  "v_cvt_pk_bf16_f32 v202, v202, v203\n"
  "v_mul_f32 v226, s47, v252\n"
  "v_mul_f32_e64 v227, -v251, v226\n"
  "v_fma_f32 v120, v120, v226, v227\n"
  "v_fma_f32 v121, v121, v226, v227\n"
  "v_fma_f32 v122, v122, v226, v227\n"
  "v_fma_f32 v123, v123, v226, v227\n"
  "v_fma_f32 v124, v124, v226, v227\n"
  "v_fma_f32 v125, v125, v226, v227\n"
  "v_fma_f32 v126, v126, v226, v227\n"
  "v_smfmac_f32_16x16x64_bf16 v[210:213], v[230:233], v[174:181], v234\n"
  "v_fma_f32 v127, v127, v226, v227\n"
  "v_cvt_pk_bf16_f32 v203, v204, v205\n"
  "v_cvt_pk_bf16_f32 v214, v120, v121\n"
  "v_cvt_pk_bf16_f32 v215, v122, v123\n"
  "v_cvt_pk_bf16_f32 v216, v124, v125\n"
  "v_cvt_pk_bf16_f32 v217, v126, v127\n"
  "v_fma_f32 v128, v128, v226, v227\n"
  "v_fma_f32 v129, v129, v226, v227\n"
  "v_fma_f32 v130, v130, v226, v227\n"
  "buffer_store_dwordx2 v[202:203], v247, s[36:39], s90 offen sc1\n"
  "ds_write_b128 v248, v[214:217]\n"
  "s_waitcnt vmcnt(9)\n"
  "v_lshlrev_b32 v204, 16, v197\n"
  "v_and_b32 v205, s49, v197\n"
  "v_lshlrev_b32 v202, 16, v196\n"
  "v_and_b32 v203, s49, v196\n"
  "v_fma_f32 v131, v131, v226, v227\n"
  "v_fma_f32 v132, v132, v226, v227\n"
  "v_fma_f32 v133, v133, v226, v227\n"
  "v_fma_f32 v134, v134, v226, v227\n"
  "v_fma_f32 v135, v135, v226, v227\n"
  "buffer_load_dwordx2 v[196:197], v247, s[28:31], s93 offen nt\n"
  "v_fma_f32 v136, v136, v226, v227\n"
  "v_fma_f32 v137, v137, v226, v227\n"
  "v_fma_f32 v138, v138, v226, v227\n"
  "v_fma_f32 v139, v139, v226, v227\n"
  "v_fma_f32 v140, v140, v226, v227\n"
  "v_fma_f32 v141, v141, v226, v227\n"
  "v_fma_f32 v142, v142, v226, v227\n"
  "v_fma_f32 v143, v143, v226, v227\n"
  "v_cvt_pk_bf16_f32 v218, v128, v129\n"
  "v_cvt_pk_bf16_f32 v219, v130, v131\n"
  "v_cvt_pk_bf16_f32 v220, v132, v133\n"
  "v_cvt_pk_bf16_f32 v221, v134, v135\n"
  "v_cvt_pk_bf16_f32 v214, v136, v137\n"
  "v_cvt_pk_bf16_f32 v215, v138, v139\n"
  "v_cvt_pk_bf16_f32 v216, v140, v141\n"
  "v_cvt_pk_bf16_f32 v217, v142, v143\n"
  "v_cvt_pk_bf16_f32 v206, v206, v207\n"
  "v_cvt_pk_bf16_f32 v210, v210, v211\n"
  "ds_read_b32 v253, v229\n"
  "ds_write_b128 v248, v[218:221] offset:256\n"
  "ds_write_b128 v248, v[214:217] offset:512\n"
  "v_cvt_pk_bf16_f32 v207, v208, v209\n"
  "buffer_store_dwordx2 v[206:207], v247, s[36:39], s90 offen offset:512 sc1\n"
  "v_cvt_pk_bf16_f32 v211, v212, v213\n"
  "s_waitcnt vmcnt(10)\n"
  "v_lshlrev_b32 v208, 16, v199\n"
  "v_and_b32 v209, s49, v199\n"
  "buffer_store_dwordx2 v[210:211], v247, s[36:39], s90 offen offset:1024 sc1\n"
  "v_lshlrev_b32 v206, 16, v198\n"
  "v_and_b32 v207, s49, v198\n"
  "s_waitcnt vmcnt(10)\n"
  "v_lshlrev_b32 v212, 16, v201\n"
  "v_and_b32 v213, s49, v201\n"
  "v_lshlrev_b32 v210, 16, v200\n"
  "v_and_b32 v211, s49, v200\n"
  "s_waitcnt lgkmcnt(0)\n"
  "v_readfirstlane_b32 s22, v253\n"
  "s_cmp_ge_u32 s22, 7424\ns_cbranch_scc0 HB15_slow_%=\nHB15_back_%=:\n"
  "ds_read_b64 v[182:183], v244 offset:24576\n"
  "ds_read_b64 v[184:185], v244 offset:30720\n"
  "ds_read_b64 v[186:187], v244 offset:36864\n"
  "ds_read_b64 v[188:189], v244 offset:43008\n"
  "buffer_load_dwordx2 v[198:199], v247, s[28:31], s93 offen offset:512 nt\n"
  "ds_read_b64 v[174:175], v245 offset:24576\n"
  "ds_read_b64 v[176:177], v245 offset:30720\n"
  "ds_read_b64 v[178:179], v245 offset:36864\n"
  "ds_read_b64 v[180:181], v245 offset:43008\n"
  "s_waitcnt lgkmcnt(0)\n"
  "v_smfmac_f32_16x16x64_bf16 v[202:205], v[230:233], v[182:189], v234\n"
  "buffer_load_dwordx2 v[200:201], v247, s[28:31], s93 offen offset:1024 nt\n"
  "ds_read_b64 v[182:183], v246 offset:24576\n"
  "ds_read_b64 v[184:185], v246 offset:30720\n"
  "ds_read_b64 v[186:187], v246 offset:36864\n"
  "ds_read_b64 v[188:189], v246 offset:43008\n"
  "ds_append v253 offset:62464\n"
  "s_nop 15\n"
  "s_nop 15\n"
  "s_nop 15\n"
  "s_nop 15\n"
  "s_nop 15\n"
  "s_nop 15\n"
  "s_nop 15\n"
  "v_smfmac_f32_16x16x64_bf16 v[206:209], v[230:233], v[174:181], v234\n"
  "s_nop 15\n"
  "s_waitcnt lgkmcnt(0)\n"
  "v_smfmac_f32_16x16x64_bf16 v[210:213], v[230:233], v[182:189], v234\n"
  "v_cvt_pk_bf16_f32 v202, v202, v203\n"
  "v_cvt_pk_bf16_f32 v203, v204, v205\n"
  "s_waitcnt vmcnt(8)\n"
  "v_lshlrev_b32 v204, 16, v191\n"
  "v_and_b32 v205, s49, v191\n"
  "buffer_store_dwordx2 v[202:203], v247, s[36:39], s91 offen sc1\n"
  "v_lshlrev_b32 v202, 16, v190\n"
  "v_and_b32 v203, s49, v190\n"
  "s_nop 15\n"
  "s_nop 15\n"
  "s_nop 15\n"
  "s_nop 15\n"
  "s_nop 15\n"
  "s_nop 15\n"
  "s_nop 15\n"
  "s_nop 2\n"
  "ds_read_b32 v253, v229\n"
  "s_waitcnt lgkmcnt(0)\n"
  "v_readfirstlane_b32 s22, v253\n"
  "v_cvt_pk_bf16_f32 v206, v206, v207\n"
  "v_cvt_pk_bf16_f32 v210, v210, v211\n"
  "v_cvt_pk_bf16_f32 v207, v208, v209\n"
  "v_cvt_pk_bf16_f32 v211, v212, v213\n"
  "s_waitcnt vmcnt(8)\n"
  "v_lshlrev_b32 v208, 16, v193\n"
  "v_and_b32 v209, s49, v193\n"
  "s_cmp_ge_u32 s22, 7936\ns_cbranch_scc0 HB16_slow_%=\nHB16_back_%=:\n"
  "ds_read_b64 v[174:175], v244\n"
  "ds_read_b64 v[176:177], v244 offset:6144\n"
  "ds_read_b64 v[178:179], v244 offset:12288\n"
  "ds_read_b64 v[180:181], v244 offset:18432\n"
  "ds_read_b64 v[182:183], v245\n"
  "ds_read_b64 v[184:185], v245 offset:6144\n"
  "ds_read_b64 v[186:187], v245 offset:12288\n"
  "ds_read_b64 v[188:189], v245 offset:18432\n"
  "buffer_store_dwordx2 v[206:207], v247, s[36:39], s91 offen offset:512 sc1\n"
  "s_waitcnt vmcnt(8)\n"
  "v_lshlrev_b32 v212, 16, v195\n"
  "v_and_b32 v213, s49, v195\n"
  "v_lshlrev_b32 v206, 16, v192\n"
  "v_and_b32 v207, s49, v192\n"
  "s_waitcnt lgkmcnt(0)\n"
  "v_smfmac_f32_16x16x64_bf16 v[202:205], v[230:233], v[174:181], v234\n"
  "ds_read_b64 v[174:175], v246\n"
  "v_smfmac_f32_16x16x64_bf16 v[206:209], v[230:233], v[182:189], v234\n"
  "ds_read_b64 v[176:177], v246 offset:6144\n"
  "ds_read_b64 v[178:179], v246 offset:12288\n"
  "ds_read_b64 v[180:181], v246 offset:18432\n"
  "ds_append v253 offset:62464\n"
  "buffer_store_dwordx2 v[210:211], v247, s[36:39], s91 offen offset:1024 sc1\n"
  "v_lshlrev_b32 v210, 16, v194\n"
  "v_and_b32 v211, s49, v194\n"
  "v_cvt_pk_bf16_f32 v202, v202, v203\n"
  "v_cvt_pk_bf16_f32 v206, v206, v207\n"
  "v_cvt_pk_bf16_f32 v203, v204, v205\n"
  "v_cvt_pk_bf16_f32 v207, v208, v209\n"
  "s_waitcnt vmcnt(7)\n"
  "v_lshlrev_b32 v204, 16, v197\n"
  "v_and_b32 v205, s49, v197\n"
  "buffer_store_dwordx2 v[202:203], v247, s[36:39], s92 offen sc1\n"
  "v_lshlrev_b32 v202, 16, v196\n"
  "v_and_b32 v203, s49, v196\n"
  "s_waitcnt lgkmcnt(0)\n"
  "v_smfmac_f32_16x16x64_bf16 v[210:213], v[230:233], v[174:181], v234\n"
  "s_waitcnt vmcnt(5)\n"
  "v_lshlrev_b32 v208, 16, v199\n"
  "v_and_b32 v209, s49, v199\n"
  "buffer_store_dwordx2 v[206:207], v247, s[36:39], s92 offen offset:512 sc1\n"
  "v_lshlrev_b32 v206, 16, v198\n"
  "v_and_b32 v207, s49, v198\n"
  "s_nop 1\n"
  "v_cvt_pk_bf16_f32 v210, v210, v211\n"
  "v_cvt_pk_bf16_f32 v211, v212, v213\n"
  "buffer_store_dwordx2 v[210:211], v247, s[36:39], s92 offen offset:1024 sc1\n"
  "s_waitcnt vmcnt(6)\n"
  "v_lshlrev_b32 v212, 16, v201\n"
  "v_and_b32 v213, s49, v201\n"
  "v_lshlrev_b32 v210, 16, v200\n"
  "v_and_b32 v211, s49, v200\n"
  "buffer_load_dwordx2 v[190:191], v247, s[28:31], s94 offen nt\n"
  "buffer_load_dwordx2 v[192:193], v247, s[28:31], s94 offen offset:512 nt\n"
  "buffer_load_dwordx2 v[194:195], v247, s[28:31], s94 offen offset:1024 nt\n"
  "buffer_load_dwordx2 v[196:197], v247, s[28:31], s95 offen nt\n"
  "buffer_load_dwordx2 v[198:199], v247, s[28:31], s95 offen offset:512 nt\n"
  "buffer_load_dwordx2 v[200:201], v247, s[28:31], s95 offen offset:1024 nt\n"
  "s_nop 15\n"
  "s_nop 15\n"
  "s_nop 15\n"
  "s_nop 15\n"
  "s_nop 15\n"
  "s_nop 15\n"
  "s_nop 15\n"
  "s_nop 15\n"
  "s_nop 15\n"
  "s_nop 15\n"
  "s_nop 1\n"
  "ds_read_b32 v253, v229\n"
  "s_waitcnt lgkmcnt(0)\n"
  "v_readfirstlane_b32 s22, v253\n"
  "s_cmp_ge_u32 s22, 8448\ns_cbranch_scc0 HB17_slow_%=\nHB17_back_%=:\n"
  "ds_read_b64 v[144:145], v244 offset:24576\n"
  "ds_read_b64 v[146:147], v244 offset:30720\n"
  "ds_read_b64 v[148:149], v244 offset:36864\n"
  "ds_read_b64 v[150:151], v244 offset:43008\n"
  "ds_read_b64 v[152:153], v245 offset:24576\n"
  "ds_read_b64 v[154:155], v245 offset:30720\n"
  "ds_read_b64 v[156:157], v245 offset:36864\n"
  "ds_read_b64 v[158:159], v245 offset:43008\n"
  "ds_read_b64 v[160:161], v246 offset:24576\n"
  "ds_read_b64 v[162:163], v246 offset:30720\n"
  "ds_read_b64 v[164:165], v246 offset:36864\n"
  "ds_read_b64 v[166:167], v246 offset:43008\n"
  "ds_append v253 offset:62464\n"
  "s_nop 15\n"
  "s_nop 15\n"
  "s_nop 15\n"
  "s_nop 15\n"
  "s_nop 15\n"
  "s_nop 15\n"
  "s_nop 15\n"
  "s_nop 15\n"
  "s_nop 15\n"
  "s_nop 15\n"
  "s_nop 15\n"
  "s_nop 15\n"
  "s_nop 15\n"
  "s_nop 15\n"
  "s_nop 15\n"
  "s_waitcnt lgkmcnt(0)\n"
  "v_smfmac_f32_16x16x64_bf16 v[202:205], v[230:233], v[144:151], v234\n"
  "s_nop 15\n"
  "v_smfmac_f32_16x16x64_bf16 v[206:209], v[230:233], v[152:159], v234\n"
  "v_cvt_pk_bf16_f32 v202, v202, v203\n"
  "v_cvt_pk_bf16_f32 v203, v204, v205\n"
  "buffer_store_dwordx2 v[202:203], v247, s[36:39], s93 offen sc1\n"
  "s_nop 4\n"
  "v_cvt_pk_bf16_f32 v206, v206, v207\n"
  "v_cvt_pk_bf16_f32 v207, v208, v209\n"
  "v_smfmac_f32_16x16x64_bf16 v[210:213], v[230:233], v[160:167], v234\n"
  "s_nop 7\n"
  "v_cvt_pk_bf16_f32 v210, v210, v211\n"
  "buffer_store_dwordx2 v[206:207], v247, s[36:39], s93 offen offset:512 sc1\n"
  "v_cvt_pk_bf16_f32 v211, v212, v213\n"
  "s_waitcnt vmcnt(7)\n"
  "v_lshlrev_b32 v204, 16, v191\n"
  "v_and_b32 v205, s49, v191\n"
  "v_lshlrev_b32 v202, 16, v190\n"
  "v_and_b32 v203, s49, v190\n"
  "s_waitcnt vmcnt(6)\n"
  "v_lshlrev_b32 v208, 16, v193\n"
  "buffer_store_dwordx2 v[210:211], v247, s[36:39], s93 offen offset:1024 sc1\n"
  "v_and_b32 v209, s49, v193\n"
  "v_lshlrev_b32 v206, 16, v192\n"
  "v_and_b32 v207, s49, v192\n"
  "s_waitcnt vmcnt(6)\n"
  "v_lshlrev_b32 v212, 16, v195\n"
  "v_and_b32 v213, s49, v195\n"
  "v_lshlrev_b32 v210, 16, v194\n"
  "v_and_b32 v211, s49, v194\n"
  "ds_read_b32 v253, v229\n"
  "s_waitcnt lgkmcnt(0)\n"
  "v_readfirstlane_b32 s22, v253\n"
  "s_cmp_ge_u32 s22, 8960\ns_cbranch_scc0 HB18_slow_%=\nHB18_back_%=:\n"
  "ds_read_b64 v[168:169], v244\n"
  "ds_read_b64 v[170:171], v244 offset:6144\n"
  "ds_read_b64 v[172:173], v244 offset:12288\n"
  "ds_read_b64 v[174:175], v244 offset:18432\n"
  "ds_read_b64 v[176:177], v245\n"
  "ds_read_b64 v[178:179], v245 offset:6144\n"
  "ds_read_b64 v[180:181], v245 offset:12288\n"
  "ds_read_b64 v[182:183], v245 offset:18432\n"
  "ds_read_b64 v[144:145], v246\n"
  "ds_read_b64 v[146:147], v246 offset:6144\n"
  "ds_read_b64 v[148:149], v246 offset:12288\n"
  "ds_read_b64 v[150:151], v246 offset:18432\n"
  "s_waitcnt lgkmcnt(8)\n"
  "v_smfmac_f32_16x16x64_bf16 v[202:205], v[230:233], v[168:175], v234\n"
  "s_nop 7\n"
  "v_cvt_pk_bf16_f32 v202, v202, v203\n"
  "v_cvt_pk_bf16_f32 v203, v204, v205\n"
  "buffer_store_dwordx2 v[202:203], v247, s[36:39], s94 offen sc1\n"
  "s_waitcnt vmcnt(6)\n"
  "v_lshlrev_b32 v204, 16, v197\n"
  "v_and_b32 v205, s49, v197\n"
  "v_lshlrev_b32 v202, 16, v196\n"
  "v_and_b32 v203, s49, v196\n"
  "s_waitcnt lgkmcnt(0)\n"
  "v_smfmac_f32_16x16x64_bf16 v[206:209], v[230:233], v[176:183], v234\n"
  "v_smfmac_f32_16x16x64_bf16 v[210:213], v[230:233], v[144:151], v234\n"
  "s_nop 6\n"
  "v_cvt_pk_bf16_f32 v206, v206, v207\n"
  "v_cvt_pk_bf16_f32 v210, v210, v211\n"
  "v_cvt_pk_bf16_f32 v207, v208, v209\n"
  "v_cvt_pk_bf16_f32 v211, v212, v213\n"
  "s_waitcnt vmcnt(5)\n"
  "v_lshlrev_b32 v208, 16, v199\n"
  "v_and_b32 v209, s49, v199\n"
  "buffer_store_dwordx2 v[206:207], v247, s[36:39], s94 offen offset:512 sc1\n"
  "v_lshlrev_b32 v206, 16, v198\n"
  "v_and_b32 v207, s49, v198\n"
  "s_waitcnt vmcnt(5)\n"
  "v_lshlrev_b32 v212, 16, v201\n"
  "v_and_b32 v213, s49, v201\n"
  "buffer_store_dwordx2 v[210:211], v247, s[36:39], s94 offen offset:1024 sc1\n"
  "v_lshlrev_b32 v210, 16, v200\n"
  "v_and_b32 v211, s49, v200\n"
  "s_barrier\n"
  "ds_read_b64 v[152:153], v244 offset:24576\n"
  "ds_read_b64 v[154:155], v244 offset:30720\n"
  "ds_read_b64 v[156:157], v244 offset:36864\n"
  "ds_read_b64 v[158:159], v244 offset:43008\n"
  "ds_read_b64 v[160:161], v245 offset:24576\n"
  "ds_read_b64 v[162:163], v245 offset:30720\n"
  "ds_read_b64 v[164:165], v245 offset:36864\n"
  "ds_read_b64 v[166:167], v245 offset:43008\n"
  "ds_read_b64 v[168:169], v246 offset:24576\n"
  "ds_read_b64 v[170:171], v246 offset:30720\n"
  "ds_read_b64 v[172:173], v246 offset:36864\n"
  "ds_read_b64 v[174:175], v246 offset:43008\n"
  "s_waitcnt lgkmcnt(8)\n"
  "v_smfmac_f32_16x16x64_bf16 v[202:205], v[230:233], v[152:159], v234\n"
  "s_waitcnt lgkmcnt(4)\n"
  "v_smfmac_f32_16x16x64_bf16 v[206:209], v[230:233], v[160:167], v234\n"
  "s_waitcnt lgkmcnt(0)\n"
  "v_smfmac_f32_16x16x64_bf16 v[210:213], v[230:233], v[168:175], v234\n"
  "s_nop 3\n"
  "v_cvt_pk_bf16_f32 v202, v202, v203\n"
  "s_nop 0\n"
  "v_cvt_pk_bf16_f32 v206, v206, v207\n"
  "v_cvt_pk_bf16_f32 v203, v204, v205\n"
  "buffer_store_dwordx2 v[202:203], v247, s[36:39], s95 offen sc1\n"
  "v_cvt_pk_bf16_f32 v210, v210, v211\n"
  "v_cvt_pk_bf16_f32 v207, v208, v209\n"
  "v_cvt_pk_bf16_f32 v211, v212, v213\n"
  "buffer_store_dwordx2 v[206:207], v247, s[36:39], s95 offen offset:512 sc1\n"
  "buffer_store_dwordx2 v[210:211], v247, s[36:39], s95 offen offset:1024 sc1\n"
  "s_endpgm\n"
  "HB2_slow_%=:\n"
  "s_mov_b32 s48, 0x40000\n"
  "HB2_spin_%=:\n"
  "s_sleep 0\n"
  "ds_read_b32 v253, v229\n"
  "s_waitcnt lgkmcnt(0)\n"
  "v_readfirstlane_b32 s22, v253\n"
  "s_nop 3\n"
  "s_cmp_ge_u32 s22, 768\n"
  "s_cbranch_scc1 HB2_back_%=\n"
  "s_sub_u32 s48, s48, 1\n"
  "s_cbranch_scc0 HB2_spin_%=\n"
  "s_branch HB2_back_%=\n"
  "HA3_slow_%=:\n"
  "s_mov_b32 s48, 0x40000\n"
  "HA3_spin_%=:\n"
  "s_sleep 0\n"
  "ds_read_b32 v253, v229\n"
  "ds_read_b64 v[220:221], v250 offset:1536\n"
  "ds_read_b64 v[218:219], v250 offset:1024\n"
  "ds_read_b64 v[216:217], v250 offset:512\n"
  "ds_read_b64 v[214:215], v250\n"
  "s_waitcnt lgkmcnt(0)\n"
  "v_readfirstlane_b32 s22, v253\n"
  "s_nop 3\n"
  "s_cmp_ge_u32 s22, 1024\n"
  "s_cbranch_scc1 HA3_back_%=\n"
  "s_sub_u32 s48, s48, 1\n"
  "s_cbranch_scc0 HA3_spin_%=\n"
  "s_branch HA3_back_%=\n"
  "HB3_slow_%=:\n"
  "s_mov_b32 s48, 0x40000\n"
  "HB3_spin_%=:\n"
  "s_sleep 0\n"
  "ds_read_b32 v253, v229\n"
  "s_waitcnt lgkmcnt(0)\n"
  "v_readfirstlane_b32 s22, v253\n"
  "s_nop 3\n"
  "s_cmp_ge_u32 s22, 1280\n"
  "s_cbranch_scc1 HB3_back_%=\n"
  "s_sub_u32 s48, s48, 1\n"
  "s_cbranch_scc0 HB3_spin_%=\n"
  "s_branch HB3_back_%=\n"
  "HB4_slow_%=:\n"
  "s_mov_b32 s48, 0x40000\n"
  "HB4_spin_%=:\n"
  "s_sleep 0\n"
  "ds_read_b32 v253, v229\n"
  "s_waitcnt lgkmcnt(0)\n"
  "v_readfirstlane_b32 s22, v253\n"
  "s_nop 3\n"
  "s_cmp_ge_u32 s22, 1792\n"
  "s_cbranch_scc1 HB4_back_%=\n"
  "s_sub_u32 s48, s48, 1\n"
  "s_cbranch_scc0 HB4_spin_%=\n"
  "s_branch HB4_back_%=\n"
  "HA5_slow_%=:\n"
  "s_mov_b32 s48, 0x40000\n"
  "HA5_spin_%=:\n"
  "s_sleep 0\n"
  "ds_read_b32 v253, v229\n"
  "ds_read_b64 v[220:221], v250 offset:1536\n"
  "ds_read_b64 v[218:219], v250 offset:1024\n"
  "ds_read_b64 v[216:217], v250 offset:512\n"
  "ds_read_b64 v[214:215], v250\n"
  "s_waitcnt lgkmcnt(0)\n"
  "v_readfirstlane_b32 s22, v253\n"
  "s_nop 3\n"
  "s_cmp_ge_u32 s22, 2048\n"
  "s_cbranch_scc1 HA5_back_%=\n"
  "s_sub_u32 s48, s48, 1\n"
  "s_cbranch_scc0 HA5_spin_%=\n"
  "s_branch HA5_back_%=\n"
  "HB5_slow_%=:\n"
  "s_mov_b32 s48, 0x40000\n"
  "HB5_spin_%=:\n"
  "s_sleep 0\n"
  "ds_read_b32 v253, v229\n"
  "s_waitcnt lgkmcnt(0)\n"
  "v_readfirstlane_b32 s22, v253\n"
  "s_nop 3\n"
  "s_cmp_ge_u32 s22, 2304\n"
  "s_cbranch_scc1 HB5_back_%=\n"
  "s_sub_u32 s48, s48, 1\n"
  "s_cbranch_scc0 HB5_spin_%=\n"
  "s_branch HB5_back_%=\n"
  "HB6_slow_%=:\n"
  "s_mov_b32 s48, 0x40000\n"
  "HB6_spin_%=:\n"
  "s_sleep 0\n"
  "ds_read_b32 v253, v229\n"
  "s_waitcnt lgkmcnt(0)\n"
  "v_readfirstlane_b32 s22, v253\n"
  "s_nop 3\n"
  "s_cmp_ge_u32 s22, 2816\n"
  "s_cbranch_scc1 HB6_back_%=\n"
  "s_sub_u32 s48, s48, 1\n"
  "s_cbranch_scc0 HB6_spin_%=\n"
  "s_branch HB6_back_%=\n"
  "HA7_slow_%=:\n"
  "s_mov_b32 s48, 0x40000\n"
  "HA7_spin_%=:\n"
  "s_sleep 0\n"
  "ds_read_b32 v253, v229\n"
  "ds_read_b64 v[220:221], v250 offset:1536\n"
  "ds_read_b64 v[218:219], v250 offset:1024\n"
  "ds_read_b64 v[216:217], v250 offset:512\n"
  "ds_read_b64 v[214:215], v250\n"
  "s_waitcnt lgkmcnt(0)\n"
  "v_readfirstlane_b32 s22, v253\n"
  "s_nop 3\n"
  "s_cmp_ge_u32 s22, 3072\n"
  "s_cbranch_scc1 HA7_back_%=\n"
  "s_sub_u32 s48, s48, 1\n"
  "s_cbranch_scc0 HA7_spin_%=\n"
  "s_branch HA7_back_%=\n"
  "HB7_slow_%=:\n"
  "s_mov_b32 s48, 0x40000\n"
  "HB7_spin_%=:\n"
  "s_sleep 0\n"
  "ds_read_b32 v253, v229\n"
  "s_waitcnt lgkmcnt(0)\n"
  "v_readfirstlane_b32 s22, v253\n"
  "s_nop 3\n"
  "s_cmp_ge_u32 s22, 3328\n"
  "s_cbranch_scc1 HB7_back_%=\n"
  "s_sub_u32 s48, s48, 1\n"
  "s_cbranch_scc0 HB7_spin_%=\n"
  "s_branch HB7_back_%=\n"
  "HB8_slow_%=:\n"
  "s_mov_b32 s48, 0x40000\n"
  "HB8_spin_%=:\n"
  "s_sleep 0\n"
  "ds_read_b32 v253, v229\n"
  "s_waitcnt lgkmcnt(0)\n"
  "v_readfirstlane_b32 s22, v253\n"
  "s_nop 3\n"
  "s_cmp_ge_u32 s22, 3840\n"
  "s_cbranch_scc1 HB8_back_%=\n"
  "s_sub_u32 s48, s48, 1\n"
  "s_cbranch_scc0 HB8_spin_%=\n"
  "s_branch HB8_back_%=\n"
  "HA9_slow_%=:\n"
  "s_mov_b32 s48, 0x40000\n"
  "HA9_spin_%=:\n"
  "s_sleep 0\n"
  "ds_read_b32 v253, v229\n"
  "ds_read_b64 v[220:221], v250 offset:1536\n"
  "ds_read_b64 v[218:219], v250 offset:1024\n"
  "ds_read_b64 v[216:217], v250 offset:512\n"
  "ds_read_b64 v[214:215], v250\n"
  "s_waitcnt lgkmcnt(0)\n"
  "v_readfirstlane_b32 s22, v253\n"
  "s_nop 3\n"
  "s_cmp_ge_u32 s22, 4096\n"
  "s_cbranch_scc1 HA9_back_%=\n"
  "s_sub_u32 s48, s48, 1\n"
  "s_cbranch_scc0 HA9_spin_%=\n"
  "s_branch HA9_back_%=\n"
  "HB9_slow_%=:\n"
  "s_mov_b32 s48, 0x40000\n"
  "HB9_spin_%=:\n"
  "s_sleep 0\n"
  "ds_read_b32 v253, v229\n"
  "s_waitcnt lgkmcnt(0)\n"
  "v_readfirstlane_b32 s22, v253\n"
  "s_nop 3\n"
  "s_cmp_ge_u32 s22, 4352\n"
  "s_cbranch_scc1 HB9_back_%=\n"
  "s_sub_u32 s48, s48, 1\n"
  "s_cbranch_scc0 HB9_spin_%=\n"
  "s_branch HB9_back_%=\n"
  "HB10_slow_%=:\n"
  "s_mov_b32 s48, 0x40000\n"
  "HB10_spin_%=:\n"
  "s_sleep 0\n"
  "ds_read_b32 v253, v229\n"
  "s_waitcnt lgkmcnt(0)\n"
  "v_readfirstlane_b32 s22, v253\n"
  "s_nop 3\n"
  "s_cmp_ge_u32 s22, 4864\n"
  "s_cbranch_scc1 HB10_back_%=\n"
  "s_sub_u32 s48, s48, 1\n"
  "s_cbranch_scc0 HB10_spin_%=\n"
  "s_branch HB10_back_%=\n"
  "HA11_slow_%=:\n"
  "s_mov_b32 s48, 0x40000\n"
  "HA11_spin_%=:\n"
  "s_sleep 0\n"
  "ds_read_b32 v253, v229\n"
  "ds_read_b64 v[220:221], v250 offset:1536\n"
  "ds_read_b64 v[218:219], v250 offset:1024\n"
  "ds_read_b64 v[216:217], v250 offset:512\n"
  "ds_read_b64 v[214:215], v250\n"
  "s_waitcnt lgkmcnt(0)\n"
  "v_readfirstlane_b32 s22, v253\n"
  "s_nop 3\n"
  "s_cmp_ge_u32 s22, 5120\n"
  "s_cbranch_scc1 HA11_back_%=\n"
  "s_sub_u32 s48, s48, 1\n"
  "s_cbranch_scc0 HA11_spin_%=\n"
  "s_branch HA11_back_%=\n"
  "HB11_slow_%=:\n"
  "s_mov_b32 s48, 0x40000\n"
  "HB11_spin_%=:\n"
  "s_sleep 0\n"
  "ds_read_b32 v253, v229\n"
  "s_waitcnt lgkmcnt(0)\n"
  "v_readfirstlane_b32 s22, v253\n"
  "s_nop 3\n"
  "s_cmp_ge_u32 s22, 5376\n"
  "s_cbranch_scc1 HB11_back_%=\n"
  "s_sub_u32 s48, s48, 1\n"
  "s_cbranch_scc0 HB11_spin_%=\n"
  "s_branch HB11_back_%=\n"
  "HB12_slow_%=:\n"
  "s_mov_b32 s48, 0x40000\n"
  "HB12_spin_%=:\n"
  "s_sleep 0\n"
  "ds_read_b32 v253, v229\n"
  "s_waitcnt lgkmcnt(0)\n"
  "v_readfirstlane_b32 s22, v253\n"
  "s_nop 3\n"
  "s_cmp_ge_u32 s22, 5888\n"
  "s_cbranch_scc1 HB12_back_%=\n"
  "s_sub_u32 s48, s48, 1\n"
  "s_cbranch_scc0 HB12_spin_%=\n"
  "s_branch HB12_back_%=\n"
  "HA13_slow_%=:\n"
  "s_mov_b32 s48, 0x40000\n"
  "HA13_spin_%=:\n"
  "s_sleep 0\n"
  "ds_read_b32 v253, v229\n"
  "ds_read_b64 v[220:221], v250 offset:1536\n"
  "ds_read_b64 v[218:219], v250 offset:1024\n"
  "ds_read_b64 v[216:217], v250 offset:512\n"
  "ds_read_b64 v[214:215], v250\n"
  "s_waitcnt lgkmcnt(0)\n"
  "v_readfirstlane_b32 s22, v253\n"
  "s_nop 3\n"
  "s_cmp_ge_u32 s22, 6144\n"
  "s_cbranch_scc1 HA13_back_%=\n"
  "s_sub_u32 s48, s48, 1\n"
  "s_cbranch_scc0 HA13_spin_%=\n"
  "s_branch HA13_back_%=\n"
  "HB13_slow_%=:\n"
  "s_mov_b32 s48, 0x40000\n"
  "HB13_spin_%=:\n"
  "s_sleep 0\n"
  "ds_read_b32 v253, v229\n"
  "s_waitcnt lgkmcnt(0)\n"
  "v_readfirstlane_b32 s22, v253\n"
  "s_nop 3\n"
  "s_cmp_ge_u32 s22, 6400\n"
  "s_cbranch_scc1 HB13_back_%=\n"
  "s_sub_u32 s48, s48, 1\n"
  "s_cbranch_scc0 HB13_spin_%=\n"
  "s_branch HB13_back_%=\n"
  "HB14_slow_%=:\n"
  "s_mov_b32 s48, 0x40000\n"
  "HB14_spin_%=:\n"
  "s_sleep 0\n"
  "ds_read_b32 v253, v229\n"
  "s_waitcnt lgkmcnt(0)\n"
  "v_readfirstlane_b32 s22, v253\n"
  "s_nop 3\n"
  "s_cmp_ge_u32 s22, 6912\n"
  "s_cbranch_scc1 HB14_back_%=\n"
  "s_sub_u32 s48, s48, 1\n"
  "s_cbranch_scc0 HB14_spin_%=\n"
  "s_branch HB14_back_%=\n"
  "HA15_slow_%=:\n"
  "s_mov_b32 s48, 0x40000\n"
  "HA15_spin_%=:\n"
  "s_sleep 0\n"
  "ds_read_b32 v253, v229\n"
  "ds_read_b64 v[220:221], v250 offset:1536\n"
  "ds_read_b64 v[218:219], v250 offset:1024\n"
  "ds_read_b64 v[216:217], v250 offset:512\n"
  "ds_read_b64 v[214:215], v250\n"
  "s_waitcnt lgkmcnt(0)\n"
  "v_readfirstlane_b32 s22, v253\n"
  "s_nop 3\n"
  "s_cmp_ge_u32 s22, 7168\n"
  "s_cbranch_scc1 HA15_back_%=\n"
  "s_sub_u32 s48, s48, 1\n"
  "s_cbranch_scc0 HA15_spin_%=\n"
  "s_branch HA15_back_%=\n"
  "HB15_slow_%=:\n"
  "s_mov_b32 s48, 0x40000\n"
  "HB15_spin_%=:\n"
  "s_sleep 0\n"
  "ds_read_b32 v253, v229\n"
  "s_waitcnt lgkmcnt(0)\n"
  "v_readfirstlane_b32 s22, v253\n"
  "s_nop 3\n"
  "s_cmp_ge_u32 s22, 7424\n"
  "s_cbranch_scc1 HB15_back_%=\n"
  "s_sub_u32 s48, s48, 1\n"
  "s_cbranch_scc0 HB15_spin_%=\n"
  "s_branch HB15_back_%=\n"
  "HB16_slow_%=:\n"
  "s_mov_b32 s48, 0x40000\n"
  "HB16_spin_%=:\n"
  "s_sleep 0\n"
  "ds_read_b32 v253, v229\n"
  "s_waitcnt lgkmcnt(0)\n"
  "v_readfirstlane_b32 s22, v253\n"
  "s_nop 3\n"
  "s_cmp_ge_u32 s22, 7936\n"
  "s_cbranch_scc1 HB16_back_%=\n"
  "s_sub_u32 s48, s48, 1\n"
  "s_cbranch_scc0 HB16_spin_%=\n"
  "s_branch HB16_back_%=\n"
  "HB17_slow_%=:\n"
  "s_mov_b32 s48, 0x40000\n"
  "HB17_spin_%=:\n"
  "s_sleep 0\n"
  "ds_read_b32 v253, v229\n"
  "s_waitcnt lgkmcnt(0)\n"
  "v_readfirstlane_b32 s22, v253\n"
  "s_nop 3\n"
  "s_cmp_ge_u32 s22, 8448\n"
  "s_cbranch_scc1 HB17_back_%=\n"
  "s_sub_u32 s48, s48, 1\n"
  "s_cbranch_scc0 HB17_spin_%=\n"
  "s_branch HB17_back_%=\n"
  "HB18_slow_%=:\n"
  "s_mov_b32 s48, 0x40000\n"
  "HB18_spin_%=:\n"
  "s_sleep 0\n"
  "ds_read_b32 v253, v229\n"
  "s_waitcnt lgkmcnt(0)\n"
  "v_readfirstlane_b32 s22, v253\n"
  "s_nop 3\n"
  "s_cmp_ge_u32 s22, 8960\n"
  "s_cbranch_scc1 HB18_back_%=\n"
  "s_sub_u32 s48, s48, 1\n"
  "s_cbranch_scc0 HB18_spin_%=\n"
  "s_branch HB18_back_%=\n"
  "GF_start_%=:\n"
  "v_sub_u32 v48, v48, v185\n"
  "v_sub_u32 v49, v49, v185\n"
  "v_sub_u32 v50, v50, v185\n"
  "v_sub_u32 v51, v51, v185\n"
  "v_sub_u32 v52, v52, v185\n"
  "v_sub_u32 v53, v53, v185\n"
  "v_sub_u32 v54, v54, v185\n"
  "v_sub_u32 v55, v55, v185\n"
  "v_sub_u32 v56, v56, v185\n"
  "v_sub_u32 v57, v57, v185\n"
  "v_sub_u32 v58, v58, v185\n"
  "v_sub_u32 v59, v59, v185\n"
  "s_waitcnt vmcnt(6)\n"
  "ds_read_b64_tr_b16 v[96:97], v250\n"
  "ds_read_b64_tr_b16 v[98:99], v250 offset:3072\n"
  "ds_read_b64_tr_b16 v[100:101], v254\n"
  "ds_read_b64_tr_b16 v[102:103], v254 offset:3072\n"
  "ds_read_b64_tr_b16 v[104:105], v250 offset:64\n"
  "ds_read_b64_tr_b16 v[106:107], v250 offset:3136\n"
  "ds_read_b64_tr_b16 v[108:109], v254 offset:64\n"
  "ds_read_b64_tr_b16 v[110:111], v254 offset:3136\n"
  "ds_read_b64_tr_b16 v[112:113], v250 offset:128\n"
  "ds_read_b64_tr_b16 v[114:115], v250 offset:3200\n"
  "ds_read_b64_tr_b16 v[116:117], v254 offset:128\n"
  "ds_read_b64_tr_b16 v[118:119], v254 offset:3200\n"
  "s_waitcnt vmcnt(0)\n"
  "ds_read_b64_tr_b16 v[120:121], v250 offset:24576\n"
  "ds_read_b64_tr_b16 v[122:123], v250 offset:27648\n"
  "ds_read_b64_tr_b16 v[124:125], v254 offset:24576\n"
  "ds_read_b64_tr_b16 v[126:127], v254 offset:27648\n"
  "ds_read_b64_tr_b16 v[128:129], v250 offset:24640\n"
  "ds_read_b64_tr_b16 v[130:131], v250 offset:27712\n"
  "ds_read_b64_tr_b16 v[132:133], v254 offset:24640\n"
  "ds_read_b64_tr_b16 v[134:135], v254 offset:27712\n"
  "ds_read_b64_tr_b16 v[136:137], v250 offset:24704\n"
  "ds_read_b64_tr_b16 v[138:139], v250 offset:27776\n"
  "ds_read_b64_tr_b16 v[140:141], v254 offset:24704\n"
  "ds_read_b64_tr_b16 v[142:143], v254 offset:27776\n"
  "s_waitcnt lgkmcnt(0)\n"
  "s_nop 7\n"
  "v_mfma_f32_16x16x32_bf16 v[192:195], v[0:3], v[224:227], 0\n"
  "v_mfma_f32_16x16x32_bf16 v[196:199], v[12:15], v[224:227], 0\n"
  "v_mfma_f32_16x16x32_bf16 v[192:195], v[4:7], v[228:231], v[192:195]\n"
  "v_mfma_f32_16x16x32_bf16 v[196:199], v[16:19], v[228:231], v[196:199]\n"
  "v_mfma_f32_16x16x32_bf16 v[192:195], v[8:11], v[232:235], v[192:195]\n"
  "v_mfma_f32_16x16x32_bf16 v[196:199], v[20:23], v[232:235], v[196:199]\n"
  "v_mfma_f32_16x16x32_bf16 v[200:203], v[24:27], v[224:227], 0\n"
  "s_nop 5\n"
  "v_mul_f32 v192, s57, v192\n"
  "v_mul_f32 v193, s57, v193\n"
  "v_mul_f32 v194, s57, v194\n"
  "v_mul_f32 v195, s57, v195\n"
  "v_fma_f32 v251, -v192, v192, s46\n"
  "v_fma_f32 v252, -v193, v193, s46\n"
  "v_fma_f32 v253, -v194, v194, s46\n"
  "v_fma_f32 v255, -v195, v195, s46\n"
  "v_mul_f32 v251, v192, v251\n"
  "v_mul_f32 v252, v193, v252\n"
  "v_mul_f32 v253, v194, v253\n"
  "v_mul_f32 v255, v195, v255\n"
  "v_mfma_f32_16x16x32_bf16 v[204:207], v[36:39], v[224:227], 0\n"
  "v_exp_f32 v251, v251\n"
  "v_exp_f32 v252, v252\n"
  "v_exp_f32 v253, v253\n"
  "v_exp_f32 v255, v255\n"
  "v_add_f32 v251, 1.0, v251\n"
  "v_add_f32 v252, 1.0, v252\n"
  "v_add_f32 v253, 1.0, v253\n"
  "v_add_f32 v255, 1.0, v255\n"
  "v_rcp_f32 v251, v251\n"
  "v_mfma_f32_16x16x32_bf16 v[200:203], v[28:31], v[228:231], v[200:203]\n"
  "v_rcp_f32 v252, v252\n"
  "v_rcp_f32 v253, v253\n"
  "v_rcp_f32 v255, v255\n"
  "v_mul_f32 v192, v192, v251\n"
  "v_mul_f32 v193, v193, v252\n"
  "v_mul_f32 v194, v194, v253\n"
  "v_mul_f32 v195, v195, v255\n"
  "v_cvt_pk_bf16_f32 v192, v192, v193\n"
  "v_cvt_pk_bf16_f32 v193, v194, v195\n"
  "v_mfma_f32_16x16x32_bf16 v[204:207], v[40:43], v[228:231], v[204:207]\n"
  "v_mul_f32 v196, s57, v196\n"
  "v_mul_f32 v197, s57, v197\n"
  "v_mul_f32 v198, s57, v198\n"
  "v_mul_f32 v199, s57, v199\n"
  "v_fma_f32 v251, -v196, v196, s46\n"
  "v_fma_f32 v252, -v197, v197, s46\n"
  "v_fma_f32 v253, -v198, v198, s46\n"
  "v_fma_f32 v255, -v199, v199, s46\n"
  "v_mul_f32 v251, v196, v251\n"
  "v_mul_f32 v252, v197, v252\n"
  "v_mul_f32 v253, v198, v253\n"
  "v_mul_f32 v255, v199, v255\n"
  "v_mfma_f32_16x16x32_bf16 v[200:203], v[32:35], v[232:235], v[200:203]\n"
  "v_exp_f32 v251, v251\n"
  "v_exp_f32 v252, v252\n"
  "v_exp_f32 v253, v253\n"
  "v_exp_f32 v255, v255\n"
  "v_add_f32 v251, 1.0, v251\n"
  "v_add_f32 v252, 1.0, v252\n"
  "v_add_f32 v253, 1.0, v253\n"
  "v_add_f32 v255, 1.0, v255\n"
  "v_rcp_f32 v251, v251\n"
  "v_mfma_f32_16x16x32_bf16 v[204:207], v[44:47], v[232:235], v[204:207]\n"
  "v_rcp_f32 v252, v252\n"
  "v_rcp_f32 v253, v253\n"
  "v_rcp_f32 v255, v255\n"
  "v_mul_f32 v196, v196, v251\n"
  "v_mul_f32 v197, v197, v252\n"
  "v_mul_f32 v198, v198, v253\n"
  "v_mul_f32 v199, v199, v255\n"
  "v_cvt_pk_bf16_f32 v194, v196, v197\n"
  "v_cvt_pk_bf16_f32 v195, v198, v199\n"
  "v_mfma_f32_16x16x32_bf16 v[208:211], v[48:51], v[224:227], 0\n"
  "v_mul_f32 v200, s57, v200\n"
  "v_mul_f32 v201, s57, v201\n"
  "v_mul_f32 v202, s57, v202\n"
  "v_mul_f32 v203, s57, v203\n"
  "v_fma_f32 v251, -v200, v200, s46\n"
  "v_fma_f32 v252, -v201, v201, s46\n"
  "v_fma_f32 v253, -v202, v202, s46\n"
  "v_fma_f32 v255, -v203, v203, s46\n"
  "v_mul_f32 v251, v200, v251\n"
  "v_mul_f32 v252, v201, v252\n"
  "v_mul_f32 v253, v202, v253\n"
  "v_mul_f32 v255, v203, v255\n"
  "v_mfma_f32_16x16x32_bf16 v[212:215], v[60:63], v[224:227], 0\n"
  "v_exp_f32 v251, v251\n"
  "v_exp_f32 v252, v252\n"
  "v_exp_f32 v253, v253\n"
  "v_exp_f32 v255, v255\n"
  "v_add_f32 v251, 1.0, v251\n"
  "v_add_f32 v252, 1.0, v252\n"
  "v_add_f32 v253, 1.0, v253\n"
  "v_add_f32 v255, 1.0, v255\n"
  "v_rcp_f32 v251, v251\n"
  "v_mfma_f32_16x16x32_bf16 v[208:211], v[52:55], v[228:231], v[208:211]\n"
  "v_rcp_f32 v252, v252\n"
  "v_rcp_f32 v253, v253\n"
  "v_rcp_f32 v255, v255\n"
  "v_mul_f32 v200, v200, v251\n"
  "v_mul_f32 v201, v201, v252\n"
  "v_mul_f32 v202, v202, v253\n"
  "v_mul_f32 v203, v203, v255\n"
  "v_cvt_pk_bf16_f32 v200, v200, v201\n"
  "v_cvt_pk_bf16_f32 v201, v202, v203\n"
  "v_mfma_f32_16x16x32_bf16 v[212:215], v[64:67], v[228:231], v[212:215]\n"
  "v_mul_f32 v204, s57, v204\n"
  "v_mul_f32 v205, s57, v205\n"
  "v_mul_f32 v206, s57, v206\n"
  "v_mul_f32 v207, s57, v207\n"
  "v_fma_f32 v251, -v204, v204, s46\n"
  "v_fma_f32 v252, -v205, v205, s46\n"
  "v_fma_f32 v253, -v206, v206, s46\n"
  "v_fma_f32 v255, -v207, v207, s46\n"
  "v_mul_f32 v251, v204, v251\n"
  "v_mul_f32 v252, v205, v252\n"
  "v_mul_f32 v253, v206, v253\n"
  "v_mul_f32 v255, v207, v255\n"
  "v_mfma_f32_16x16x32_bf16 v[208:211], v[56:59], v[232:235], v[208:211]\n"
  "v_exp_f32 v251, v251\n"
  "v_exp_f32 v252, v252\n"
  "v_exp_f32 v253, v253\n"
  "v_exp_f32 v255, v255\n"
  "v_add_f32 v251, 1.0, v251\n"
  "v_add_f32 v252, 1.0, v252\n"
  "v_add_f32 v253, 1.0, v253\n"
  "v_add_f32 v255, 1.0, v255\n"
  "v_rcp_f32 v251, v251\n"
  "v_mfma_f32_16x16x32_bf16 v[212:215], v[68:71], v[232:235], v[212:215]\n"
  "v_rcp_f32 v252, v252\n"
  "v_rcp_f32 v253, v253\n"
  "v_rcp_f32 v255, v255\n"
  "v_mul_f32 v204, v204, v251\n"
  "v_mul_f32 v205, v205, v252\n"
  "v_mul_f32 v206, v206, v253\n"
  "v_mul_f32 v207, v207, v255\n"
  "v_cvt_pk_bf16_f32 v202, v204, v205\n"
  "v_cvt_pk_bf16_f32 v203, v206, v207\n"
  "v_mfma_f32_16x16x32_bf16 v[216:219], v[0:3], v[236:239], 0\n"
  "v_mul_f32 v208, s57, v208\n"
  "v_mul_f32 v209, s57, v209\n"
  "v_mul_f32 v210, s57, v210\n"
  "v_mul_f32 v211, s57, v211\n"
  "v_fma_f32 v251, -v208, v208, s46\n"
  "v_fma_f32 v252, -v209, v209, s46\n"
  "v_mfma_f32_16x16x32_bf16 v[220:223], v[12:15], v[236:239], 0\n"
  "v_fma_f32 v253, -v210, v210, s46\n"
  "v_fma_f32 v255, -v211, v211, s46\n"
  "v_mul_f32 v251, v208, v251\n"
  "v_mul_f32 v252, v209, v252\n"
  "v_mul_f32 v253, v210, v253\n"
  "v_mul_f32 v255, v211, v255\n"
  "v_mfma_f32_16x16x32_bf16 v[216:219], v[4:7], v[240:243], v[216:219]\n"
  "v_exp_f32 v251, v251\n"
  "v_exp_f32 v252, v252\n"
  "v_exp_f32 v253, v253\n"
  "v_mfma_f32_16x16x32_bf16 v[220:223], v[16:19], v[240:243], v[220:223]\n"
  "v_exp_f32 v255, v255\n"
  "v_add_f32 v251, 1.0, v251\n"
  "v_add_f32 v252, 1.0, v252\n"
  "v_add_f32 v253, 1.0, v253\n"
  "v_add_f32 v255, 1.0, v255\n"
  "v_rcp_f32 v251, v251\n"
  "v_mfma_f32_16x16x32_bf16 v[216:219], v[8:11], v[244:247], v[216:219]\n"
  "v_rcp_f32 v252, v252\n"
  "v_rcp_f32 v253, v253\n"
  "v_rcp_f32 v255, v255\n"
  "v_mfma_f32_16x16x32_bf16 v[220:223], v[20:23], v[244:247], v[220:223]\n"
  "v_mul_f32 v208, v208, v251\n"
  "v_mul_f32 v209, v209, v252\n"
  "v_mul_f32 v210, v210, v253\n"
  "v_mul_f32 v211, v211, v255\n"
  "v_cvt_pk_bf16_f32 v208, v208, v209\n"
  "v_cvt_pk_bf16_f32 v209, v210, v211\n"
  "v_mfma_f32_16x16x32_bf16 v[144:147], v[72:75], v[192:195], 0\n"
  "v_mul_f32 v212, s57, v212\n"
  "v_mul_f32 v213, s57, v213\n"
  "v_mul_f32 v214, s57, v214\n"
  "v_mul_f32 v215, s57, v215\n"
  "v_fma_f32 v251, -v212, v212, s46\n"
  "v_fma_f32 v252, -v213, v213, s46\n"
  "v_mfma_f32_16x16x32_bf16 v[148:151], v[76:79], v[192:195], 0\n"
  "v_fma_f32 v253, -v214, v214, s46\n"
  "v_fma_f32 v255, -v215, v215, s46\n"
  "v_mul_f32 v251, v212, v251\n"
  "v_mul_f32 v252, v213, v252\n"
  "v_mul_f32 v253, v214, v253\n"
  "v_mul_f32 v255, v215, v255\n"
  "v_mfma_f32_16x16x32_bf16 v[152:155], v[80:83], v[192:195], 0\n"
  "v_exp_f32 v251, v251\n"
  "v_exp_f32 v252, v252\n"
  "v_exp_f32 v253, v253\n"
  "v_mfma_f32_16x16x32_bf16 v[156:159], v[84:87], v[192:195], 0\n"
  "v_exp_f32 v255, v255\n"
  "v_add_f32 v251, 1.0, v251\n"
  "v_add_f32 v252, 1.0, v252\n"
  "v_add_f32 v253, 1.0, v253\n"
  "v_add_f32 v255, 1.0, v255\n"
  "v_rcp_f32 v251, v251\n"
  "v_mfma_f32_16x16x32_bf16 v[160:163], v[88:91], v[192:195], 0\n"
  "v_rcp_f32 v252, v252\n"
  "v_rcp_f32 v253, v253\n"
  "v_rcp_f32 v255, v255\n"
  "v_mfma_f32_16x16x32_bf16 v[164:167], v[92:95], v[192:195], 0\n"
  "v_mul_f32 v212, v212, v251\n"
  "v_mul_f32 v213, v213, v252\n"
  "v_mul_f32 v214, v214, v253\n"
  "v_mul_f32 v215, v215, v255\n"
  "v_cvt_pk_bf16_f32 v210, v212, v213\n"
  "v_cvt_pk_bf16_f32 v211, v214, v215\n"
  "s_branch GF_od_%=\n"
  ".p2align 6\n"
  "GF_ev_%=:\n"
  "v_mfma_f32_16x16x32_bf16 v[208:211], v[24:27], v[236:239], 0\n"
  "s_cmp_ge_u32 s22, s23\n"
  "s_cbranch_scc0 GFa_slow_%=\n"
  "GFa_back_%=:\n"
  "v_mul_f32 v200, s57, v200\n"
  "v_mul_f32 v201, s57, v201\n"
  "v_mul_f32 v202, s57, v202\n"
  "v_mul_f32 v203, s57, v203\n"
  "v_fma_f32 v251, -v200, v200, s46\n"
  "v_fma_f32 v252, -v201, v201, s46\n"
  "v_fma_f32 v253, -v202, v202, s46\n"
  "v_mfma_f32_16x16x32_bf16 v[212:215], v[36:39], v[236:239], 0\n"
  "v_fma_f32 v255, -v203, v203, s46\n"
  "v_mul_f32 v251, v200, v251\n"
  "v_mul_f32 v252, v201, v252\n"
  "v_mul_f32 v253, v202, v253\n"
  "v_mul_f32 v255, v203, v255\n"
  "v_exp_f32 v251, v251\n"
  "v_mfma_f32_16x16x32_bf16 v[208:211], v[28:31], v[240:243], v[208:211]\n"
  "v_exp_f32 v252, v252\n"
  "v_exp_f32 v253, v253\n"
  "v_exp_f32 v255, v255\n"
  "v_mfma_f32_16x16x32_bf16 v[212:215], v[40:43], v[240:243], v[212:215]\n"
  "v_add_f32 v251, 1.0, v251\n"
  "v_add_f32 v252, 1.0, v252\n"
  "v_add_f32 v253, 1.0, v253\n"
  "v_add_f32 v255, 1.0, v255\n"
  "v_rcp_f32 v251, v251\n"
  "v_mfma_f32_16x16x32_bf16 v[208:211], v[32:35], v[244:247], v[208:211]\n"
  "v_rcp_f32 v252, v252\n"
  "v_rcp_f32 v253, v253\n"
  "v_rcp_f32 v255, v255\n"
  "v_mul_f32 v200, v200, v251\n"
  "v_mfma_f32_16x16x32_bf16 v[212:215], v[44:47], v[244:247], v[212:215]\n"
  "v_mul_f32 v201, v201, v252\n"
  "v_mul_f32 v202, v202, v253\n"
  "v_mul_f32 v203, v203, v255\n"
  "v_cvt_pk_bf16_f32 v200, v200, v201\n"
  "v_cvt_pk_bf16_f32 v201, v202, v203\n"
  "v_mul_f32 v204, s57, v204\n"
  "v_mul_f32 v205, s57, v205\n"
  "v_mfma_f32_16x16x32_bf16 v[144:147], v[96:99], v[216:219], v[144:147]\n"
  "v_mul_f32 v206, s57, v206\n"
  "v_mul_f32 v207, s57, v207\n"
  "v_fma_f32 v251, -v204, v204, s46\n"
  "v_fma_f32 v252, -v205, v205, s46\n"
  "v_fma_f32 v253, -v206, v206, s46\n"
  "v_fma_f32 v255, -v207, v207, s46\n"
  "v_mul_f32 v251, v204, v251\n"
  "v_mfma_f32_16x16x32_bf16 v[148:151], v[100:103], v[216:219], v[148:151]\n"
  "v_cvt_pk_bf16_f32 v176, v176, v177\n"
  "v_cvt_pk_bf16_f32 v177, v178, v179\n"
  "v_cvt_pk_bf16_f32 v178, v180, v181\n"
  "v_cvt_pk_bf16_f32 v179, v182, v183\n"
  "v_mul_f32 v252, v205, v252\n"
  "v_mul_f32 v253, v206, v253\n"
  "v_mfma_f32_16x16x32_bf16 v[152:155], v[104:107], v[216:219], v[152:155]\n"
  "ds_write_b128 v249, v[176:179] offset:3136\n"
  "v_mul_f32 v255, v207, v255\n"
  "v_exp_f32 v251, v251\n"
  "v_exp_f32 v252, v252\n"
  "v_exp_f32 v253, v253\n"
  "v_mfma_f32_16x16x32_bf16 v[156:159], v[108:111], v[216:219], v[156:159]\n"
  "ds_read_b128 v[224:227], v248\n"
  "v_exp_f32 v255, v255\n"
  "v_add_f32 v251, 1.0, v251\n"
  "v_add_f32 v252, 1.0, v252\n"
  "v_add_f32 v253, 1.0, v253\n"
  "v_add_f32 v255, 1.0, v255\n"
  "v_mfma_f32_16x16x32_bf16 v[160:163], v[112:115], v[216:219], v[160:163]\n"
  "v_rcp_f32 v251, v251\n"
  "v_rcp_f32 v252, v252\n"
  "v_rcp_f32 v253, v253\n"
  "v_rcp_f32 v255, v255\n"
  "v_mfma_f32_16x16x32_bf16 v[164:167], v[116:119], v[216:219], v[164:167]\n"
  "v_mul_f32 v204, v204, v251\n"
  "v_mul_f32 v205, v205, v252\n"
  "v_mul_f32 v206, v206, v253\n"
  "v_mul_f32 v207, v207, v255\n"
  "v_cvt_pk_bf16_f32 v202, v204, v205\n"
  "v_cvt_pk_bf16_f32 v203, v206, v207\n"
  "v_mfma_f32_16x16x32_bf16 v[216:219], v[48:51], v[236:239], 0\n"
  "v_cvt_pk_bf16_f32 v184, v184, v185\n"
  "v_cvt_pk_bf16_f32 v185, v186, v187\n"
  "v_cvt_pk_bf16_f32 v186, v188, v189\n"
  "v_cvt_pk_bf16_f32 v187, v190, v191\n"
  "v_mul_f32 v208, s57, v208\n"
  "v_mul_f32 v209, s57, v209\n"
  "v_mul_f32 v210, s57, v210\n"
  "v_mfma_f32_16x16x32_bf16 v[220:223], v[60:63], v[236:239], 0\n"
  "ds_write_b128 v249, v[184:187] offset:3200\n"
  "v_mul_f32 v211, s57, v211\n"
  "v_fma_f32 v251, -v208, v208, s46\n"
  "v_fma_f32 v252, -v209, v209, s46\n"
  "v_fma_f32 v253, -v210, v210, s46\n"
  "v_fma_f32 v255, -v211, v211, s46\n"
  "v_mul_f32 v251, v208, v251\n"
  "v_mfma_f32_16x16x32_bf16 v[216:219], v[52:55], v[240:243], v[216:219]\n"
  "ds_append v254 offset:62464\n"
  "v_mul_f32 v252, v209, v252\n"
  "v_mul_f32 v253, v210, v253\n"
  "v_mul_f32 v255, v211, v255\n"
  "v_exp_f32 v251, v251\n"
  "v_exp_f32 v252, v252\n"
  "v_mfma_f32_16x16x32_bf16 v[220:223], v[64:67], v[240:243], v[220:223]\n"
  "ds_read_b128 v[228:231], v248 offset:1024\n"
  "v_exp_f32 v253, v253\n"
  "v_exp_f32 v255, v255\n"
  "v_add_f32 v251, 1.0, v251\n"
  "v_add_f32 v252, 1.0, v252\n"
  "v_add_f32 v253, 1.0, v253\n"
  "v_mfma_f32_16x16x32_bf16 v[216:219], v[56:59], v[244:247], v[216:219]\n"
  "ds_read_b128 v[232:235], v248 offset:2048\n"
  "v_add_f32 v255, 1.0, v255\n"
  "v_rcp_f32 v251, v251\n"
  "v_rcp_f32 v252, v252\n"
  "v_rcp_f32 v253, v253\n"
  "v_mfma_f32_16x16x32_bf16 v[220:223], v[68:71], v[244:247], v[220:223]\n"
  "v_rcp_f32 v255, v255\n"
  "v_mul_f32 v208, v208, v251\n"
  "v_mul_f32 v209, v209, v252\n"
  "v_mul_f32 v210, v210, v253\n"
  "v_mul_f32 v211, v211, v255\n"
  "v_mfma_f32_16x16x32_bf16 v[144:147], v[120:123], v[192:195], v[144:147]\n"
  "v_cvt_pk_bf16_f32 v208, v208, v209\n"
  "v_cvt_pk_bf16_f32 v209, v210, v211\n"
  "v_mul_f32 v212, s57, v212\n"
  "v_mul_f32 v213, s57, v213\n"
  "v_mul_f32 v214, s57, v214\n"
  "v_mul_f32 v215, s57, v215\n"
  "v_fma_f32 v251, -v212, v212, s46\n"
  "v_mfma_f32_16x16x32_bf16 v[148:151], v[124:127], v[192:195], v[148:151]\n"
  "v_fma_f32 v252, -v213, v213, s46\n"
  "v_fma_f32 v253, -v214, v214, s46\n"
  "v_fma_f32 v255, -v215, v215, s46\n"
  "v_mul_f32 v251, v212, v251\n"
  "v_mul_f32 v252, v213, v252\n"
  "v_mul_f32 v253, v214, v253\n"
  "v_mfma_f32_16x16x32_bf16 v[152:155], v[128:131], v[192:195], v[152:155]\n"
  "v_mul_f32 v255, v215, v255\n"
  "v_exp_f32 v251, v251\n"
  "v_exp_f32 v252, v252\n"
  "v_exp_f32 v253, v253\n"
  "v_mfma_f32_16x16x32_bf16 v[156:159], v[132:135], v[192:195], v[156:159]\n"
  "v_exp_f32 v255, v255\n"
  "v_add_f32 v251, 1.0, v251\n"
  "v_add_f32 v252, 1.0, v252\n"
  "v_add_f32 v253, 1.0, v253\n"
  "v_add_f32 v255, 1.0, v255\n"
  "v_mfma_f32_16x16x32_bf16 v[160:163], v[136:139], v[192:195], v[160:163]\n"
  "v_rcp_f32 v251, v251\n"
  "v_rcp_f32 v252, v252\n"
  "v_rcp_f32 v253, v253\n"
  "v_rcp_f32 v255, v255\n"
  "v_mfma_f32_16x16x32_bf16 v[164:167], v[140:143], v[192:195], v[164:167]\n"
  "s_waitcnt lgkmcnt(0)\n"
  "v_mul_f32 v212, v212, v251\n"
  "v_mul_f32 v213, v213, v252\n"
  "v_mul_f32 v214, v214, v253\n"
  "v_mul_f32 v215, v215, v255\n"
  "v_cvt_pk_bf16_f32 v210, v212, v213\n"
  "v_cvt_pk_bf16_f32 v211, v214, v215\n"
  "v_mfma_f32_16x16x32_bf16 v[192:195], v[0:3], v[224:227], 0\n"
  "v_mul_f32 v216, s57, v216\n"
  "v_mul_f32 v217, s57, v217\n"
  "v_mul_f32 v218, s57, v218\n"
  "v_mul_f32 v219, s57, v219\n"
  "v_fma_f32 v251, -v216, v216, s46\n"
  "v_fma_f32 v252, -v217, v217, s46\n"
  "v_fma_f32 v253, -v218, v218, s46\n"
  "v_mfma_f32_16x16x32_bf16 v[196:199], v[12:15], v[224:227], 0\n"
  "v_fma_f32 v255, -v219, v219, s46\n"
  "v_mul_f32 v251, v216, v251\n"
  "v_mul_f32 v252, v217, v252\n"
  "v_mul_f32 v253, v218, v253\n"
  "v_mul_f32 v255, v219, v255\n"
  "v_exp_f32 v251, v251\n"
  "v_mfma_f32_16x16x32_bf16 v[192:195], v[4:7], v[228:231], v[192:195]\n"
  "v_exp_f32 v252, v252\n"
  "v_exp_f32 v253, v253\n"
  "v_exp_f32 v255, v255\n"
  "v_mfma_f32_16x16x32_bf16 v[196:199], v[16:19], v[228:231], v[196:199]\n"
  "v_add_f32 v251, 1.0, v251\n"
  "v_add_f32 v252, 1.0, v252\n"
  "v_add_f32 v253, 1.0, v253\n"
  "v_add_f32 v255, 1.0, v255\n"
  "v_rcp_f32 v251, v251\n"
  "v_mfma_f32_16x16x32_bf16 v[192:195], v[8:11], v[232:235], v[192:195]\n"
  "v_rcp_f32 v252, v252\n"
  "v_rcp_f32 v253, v253\n"
  "v_rcp_f32 v255, v255\n"
  "v_mul_f32 v216, v216, v251\n"
  "v_mfma_f32_16x16x32_bf16 v[196:199], v[20:23], v[232:235], v[196:199]\n"
  "v_mul_f32 v217, v217, v252\n"
  "v_mul_f32 v218, v218, v253\n"
  "v_mul_f32 v219, v219, v255\n"
  "v_cvt_pk_bf16_f32 v216, v216, v217\n"
  "v_cvt_pk_bf16_f32 v217, v218, v219\n"
  "v_mul_f32 v220, s57, v220\n"
  "v_mul_f32 v221, s57, v221\n"
  "v_mfma_f32_16x16x32_bf16 v[168:171], v[72:75], v[200:203], 0\n"
  "v_mul_f32 v222, s57, v222\n"
  "v_mul_f32 v223, s57, v223\n"
  "v_fma_f32 v251, -v220, v220, s46\n"
  "v_fma_f32 v252, -v221, v221, s46\n"
  "v_fma_f32 v253, -v222, v222, s46\n"
  "v_fma_f32 v255, -v223, v223, s46\n"
  "v_mul_f32 v251, v220, v251\n"
  "v_mfma_f32_16x16x32_bf16 v[172:175], v[76:79], v[200:203], 0\n"
  "v_mul_f32 v252, v221, v252\n"
  "v_mul_f32 v253, v222, v253\n"
  "v_mul_f32 v255, v223, v255\n"
  "v_exp_f32 v251, v251\n"
  "v_exp_f32 v252, v252\n"
  "v_mfma_f32_16x16x32_bf16 v[176:179], v[80:83], v[200:203], 0\n"
  "v_exp_f32 v253, v253\n"
  "v_exp_f32 v255, v255\n"
  "v_add_f32 v251, 1.0, v251\n"
  "v_add_f32 v252, 1.0, v252\n"
  "v_mfma_f32_16x16x32_bf16 v[180:183], v[84:87], v[200:203], 0\n"
  "v_cvt_pk_bf16_f32 v144, v144, v145\n"
  "v_cvt_pk_bf16_f32 v145, v146, v147\n"
  "v_cvt_pk_bf16_f32 v146, v148, v149\n"
  "v_cvt_pk_bf16_f32 v147, v150, v151\n"
  "v_add_f32 v253, 1.0, v253\n"
  "v_add_f32 v255, 1.0, v255\n"
  "v_mfma_f32_16x16x32_bf16 v[184:187], v[88:91], v[200:203], 0\n"
  "ds_write_b128 v249, v[144:147] offset:24576\n"
  "v_rcp_f32 v251, v251\n"
  "v_rcp_f32 v252, v252\n"
  "v_rcp_f32 v253, v253\n"
  "v_rcp_f32 v255, v255\n"
  "v_mfma_f32_16x16x32_bf16 v[188:191], v[92:95], v[200:203], 0\n"
  "v_mul_f32 v220, v220, v251\n"
  "v_mul_f32 v221, v221, v252\n"
  "v_mul_f32 v222, v222, v253\n"
  "v_mul_f32 v223, v223, v255\n"
  "v_cvt_pk_bf16_f32 v218, v220, v221\n"
  "v_cvt_pk_bf16_f32 v219, v222, v223\n"
  "v_mfma_f32_16x16x32_bf16 v[200:203], v[24:27], v[224:227], 0\n"
  "v_mul_f32 v192, s57, v192\n"
  "v_mul_f32 v193, s57, v193\n"
  "v_mul_f32 v194, s57, v194\n"
  "v_mul_f32 v195, s57, v195\n"
  "v_fma_f32 v251, -v192, v192, s46\n"
  "v_fma_f32 v252, -v193, v193, s46\n"
  "v_fma_f32 v253, -v194, v194, s46\n"
  "v_mfma_f32_16x16x32_bf16 v[204:207], v[36:39], v[224:227], 0\n"
  "v_fma_f32 v255, -v195, v195, s46\n"
  "v_mul_f32 v251, v192, v251\n"
  "v_mul_f32 v252, v193, v252\n"
  "v_mul_f32 v253, v194, v253\n"
  "v_mul_f32 v255, v195, v255\n"
  "v_exp_f32 v251, v251\n"
  "v_mfma_f32_16x16x32_bf16 v[200:203], v[28:31], v[228:231], v[200:203]\n"
  "v_exp_f32 v252, v252\n"
  "v_exp_f32 v253, v253\n"
  "v_exp_f32 v255, v255\n"
  "v_mfma_f32_16x16x32_bf16 v[204:207], v[40:43], v[228:231], v[204:207]\n"
  "v_add_f32 v251, 1.0, v251\n"
  "v_add_f32 v252, 1.0, v252\n"
  "v_add_f32 v253, 1.0, v253\n"
  "v_add_f32 v255, 1.0, v255\n"
  "v_rcp_f32 v251, v251\n"
  "v_mfma_f32_16x16x32_bf16 v[200:203], v[32:35], v[232:235], v[200:203]\n"
  "v_rcp_f32 v252, v252\n"
  "v_rcp_f32 v253, v253\n"
  "v_rcp_f32 v255, v255\n"
  "v_mul_f32 v192, v192, v251\n"
  "v_mfma_f32_16x16x32_bf16 v[204:207], v[44:47], v[232:235], v[204:207]\n"
  "v_mul_f32 v193, v193, v252\n"
  "v_mul_f32 v194, v194, v253\n"
  "v_mul_f32 v195, v195, v255\n"
  "v_cvt_pk_bf16_f32 v192, v192, v193\n"
  "v_cvt_pk_bf16_f32 v193, v194, v195\n"
  "v_mul_f32 v196, s57, v196\n"
  "v_mul_f32 v197, s57, v197\n"
  "v_mfma_f32_16x16x32_bf16 v[168:171], v[96:99], v[208:211], v[168:171]\n"
  "v_mul_f32 v198, s57, v198\n"
  "v_mul_f32 v199, s57, v199\n"
  "v_fma_f32 v251, -v196, v196, s46\n"
  "v_fma_f32 v252, -v197, v197, s46\n"
  "v_fma_f32 v253, -v198, v198, s46\n"
  "v_fma_f32 v255, -v199, v199, s46\n"
  "v_mul_f32 v251, v196, v251\n"
  "v_mfma_f32_16x16x32_bf16 v[172:175], v[100:103], v[208:211], v[172:175]\n"
  "v_cvt_pk_bf16_f32 v152, v152, v153\n"
  "v_cvt_pk_bf16_f32 v153, v154, v155\n"
  "v_cvt_pk_bf16_f32 v154, v156, v157\n"
  "v_cvt_pk_bf16_f32 v155, v158, v159\n"
  "v_mul_f32 v252, v197, v252\n"
  "v_mul_f32 v253, v198, v253\n"
  "v_mfma_f32_16x16x32_bf16 v[176:179], v[104:107], v[208:211], v[176:179]\n"
  "ds_write_b128 v249, v[152:155] offset:24640\n"
  "v_mul_f32 v255, v199, v255\n"
  "v_exp_f32 v251, v251\n"
  "v_exp_f32 v252, v252\n"
  "v_exp_f32 v253, v253\n"
  "v_mfma_f32_16x16x32_bf16 v[180:183], v[108:111], v[208:211], v[180:183]\n"
  "ds_read_b128 v[236:239], v248 offset:3072\n"
  "v_exp_f32 v255, v255\n"
  "v_add_f32 v251, 1.0, v251\n"
  "v_add_f32 v252, 1.0, v252\n"
  "v_add_f32 v253, 1.0, v253\n"
  "v_add_f32 v255, 1.0, v255\n"
  "v_mfma_f32_16x16x32_bf16 v[184:187], v[112:115], v[208:211], v[184:187]\n"
  "v_rcp_f32 v251, v251\n"
  "v_rcp_f32 v252, v252\n"
  "v_rcp_f32 v253, v253\n"
  "v_rcp_f32 v255, v255\n"
  "v_mfma_f32_16x16x32_bf16 v[188:191], v[116:119], v[208:211], v[188:191]\n"
  "v_mul_f32 v196, v196, v251\n"
  "v_mul_f32 v197, v197, v252\n"
  "v_mul_f32 v198, v198, v253\n"
  "v_mul_f32 v199, v199, v255\n"
  "v_cvt_pk_bf16_f32 v194, v196, v197\n"
  "v_cvt_pk_bf16_f32 v195, v198, v199\n"
  "v_mfma_f32_16x16x32_bf16 v[208:211], v[48:51], v[224:227], 0\n"
  "v_cvt_pk_bf16_f32 v160, v160, v161\n"
  "v_cvt_pk_bf16_f32 v161, v162, v163\n"
  "v_cvt_pk_bf16_f32 v162, v164, v165\n"
  "v_cvt_pk_bf16_f32 v163, v166, v167\n"
  "v_mul_f32 v200, s57, v200\n"
  "v_mul_f32 v201, s57, v201\n"
  "v_mul_f32 v202, s57, v202\n"
  "v_mfma_f32_16x16x32_bf16 v[212:215], v[60:63], v[224:227], 0\n"
  "ds_write_b128 v249, v[160:163] offset:24704\n"
  "v_mul_f32 v203, s57, v203\n"
  "v_fma_f32 v251, -v200, v200, s46\n"
  "v_fma_f32 v252, -v201, v201, s46\n"
  "v_fma_f32 v253, -v202, v202, s46\n"
  "v_fma_f32 v255, -v203, v203, s46\n"
  "v_mul_f32 v251, v200, v251\n"
  "v_mfma_f32_16x16x32_bf16 v[208:211], v[52:55], v[228:231], v[208:211]\n"
  "v_mul_f32 v252, v201, v252\n"
  "v_mul_f32 v253, v202, v253\n"
  "v_mul_f32 v255, v203, v255\n"
  "v_exp_f32 v251, v251\n"
  "v_exp_f32 v252, v252\n"
  "v_mfma_f32_16x16x32_bf16 v[212:215], v[64:67], v[228:231], v[212:215]\n"
  "ds_read_b128 v[240:243], v248 offset:4096\n"
  "v_exp_f32 v253, v253\n"
  "v_exp_f32 v255, v255\n"
  "v_add_f32 v251, 1.0, v251\n"
  "v_add_f32 v252, 1.0, v252\n"
  "v_add_f32 v253, 1.0, v253\n"
  "v_mfma_f32_16x16x32_bf16 v[208:211], v[56:59], v[232:235], v[208:211]\n"
  "ds_read_b128 v[244:247], v248 offset:5120\n"
  "v_add_f32 v255, 1.0, v255\n"
  "v_rcp_f32 v251, v251\n"
  "v_rcp_f32 v252, v252\n"
  "v_rcp_f32 v253, v253\n"
  "v_mfma_f32_16x16x32_bf16 v[212:215], v[68:71], v[232:235], v[212:215]\n"
  "v_rcp_f32 v255, v255\n"
  "v_mul_f32 v200, v200, v251\n"
  "v_mul_f32 v201, v201, v252\n"
  "v_mul_f32 v202, v202, v253\n"
  "v_mul_f32 v203, v203, v255\n"
  "v_mfma_f32_16x16x32_bf16 v[168:171], v[120:123], v[216:219], v[168:171]\n"
  "v_cvt_pk_bf16_f32 v200, v200, v201\n"
  "v_cvt_pk_bf16_f32 v201, v202, v203\n"
  "v_mul_f32 v204, s57, v204\n"
  "v_mul_f32 v205, s57, v205\n"
  "v_mul_f32 v206, s57, v206\n"
  "v_mul_f32 v207, s57, v207\n"
  "v_fma_f32 v251, -v204, v204, s46\n"
  "v_mfma_f32_16x16x32_bf16 v[172:175], v[124:127], v[216:219], v[172:175]\n"
  "v_fma_f32 v252, -v205, v205, s46\n"
  "v_fma_f32 v253, -v206, v206, s46\n"
  "v_fma_f32 v255, -v207, v207, s46\n"
  "v_mul_f32 v251, v204, v251\n"
  "v_mul_f32 v252, v205, v252\n"
  "v_mul_f32 v253, v206, v253\n"
  "v_mfma_f32_16x16x32_bf16 v[176:179], v[128:131], v[216:219], v[176:179]\n"
  "v_mul_f32 v255, v207, v255\n"
  "v_exp_f32 v251, v251\n"
  "v_exp_f32 v252, v252\n"
  "v_exp_f32 v253, v253\n"
  "v_mfma_f32_16x16x32_bf16 v[180:183], v[132:135], v[216:219], v[180:183]\n"
  "v_exp_f32 v255, v255\n"
  "v_add_f32 v251, 1.0, v251\n"
  "v_add_f32 v252, 1.0, v252\n"
  "v_add_f32 v253, 1.0, v253\n"
  "v_add_f32 v255, 1.0, v255\n"
  "v_mfma_f32_16x16x32_bf16 v[184:187], v[136:139], v[216:219], v[184:187]\n"
  "v_rcp_f32 v251, v251\n"
  "v_rcp_f32 v252, v252\n"
  "v_rcp_f32 v253, v253\n"
  "v_rcp_f32 v255, v255\n"
  "v_mfma_f32_16x16x32_bf16 v[188:191], v[140:143], v[216:219], v[188:191]\n"
  "s_waitcnt lgkmcnt(0)\n"
  "v_mul_f32 v204, v204, v251\n"
  "v_mul_f32 v205, v205, v252\n"
  "v_mul_f32 v206, v206, v253\n"
  "v_mul_f32 v207, v207, v255\n"
  "v_cvt_pk_bf16_f32 v202, v204, v205\n"
  "v_cvt_pk_bf16_f32 v203, v206, v207\n"
  "v_mfma_f32_16x16x32_bf16 v[216:219], v[0:3], v[236:239], 0\n"
  "v_mul_f32 v208, s57, v208\n"
  "v_mul_f32 v209, s57, v209\n"
  "v_mul_f32 v210, s57, v210\n"
  "v_mul_f32 v211, s57, v211\n"
  "v_fma_f32 v251, -v208, v208, s46\n"
  "v_fma_f32 v252, -v209, v209, s46\n"
  "v_fma_f32 v253, -v210, v210, s46\n"
  "v_mfma_f32_16x16x32_bf16 v[220:223], v[12:15], v[236:239], 0\n"
  "s_add_u32 s23, s23, 512\n"
  "v_fma_f32 v255, -v211, v211, s46\n"
  "v_mul_f32 v251, v208, v251\n"
  "v_mul_f32 v252, v209, v252\n"
  "v_mul_f32 v253, v210, v253\n"
  "v_mul_f32 v255, v211, v255\n"
  "v_exp_f32 v251, v251\n"
  "v_mfma_f32_16x16x32_bf16 v[216:219], v[4:7], v[240:243], v[216:219]\n"
  "v_exp_f32 v252, v252\n"
  "v_exp_f32 v253, v253\n"
  "v_exp_f32 v255, v255\n"
  "v_mfma_f32_16x16x32_bf16 v[220:223], v[16:19], v[240:243], v[220:223]\n"
  "v_add_f32 v251, 1.0, v251\n"
  "v_add_f32 v252, 1.0, v252\n"
  "v_add_f32 v253, 1.0, v253\n"
  "v_add_f32 v255, 1.0, v255\n"
  "v_rcp_f32 v251, v251\n"
  "v_mfma_f32_16x16x32_bf16 v[216:219], v[8:11], v[244:247], v[216:219]\n"
  "v_rcp_f32 v252, v252\n"
  "v_rcp_f32 v253, v253\n"
  "v_rcp_f32 v255, v255\n"
  "v_mul_f32 v208, v208, v251\n"
  "v_mul_f32 v209, v209, v252\n"
  "v_mfma_f32_16x16x32_bf16 v[220:223], v[20:23], v[244:247], v[220:223]\n"
  "ds_read_addtid_b32 v254 offset:62464\n"
  "v_mul_f32 v210, v210, v253\n"
  "v_mul_f32 v211, v211, v255\n"
  "v_cvt_pk_bf16_f32 v208, v208, v209\n"
  "v_cvt_pk_bf16_f32 v209, v210, v211\n"
  "v_mul_f32 v212, s57, v212\n"
  "v_mul_f32 v213, s57, v213\n"
  "v_mfma_f32_16x16x32_bf16 v[144:147], v[72:75], v[192:195], 0\n"
  "v_mul_f32 v214, s57, v214\n"
  "v_mul_f32 v215, s57, v215\n"
  "v_fma_f32 v251, -v212, v212, s46\n"
  "v_fma_f32 v252, -v213, v213, s46\n"
  "v_fma_f32 v253, -v214, v214, s46\n"
  "v_fma_f32 v255, -v215, v215, s46\n"
  "v_mul_f32 v251, v212, v251\n"
  "v_mfma_f32_16x16x32_bf16 v[148:151], v[76:79], v[192:195], 0\n"
  "v_mul_f32 v252, v213, v252\n"
  "v_mul_f32 v253, v214, v253\n"
  "v_mul_f32 v255, v215, v255\n"
  "v_exp_f32 v251, v251\n"
  "v_exp_f32 v252, v252\n"
  "v_mfma_f32_16x16x32_bf16 v[152:155], v[80:83], v[192:195], 0\n"
  "v_exp_f32 v253, v253\n"
  "v_exp_f32 v255, v255\n"
  "v_add_f32 v251, 1.0, v251\n"
  "v_add_f32 v252, 1.0, v252\n"
  "v_add_f32 v253, 1.0, v253\n"
  "v_mfma_f32_16x16x32_bf16 v[156:159], v[84:87], v[192:195], 0\n"
  "v_cvt_pk_bf16_f32 v168, v168, v169\n"
  "v_cvt_pk_bf16_f32 v169, v170, v171\n"
  "v_cvt_pk_bf16_f32 v170, v172, v173\n"
  "v_cvt_pk_bf16_f32 v171, v174, v175\n"
  "v_add_f32 v255, 1.0, v255\n"
  "v_rcp_f32 v251, v251\n"
  "v_mfma_f32_16x16x32_bf16 v[160:163], v[88:91], v[192:195], 0\n"
  "s_waitcnt lgkmcnt(0)\n"
  "ds_write_b128 v249, v[168:171] offset:27648\n"
  "v_readfirstlane_b32 s22, v254\n"
  "v_rcp_f32 v252, v252\n"
  "v_rcp_f32 v253, v253\n"
  "v_rcp_f32 v255, v255\n"
  "v_mfma_f32_16x16x32_bf16 v[164:167], v[92:95], v[192:195], 0\n"
  "v_mul_f32 v212, v212, v251\n"
  "v_mul_f32 v213, v213, v252\n"
  "v_mul_f32 v214, v214, v253\n"
  "v_mul_f32 v215, v215, v255\n"
  "v_cvt_pk_bf16_f32 v210, v212, v213\n"
  "v_cvt_pk_bf16_f32 v211, v214, v215\n"
  "GF_od_%=:\n"
  "v_mfma_f32_16x16x32_bf16 v[192:195], v[24:27], v[236:239], 0\n"
  "s_cmp_ge_u32 s22, s23\n"
  "s_cbranch_scc0 GFb_slow_%=\n"
  "GFb_back_%=:\n"
  "v_mul_f32 v216, s57, v216\n"
  "v_mul_f32 v217, s57, v217\n"
  "v_mul_f32 v218, s57, v218\n"
  "v_mul_f32 v219, s57, v219\n"
  "v_fma_f32 v251, -v216, v216, s46\n"
  "v_fma_f32 v252, -v217, v217, s46\n"
  "v_fma_f32 v253, -v218, v218, s46\n"
  "v_mfma_f32_16x16x32_bf16 v[196:199], v[36:39], v[236:239], 0\n"
  "v_fma_f32 v255, -v219, v219, s46\n"
  "v_mul_f32 v251, v216, v251\n"
  "v_mul_f32 v252, v217, v252\n"
  "v_mul_f32 v253, v218, v253\n"
  "v_mul_f32 v255, v219, v255\n"
  "v_exp_f32 v251, v251\n"
  "v_mfma_f32_16x16x32_bf16 v[192:195], v[28:31], v[240:243], v[192:195]\n"
  "v_exp_f32 v252, v252\n"
  "v_exp_f32 v253, v253\n"
  "v_exp_f32 v255, v255\n"
  "v_mfma_f32_16x16x32_bf16 v[196:199], v[40:43], v[240:243], v[196:199]\n"
  "v_add_f32 v251, 1.0, v251\n"
  "v_add_f32 v252, 1.0, v252\n"
  "v_add_f32 v253, 1.0, v253\n"
  "v_add_f32 v255, 1.0, v255\n"
  "v_rcp_f32 v251, v251\n"
  "v_mfma_f32_16x16x32_bf16 v[192:195], v[32:35], v[244:247], v[192:195]\n"
  "v_rcp_f32 v252, v252\n"
  "v_rcp_f32 v253, v253\n"
  "v_rcp_f32 v255, v255\n"
  "v_mul_f32 v216, v216, v251\n"
  "v_mfma_f32_16x16x32_bf16 v[196:199], v[44:47], v[244:247], v[196:199]\n"
  "v_mul_f32 v217, v217, v252\n"
  "v_mul_f32 v218, v218, v253\n"
  "v_mul_f32 v219, v219, v255\n"
  "v_cvt_pk_bf16_f32 v216, v216, v217\n"
  "v_cvt_pk_bf16_f32 v217, v218, v219\n"
  "v_mul_f32 v220, s57, v220\n"
  "v_mul_f32 v221, s57, v221\n"
  "v_mfma_f32_16x16x32_bf16 v[144:147], v[96:99], v[200:203], v[144:147]\n"
  "v_mul_f32 v222, s57, v222\n"
  "v_mul_f32 v223, s57, v223\n"
  "v_fma_f32 v251, -v220, v220, s46\n"
  "v_fma_f32 v252, -v221, v221, s46\n"
  "v_fma_f32 v253, -v222, v222, s46\n"
  "v_fma_f32 v255, -v223, v223, s46\n"
  "v_mul_f32 v251, v220, v251\n"
  "v_mfma_f32_16x16x32_bf16 v[148:151], v[100:103], v[200:203], v[148:151]\n"
  "v_cvt_pk_bf16_f32 v176, v176, v177\n"
  "v_cvt_pk_bf16_f32 v177, v178, v179\n"
  "v_cvt_pk_bf16_f32 v178, v180, v181\n"
  "v_cvt_pk_bf16_f32 v179, v182, v183\n"
  "v_mul_f32 v252, v221, v252\n"
  "v_mul_f32 v253, v222, v253\n"
  "v_mfma_f32_16x16x32_bf16 v[152:155], v[104:107], v[200:203], v[152:155]\n"
  "ds_write_b128 v249, v[176:179] offset:27712\n"
  "v_mul_f32 v255, v223, v255\n"
  "v_exp_f32 v251, v251\n"
  "v_exp_f32 v252, v252\n"
  "v_exp_f32 v253, v253\n"
  "v_mfma_f32_16x16x32_bf16 v[156:159], v[108:111], v[200:203], v[156:159]\n"
  "ds_read_b128 v[224:227], v248 offset:6144\n"
  "v_exp_f32 v255, v255\n"
  "v_add_f32 v251, 1.0, v251\n"
  "v_add_f32 v252, 1.0, v252\n"
  "v_add_f32 v253, 1.0, v253\n"
  "v_add_f32 v255, 1.0, v255\n"
  "v_mfma_f32_16x16x32_bf16 v[160:163], v[112:115], v[200:203], v[160:163]\n"
  "v_rcp_f32 v251, v251\n"
  "v_rcp_f32 v252, v252\n"
  "v_rcp_f32 v253, v253\n"
  "v_rcp_f32 v255, v255\n"
  "v_mfma_f32_16x16x32_bf16 v[164:167], v[116:119], v[200:203], v[164:167]\n"
  "v_mul_f32 v220, v220, v251\n"
  "v_mul_f32 v221, v221, v252\n"
  "v_mul_f32 v222, v222, v253\n"
  "v_mul_f32 v223, v223, v255\n"
  "v_cvt_pk_bf16_f32 v218, v220, v221\n"
  "v_cvt_pk_bf16_f32 v219, v222, v223\n"
  "v_mfma_f32_16x16x32_bf16 v[200:203], v[48:51], v[236:239], 0\n"
  "v_cvt_pk_bf16_f32 v184, v184, v185\n"
  "v_cvt_pk_bf16_f32 v185, v186, v187\n"
  "v_cvt_pk_bf16_f32 v186, v188, v189\n"
  "v_cvt_pk_bf16_f32 v187, v190, v191\n"
  "v_mul_f32 v192, s57, v192\n"
  "v_mul_f32 v193, s57, v193\n"
  "v_mul_f32 v194, s57, v194\n"
  "v_mfma_f32_16x16x32_bf16 v[204:207], v[60:63], v[236:239], 0\n"
  "ds_write_b128 v249, v[184:187] offset:27776\n"
  "v_mul_f32 v195, s57, v195\n"
  "v_fma_f32 v251, -v192, v192, s46\n"
  "v_fma_f32 v252, -v193, v193, s46\n"
  "v_fma_f32 v253, -v194, v194, s46\n"
  "v_fma_f32 v255, -v195, v195, s46\n"
  "v_mul_f32 v251, v192, v251\n"
  "v_mfma_f32_16x16x32_bf16 v[200:203], v[52:55], v[240:243], v[200:203]\n"
  "ds_append v254 offset:62464\n"
  "v_mul_f32 v252, v193, v252\n"
  "v_mul_f32 v253, v194, v253\n"
  "v_mul_f32 v255, v195, v255\n"
  "v_exp_f32 v251, v251\n"
  "v_exp_f32 v252, v252\n"
  "v_mfma_f32_16x16x32_bf16 v[204:207], v[64:67], v[240:243], v[204:207]\n"
  "ds_read_b128 v[228:231], v248 offset:7168\n"
  "v_exp_f32 v253, v253\n"
  "v_exp_f32 v255, v255\n"
  "v_add_f32 v251, 1.0, v251\n"
  "v_add_f32 v252, 1.0, v252\n"
  "v_add_f32 v253, 1.0, v253\n"
  "v_mfma_f32_16x16x32_bf16 v[200:203], v[56:59], v[244:247], v[200:203]\n"
  "ds_read_b128 v[232:235], v248 offset:8192\n"
  "v_add_f32 v255, 1.0, v255\n"
  "v_rcp_f32 v251, v251\n"
  "v_rcp_f32 v252, v252\n"
  "v_rcp_f32 v253, v253\n"
  "v_mfma_f32_16x16x32_bf16 v[204:207], v[68:71], v[244:247], v[204:207]\n"
  "v_rcp_f32 v255, v255\n"
  "v_mul_f32 v192, v192, v251\n"
  "v_mul_f32 v193, v193, v252\n"
  "v_mul_f32 v194, v194, v253\n"
  "v_mul_f32 v195, v195, v255\n"
  "v_mfma_f32_16x16x32_bf16 v[144:147], v[120:123], v[208:211], v[144:147]\n"
  "v_cvt_pk_bf16_f32 v192, v192, v193\n"
  "v_cvt_pk_bf16_f32 v193, v194, v195\n"
  "v_mul_f32 v196, s57, v196\n"
  "v_mul_f32 v197, s57, v197\n"
  "v_mul_f32 v198, s57, v198\n"
  "v_mul_f32 v199, s57, v199\n"
  "v_fma_f32 v251, -v196, v196, s46\n"
  "v_mfma_f32_16x16x32_bf16 v[148:151], v[124:127], v[208:211], v[148:151]\n"
  "v_fma_f32 v252, -v197, v197, s46\n"
  "v_fma_f32 v253, -v198, v198, s46\n"
  "v_fma_f32 v255, -v199, v199, s46\n"
  "v_mul_f32 v251, v196, v251\n"
  "v_mul_f32 v252, v197, v252\n"
  "v_mul_f32 v253, v198, v253\n"
  "v_mfma_f32_16x16x32_bf16 v[152:155], v[128:131], v[208:211], v[152:155]\n"
  "v_mul_f32 v255, v199, v255\n"
  "v_exp_f32 v251, v251\n"
  "v_exp_f32 v252, v252\n"
  "v_exp_f32 v253, v253\n"
  "v_mfma_f32_16x16x32_bf16 v[156:159], v[132:135], v[208:211], v[156:159]\n"
  "v_exp_f32 v255, v255\n"
  "v_add_f32 v251, 1.0, v251\n"
  "v_add_f32 v252, 1.0, v252\n"
  "v_add_f32 v253, 1.0, v253\n"
  "v_add_f32 v255, 1.0, v255\n"
  "v_mfma_f32_16x16x32_bf16 v[160:163], v[136:139], v[208:211], v[160:163]\n"
  "v_rcp_f32 v251, v251\n"
  "v_rcp_f32 v252, v252\n"
  "v_rcp_f32 v253, v253\n"
  "v_rcp_f32 v255, v255\n"
  "v_mfma_f32_16x16x32_bf16 v[164:167], v[140:143], v[208:211], v[164:167]\n"
  "s_waitcnt lgkmcnt(0)\n"
  "v_mul_f32 v196, v196, v251\n"
  "v_mul_f32 v197, v197, v252\n"
  "v_mul_f32 v198, v198, v253\n"
  "v_mul_f32 v199, v199, v255\n"
  "v_cvt_pk_bf16_f32 v194, v196, v197\n"
  "v_cvt_pk_bf16_f32 v195, v198, v199\n"
  "v_mfma_f32_16x16x32_bf16 v[208:211], v[0:3], v[224:227], 0\n"
  "v_mul_f32 v200, s57, v200\n"
  "v_mul_f32 v201, s57, v201\n"
  "v_mul_f32 v202, s57, v202\n"
  "v_mul_f32 v203, s57, v203\n"
  "v_fma_f32 v251, -v200, v200, s46\n"
  "v_fma_f32 v252, -v201, v201, s46\n"
  "v_fma_f32 v253, -v202, v202, s46\n"
  "v_mfma_f32_16x16x32_bf16 v[212:215], v[12:15], v[224:227], 0\n"
  "v_fma_f32 v255, -v203, v203, s46\n"
  "v_mul_f32 v251, v200, v251\n"
  "v_mul_f32 v252, v201, v252\n"
  "v_mul_f32 v253, v202, v253\n"
  "v_mul_f32 v255, v203, v255\n"
  "v_exp_f32 v251, v251\n"
  "v_mfma_f32_16x16x32_bf16 v[208:211], v[4:7], v[228:231], v[208:211]\n"
  "v_exp_f32 v252, v252\n"
  "v_exp_f32 v253, v253\n"
  "v_exp_f32 v255, v255\n"
  "v_mfma_f32_16x16x32_bf16 v[212:215], v[16:19], v[228:231], v[212:215]\n"
  "v_add_f32 v251, 1.0, v251\n"
  "v_add_f32 v252, 1.0, v252\n"
  "v_add_f32 v253, 1.0, v253\n"
  "v_add_f32 v255, 1.0, v255\n"
  "v_rcp_f32 v251, v251\n"
  "v_mfma_f32_16x16x32_bf16 v[208:211], v[8:11], v[232:235], v[208:211]\n"
  "v_rcp_f32 v252, v252\n"
  "v_rcp_f32 v253, v253\n"
  "v_rcp_f32 v255, v255\n"
  "v_mul_f32 v200, v200, v251\n"
  "v_mfma_f32_16x16x32_bf16 v[212:215], v[20:23], v[232:235], v[212:215]\n"
  "v_mul_f32 v201, v201, v252\n"
  "v_mul_f32 v202, v202, v253\n"
  "v_mul_f32 v203, v203, v255\n"
  "v_cvt_pk_bf16_f32 v200, v200, v201\n"
  "v_cvt_pk_bf16_f32 v201, v202, v203\n"
  "v_mul_f32 v204, s57, v204\n"
  "v_mul_f32 v205, s57, v205\n"
  "v_mfma_f32_16x16x32_bf16 v[168:171], v[72:75], v[216:219], 0\n"
  "v_mul_f32 v206, s57, v206\n"
  "v_mul_f32 v207, s57, v207\n"
  "v_fma_f32 v251, -v204, v204, s46\n"
  "v_fma_f32 v252, -v205, v205, s46\n"
  "v_fma_f32 v253, -v206, v206, s46\n"
  "v_fma_f32 v255, -v207, v207, s46\n"
  "v_mul_f32 v251, v204, v251\n"
  "v_mfma_f32_16x16x32_bf16 v[172:175], v[76:79], v[216:219], 0\n"
  "v_mul_f32 v252, v205, v252\n"
  "v_mul_f32 v253, v206, v253\n"
  "v_mul_f32 v255, v207, v255\n"
  "v_exp_f32 v251, v251\n"
  "v_exp_f32 v252, v252\n"
  "v_mfma_f32_16x16x32_bf16 v[176:179], v[80:83], v[216:219], 0\n"
  "v_exp_f32 v253, v253\n"
  "v_exp_f32 v255, v255\n"
  "v_add_f32 v251, 1.0, v251\n"
  "v_add_f32 v252, 1.0, v252\n"
  "v_mfma_f32_16x16x32_bf16 v[180:183], v[84:87], v[216:219], 0\n"
  "v_cvt_pk_bf16_f32 v144, v144, v145\n"
  "v_cvt_pk_bf16_f32 v145, v146, v147\n"
  "v_cvt_pk_bf16_f32 v146, v148, v149\n"
  "v_cvt_pk_bf16_f32 v147, v150, v151\n"
  "v_add_f32 v253, 1.0, v253\n"
  "v_add_f32 v255, 1.0, v255\n"
  "v_mfma_f32_16x16x32_bf16 v[184:187], v[88:91], v[216:219], 0\n"
  "ds_write_b128 v249, v[144:147]\n"
  "v_rcp_f32 v251, v251\n"
  "v_rcp_f32 v252, v252\n"
  "v_rcp_f32 v253, v253\n"
  "v_rcp_f32 v255, v255\n"
  "v_mfma_f32_16x16x32_bf16 v[188:191], v[92:95], v[216:219], 0\n"
  "v_mul_f32 v204, v204, v251\n"
  "v_mul_f32 v205, v205, v252\n"
  "v_mul_f32 v206, v206, v253\n"
  "v_mul_f32 v207, v207, v255\n"
  "v_cvt_pk_bf16_f32 v202, v204, v205\n"
  "v_cvt_pk_bf16_f32 v203, v206, v207\n"
  "v_mfma_f32_16x16x32_bf16 v[216:219], v[24:27], v[224:227], 0\n"
  "v_mul_f32 v208, s57, v208\n"
  "v_mul_f32 v209, s57, v209\n"
  "v_mul_f32 v210, s57, v210\n"
  "v_mul_f32 v211, s57, v211\n"
  "v_fma_f32 v251, -v208, v208, s46\n"
  "v_fma_f32 v252, -v209, v209, s46\n"
  "v_fma_f32 v253, -v210, v210, s46\n"
  "v_mfma_f32_16x16x32_bf16 v[220:223], v[36:39], v[224:227], 0\n"
  "v_fma_f32 v255, -v211, v211, s46\n"
  "v_mul_f32 v251, v208, v251\n"
  "v_mul_f32 v252, v209, v252\n"
  "v_mul_f32 v253, v210, v253\n"
  "v_mul_f32 v255, v211, v255\n"
  "v_exp_f32 v251, v251\n"
  "v_mfma_f32_16x16x32_bf16 v[216:219], v[28:31], v[228:231], v[216:219]\n"
  "v_exp_f32 v252, v252\n"
  "v_exp_f32 v253, v253\n"
  "v_exp_f32 v255, v255\n"
  "v_mfma_f32_16x16x32_bf16 v[220:223], v[40:43], v[228:231], v[220:223]\n"
  "v_add_f32 v251, 1.0, v251\n"
  "v_add_f32 v252, 1.0, v252\n"
  "v_add_f32 v253, 1.0, v253\n"
  "v_add_f32 v255, 1.0, v255\n"
  "v_rcp_f32 v251, v251\n"
  "v_mfma_f32_16x16x32_bf16 v[216:219], v[32:35], v[232:235], v[216:219]\n"
  "v_rcp_f32 v252, v252\n"
  "v_rcp_f32 v253, v253\n"
  "v_rcp_f32 v255, v255\n"
  "v_mul_f32 v208, v208, v251\n"
  "v_mfma_f32_16x16x32_bf16 v[220:223], v[44:47], v[232:235], v[220:223]\n"
  "v_mul_f32 v209, v209, v252\n"
  "v_mul_f32 v210, v210, v253\n"
  "v_mul_f32 v211, v211, v255\n"
  "v_cvt_pk_bf16_f32 v208, v208, v209\n"
  "v_cvt_pk_bf16_f32 v209, v210, v211\n"
  "v_mul_f32 v212, s57, v212\n"
  "v_mul_f32 v213, s57, v213\n"
  "v_mfma_f32_16x16x32_bf16 v[168:171], v[96:99], v[192:195], v[168:171]\n"
  "v_mul_f32 v214, s57, v214\n"
  "v_mul_f32 v215, s57, v215\n"
  "v_fma_f32 v251, -v212, v212, s46\n"
  "v_fma_f32 v252, -v213, v213, s46\n"
  "v_fma_f32 v253, -v214, v214, s46\n"
  "v_fma_f32 v255, -v215, v215, s46\n"
  "v_mul_f32 v251, v212, v251\n"
  "v_mfma_f32_16x16x32_bf16 v[172:175], v[100:103], v[192:195], v[172:175]\n"
  "v_cvt_pk_bf16_f32 v152, v152, v153\n"
  "v_cvt_pk_bf16_f32 v153, v154, v155\n"
  "v_cvt_pk_bf16_f32 v154, v156, v157\n"
  "v_cvt_pk_bf16_f32 v155, v158, v159\n"
  "v_mul_f32 v252, v213, v252\n"
  "v_mul_f32 v253, v214, v253\n"
  "v_mfma_f32_16x16x32_bf16 v[176:179], v[104:107], v[192:195], v[176:179]\n"
  "ds_write_b128 v249, v[152:155] offset:64\n"
  "v_mul_f32 v255, v215, v255\n"
  "v_exp_f32 v251, v251\n"
  "v_exp_f32 v252, v252\n"
  "v_exp_f32 v253, v253\n"
  "v_mfma_f32_16x16x32_bf16 v[180:183], v[108:111], v[192:195], v[180:183]\n"
  "ds_read_b128 v[236:239], v248 offset:9216\n"
  "v_exp_f32 v255, v255\n"
  "v_add_f32 v251, 1.0, v251\n"
  "v_add_f32 v252, 1.0, v252\n"
  "v_add_f32 v253, 1.0, v253\n"
  "v_add_f32 v255, 1.0, v255\n"
  "v_mfma_f32_16x16x32_bf16 v[184:187], v[112:115], v[192:195], v[184:187]\n"
  "v_rcp_f32 v251, v251\n"
  "v_rcp_f32 v252, v252\n"
  "v_rcp_f32 v253, v253\n"
  "v_rcp_f32 v255, v255\n"
  "v_mfma_f32_16x16x32_bf16 v[188:191], v[116:119], v[192:195], v[188:191]\n"
  "v_mul_f32 v212, v212, v251\n"
  "v_mul_f32 v213, v213, v252\n"
  "v_mul_f32 v214, v214, v253\n"
  "v_mul_f32 v215, v215, v255\n"
  "v_cvt_pk_bf16_f32 v210, v212, v213\n"
  "v_cvt_pk_bf16_f32 v211, v214, v215\n"
  "v_mfma_f32_16x16x32_bf16 v[192:195], v[48:51], v[224:227], 0\n"
  "v_cvt_pk_bf16_f32 v160, v160, v161\n"
  "v_cvt_pk_bf16_f32 v161, v162, v163\n"
  "v_cvt_pk_bf16_f32 v162, v164, v165\n"
  "v_cvt_pk_bf16_f32 v163, v166, v167\n"
  "v_mul_f32 v216, s57, v216\n"
  "v_mul_f32 v217, s57, v217\n"
  "v_mul_f32 v218, s57, v218\n"
  "v_mfma_f32_16x16x32_bf16 v[196:199], v[60:63], v[224:227], 0\n"
  "ds_write_b128 v249, v[160:163] offset:128\n"
  "v_mul_f32 v219, s57, v219\n"
  "v_fma_f32 v251, -v216, v216, s46\n"
  "v_fma_f32 v252, -v217, v217, s46\n"
  "v_fma_f32 v253, -v218, v218, s46\n"
  "v_fma_f32 v255, -v219, v219, s46\n"
  "v_mul_f32 v251, v216, v251\n"
  "v_mfma_f32_16x16x32_bf16 v[192:195], v[52:55], v[228:231], v[192:195]\n"
  "v_mul_f32 v252, v217, v252\n"
  "v_mul_f32 v253, v218, v253\n"
  "v_mul_f32 v255, v219, v255\n"
  "v_exp_f32 v251, v251\n"
  "v_exp_f32 v252, v252\n"
  "v_mfma_f32_16x16x32_bf16 v[196:199], v[64:67], v[228:231], v[196:199]\n"
  "ds_read_b128 v[240:243], v248 offset:10240\n"
  "v_exp_f32 v253, v253\n"
  "v_exp_f32 v255, v255\n"
  "v_add_f32 v251, 1.0, v251\n"
  "v_add_f32 v252, 1.0, v252\n"
  "v_add_f32 v253, 1.0, v253\n"
  "v_mfma_f32_16x16x32_bf16 v[192:195], v[56:59], v[232:235], v[192:195]\n"
  "ds_read_b128 v[244:247], v248 offset:11264\n"
  "v_add_f32 v255, 1.0, v255\n"
  "v_rcp_f32 v251, v251\n"
  "v_rcp_f32 v252, v252\n"
  "v_rcp_f32 v253, v253\n"
  "v_mfma_f32_16x16x32_bf16 v[196:199], v[68:71], v[232:235], v[196:199]\n"
  "v_rcp_f32 v255, v255\n"
  "v_mul_f32 v216, v216, v251\n"
  "v_mul_f32 v217, v217, v252\n"
  "v_mul_f32 v218, v218, v253\n"
  "v_mul_f32 v219, v219, v255\n"
  "v_mfma_f32_16x16x32_bf16 v[168:171], v[120:123], v[200:203], v[168:171]\n"
  "v_cvt_pk_bf16_f32 v216, v216, v217\n"
  "v_cvt_pk_bf16_f32 v217, v218, v219\n"
  "v_mul_f32 v220, s57, v220\n"
  "v_mul_f32 v221, s57, v221\n"
  "v_mul_f32 v222, s57, v222\n"
  "v_mul_f32 v223, s57, v223\n"
  "v_fma_f32 v251, -v220, v220, s46\n"
  "v_mfma_f32_16x16x32_bf16 v[172:175], v[124:127], v[200:203], v[172:175]\n"
  "v_fma_f32 v252, -v221, v221, s46\n"
  "v_fma_f32 v253, -v222, v222, s46\n"
  "v_fma_f32 v255, -v223, v223, s46\n"
  "v_mul_f32 v251, v220, v251\n"
  "v_mul_f32 v252, v221, v252\n"
  "v_mul_f32 v253, v222, v253\n"
  "v_mfma_f32_16x16x32_bf16 v[176:179], v[128:131], v[200:203], v[176:179]\n"
  "v_mul_f32 v255, v223, v255\n"
  "v_exp_f32 v251, v251\n"
  "v_exp_f32 v252, v252\n"
  "v_exp_f32 v253, v253\n"
  "v_mfma_f32_16x16x32_bf16 v[180:183], v[132:135], v[200:203], v[180:183]\n"
  "v_exp_f32 v255, v255\n"
  "v_add_f32 v251, 1.0, v251\n"
  "v_add_f32 v252, 1.0, v252\n"
  "v_add_f32 v253, 1.0, v253\n"
  "v_add_f32 v255, 1.0, v255\n"
  "v_mfma_f32_16x16x32_bf16 v[184:187], v[136:139], v[200:203], v[184:187]\n"
  "v_rcp_f32 v251, v251\n"
  "v_rcp_f32 v252, v252\n"
  "v_rcp_f32 v253, v253\n"
  "v_rcp_f32 v255, v255\n"
  "v_mfma_f32_16x16x32_bf16 v[188:191], v[140:143], v[200:203], v[188:191]\n"
  "s_waitcnt lgkmcnt(0)\n"
  "v_mul_f32 v220, v220, v251\n"
  "v_mul_f32 v221, v221, v252\n"
  "v_mul_f32 v222, v222, v253\n"
  "v_mul_f32 v223, v223, v255\n"
  "v_cvt_pk_bf16_f32 v218, v220, v221\n"
  "v_cvt_pk_bf16_f32 v219, v222, v223\n"
  "v_mfma_f32_16x16x32_bf16 v[200:203], v[0:3], v[236:239], 0\n"
  "v_mul_f32 v192, s57, v192\n"
  "v_mul_f32 v193, s57, v193\n"
  "v_mul_f32 v194, s57, v194\n"
  "v_mul_f32 v195, s57, v195\n"
  "v_fma_f32 v251, -v192, v192, s46\n"
  "v_fma_f32 v252, -v193, v193, s46\n"
  "v_fma_f32 v253, -v194, v194, s46\n"
  "v_mfma_f32_16x16x32_bf16 v[204:207], v[12:15], v[236:239], 0\n"
  "s_add_u32 s23, s23, 512\n"
  "v_fma_f32 v255, -v195, v195, s46\n"
  "v_mul_f32 v251, v192, v251\n"
  "v_mul_f32 v252, v193, v252\n"
  "v_mul_f32 v253, v194, v253\n"
  "v_mul_f32 v255, v195, v255\n"
  "v_exp_f32 v251, v251\n"
  "v_mfma_f32_16x16x32_bf16 v[200:203], v[4:7], v[240:243], v[200:203]\n"
  "v_exp_f32 v252, v252\n"
  "v_exp_f32 v253, v253\n"
  "v_exp_f32 v255, v255\n"
  "v_mfma_f32_16x16x32_bf16 v[204:207], v[16:19], v[240:243], v[204:207]\n"
  "v_add_f32 v251, 1.0, v251\n"
  "v_add_f32 v252, 1.0, v252\n"
  "v_add_f32 v253, 1.0, v253\n"
  "v_add_f32 v255, 1.0, v255\n"
  "v_rcp_f32 v251, v251\n"
  "v_mfma_f32_16x16x32_bf16 v[200:203], v[8:11], v[244:247], v[200:203]\n"
  "v_rcp_f32 v252, v252\n"
  "v_rcp_f32 v253, v253\n"
  "v_rcp_f32 v255, v255\n"
  "v_mul_f32 v192, v192, v251\n"
  "v_mul_f32 v193, v193, v252\n"
  "v_add_u32 v248, s54, v248\n"
  "v_mfma_f32_16x16x32_bf16 v[204:207], v[20:23], v[244:247], v[204:207]\n"
  "s_sub_u32 s54, 0, s54\n"
  "ds_read_addtid_b32 v254 offset:62464\n"
  "v_mul_f32 v194, v194, v253\n"
  "v_mul_f32 v195, v195, v255\n"
  "v_cvt_pk_bf16_f32 v192, v192, v193\n"
  "v_cvt_pk_bf16_f32 v193, v194, v195\n"
  "v_mul_f32 v196, s57, v196\n"
  "v_mul_f32 v197, s57, v197\n"
  "v_mfma_f32_16x16x32_bf16 v[144:147], v[72:75], v[208:211], 0\n"
  "v_mul_f32 v198, s57, v198\n"
  "v_mul_f32 v199, s57, v199\n"
  "v_fma_f32 v251, -v196, v196, s46\n"
  "v_fma_f32 v252, -v197, v197, s46\n"
  "v_fma_f32 v253, -v198, v198, s46\n"
  "v_fma_f32 v255, -v199, v199, s46\n"
  "v_mul_f32 v251, v196, v251\n"
  "v_mfma_f32_16x16x32_bf16 v[148:151], v[76:79], v[208:211], 0\n"
  "v_mul_f32 v252, v197, v252\n"
  "v_mul_f32 v253, v198, v253\n"
  "v_mul_f32 v255, v199, v255\n"
  "v_exp_f32 v251, v251\n"
  "v_exp_f32 v252, v252\n"
  "v_mfma_f32_16x16x32_bf16 v[152:155], v[80:83], v[208:211], 0\n"
  "s_sub_u32 s55, s55, 1\n"
  "v_exp_f32 v253, v253\n"
  "v_exp_f32 v255, v255\n"
  "v_add_f32 v251, 1.0, v251\n"
  "v_add_f32 v252, 1.0, v252\n"
  "v_add_f32 v253, 1.0, v253\n"
  "v_mfma_f32_16x16x32_bf16 v[156:159], v[84:87], v[208:211], 0\n"
  "v_cvt_pk_bf16_f32 v168, v168, v169\n"
  "v_cvt_pk_bf16_f32 v169, v170, v171\n"
  "v_cvt_pk_bf16_f32 v170, v172, v173\n"
  "v_cvt_pk_bf16_f32 v171, v174, v175\n"
  "v_add_f32 v255, 1.0, v255\n"
  "v_rcp_f32 v251, v251\n"
  "v_mfma_f32_16x16x32_bf16 v[160:163], v[88:91], v[208:211], 0\n"
  "s_waitcnt lgkmcnt(0)\n"
  "ds_write_b128 v249, v[168:171] offset:3072\n"
  "v_readfirstlane_b32 s22, v254\n"
  "v_rcp_f32 v252, v252\n"
  "v_rcp_f32 v253, v253\n"
  "v_rcp_f32 v255, v255\n"
  "v_mfma_f32_16x16x32_bf16 v[164:167], v[92:95], v[208:211], 0\n"
  "v_mul_f32 v196, v196, v251\n"
  "v_mul_f32 v197, v197, v252\n"
  "v_mul_f32 v198, v198, v253\n"
  "v_mul_f32 v199, v199, v255\n"
  "v_cvt_pk_bf16_f32 v194, v196, v197\n"
  "v_cvt_pk_bf16_f32 v195, v198, v199\n"
  "s_cbranch_scc0 GF_ev_%=\n"
  "v_mfma_f32_16x16x32_bf16 v[208:211], v[24:27], v[236:239], 0\n"
  "s_cmp_ge_u32 s22, s23\n"
  "s_cbranch_scc0 GFe_slow_%=\n"
  "GFe_back_%=:\n"
  "v_mul_f32 v200, s57, v200\n"
  "v_mul_f32 v201, s57, v201\n"
  "v_mul_f32 v202, s57, v202\n"
  "v_mul_f32 v203, s57, v203\n"
  "v_fma_f32 v251, -v200, v200, s46\n"
  "v_fma_f32 v252, -v201, v201, s46\n"
  "v_fma_f32 v253, -v202, v202, s46\n"
  "v_mfma_f32_16x16x32_bf16 v[212:215], v[36:39], v[236:239], 0\n"
  "v_fma_f32 v255, -v203, v203, s46\n"
  "v_mul_f32 v251, v200, v251\n"
  "v_mul_f32 v252, v201, v252\n"
  "v_mul_f32 v253, v202, v253\n"
  "v_mul_f32 v255, v203, v255\n"
  "v_exp_f32 v251, v251\n"
  "v_mfma_f32_16x16x32_bf16 v[208:211], v[28:31], v[240:243], v[208:211]\n"
  "v_exp_f32 v252, v252\n"
  "v_exp_f32 v253, v253\n"
  "v_exp_f32 v255, v255\n"
  "v_mfma_f32_16x16x32_bf16 v[212:215], v[40:43], v[240:243], v[212:215]\n"
  "v_add_f32 v251, 1.0, v251\n"
  "v_add_f32 v252, 1.0, v252\n"
  "v_add_f32 v253, 1.0, v253\n"
  "v_add_f32 v255, 1.0, v255\n"
  "v_rcp_f32 v251, v251\n"
  "v_mfma_f32_16x16x32_bf16 v[208:211], v[32:35], v[244:247], v[208:211]\n"
  "v_rcp_f32 v252, v252\n"
  "v_rcp_f32 v253, v253\n"
  "v_rcp_f32 v255, v255\n"
  "v_mul_f32 v200, v200, v251\n"
  "v_mfma_f32_16x16x32_bf16 v[212:215], v[44:47], v[244:247], v[212:215]\n"
  "v_mul_f32 v201, v201, v252\n"
  "v_mul_f32 v202, v202, v253\n"
  "v_mul_f32 v203, v203, v255\n"
  "v_cvt_pk_bf16_f32 v200, v200, v201\n"
  "v_cvt_pk_bf16_f32 v201, v202, v203\n"
  "v_mul_f32 v204, s57, v204\n"
  "v_mul_f32 v205, s57, v205\n"
  "v_mfma_f32_16x16x32_bf16 v[144:147], v[96:99], v[216:219], v[144:147]\n"
  "v_mul_f32 v206, s57, v206\n"
  "v_mul_f32 v207, s57, v207\n"
  "v_fma_f32 v251, -v204, v204, s46\n"
  "v_fma_f32 v252, -v205, v205, s46\n"
  "v_fma_f32 v253, -v206, v206, s46\n"
  "v_fma_f32 v255, -v207, v207, s46\n"
  "v_mul_f32 v251, v204, v251\n"
  "v_mfma_f32_16x16x32_bf16 v[148:151], v[100:103], v[216:219], v[148:151]\n"
  "v_cvt_pk_bf16_f32 v176, v176, v177\n"
  "v_cvt_pk_bf16_f32 v177, v178, v179\n"
  "v_cvt_pk_bf16_f32 v178, v180, v181\n"
  "v_cvt_pk_bf16_f32 v179, v182, v183\n"
  "v_mul_f32 v252, v205, v252\n"
  "v_mul_f32 v253, v206, v253\n"
  "v_mfma_f32_16x16x32_bf16 v[152:155], v[104:107], v[216:219], v[152:155]\n"
  "ds_write_b128 v249, v[176:179] offset:3136\n"
  "v_mul_f32 v255, v207, v255\n"
  "v_exp_f32 v251, v251\n"
  "v_exp_f32 v252, v252\n"
  "v_exp_f32 v253, v253\n"
  "v_mfma_f32_16x16x32_bf16 v[156:159], v[108:111], v[216:219], v[156:159]\n"
  "v_exp_f32 v255, v255\n"
  "v_add_f32 v251, 1.0, v251\n"
  "v_add_f32 v252, 1.0, v252\n"
  "v_add_f32 v253, 1.0, v253\n"
  "v_add_f32 v255, 1.0, v255\n"
  "v_mfma_f32_16x16x32_bf16 v[160:163], v[112:115], v[216:219], v[160:163]\n"
  "v_rcp_f32 v251, v251\n"
  "v_rcp_f32 v252, v252\n"
  "v_rcp_f32 v253, v253\n"
  "v_rcp_f32 v255, v255\n"
  "v_mfma_f32_16x16x32_bf16 v[164:167], v[116:119], v[216:219], v[164:167]\n"
  "v_mul_f32 v204, v204, v251\n"
  "v_mul_f32 v205, v205, v252\n"
  "v_mul_f32 v206, v206, v253\n"
  "v_mul_f32 v207, v207, v255\n"
  "v_cvt_pk_bf16_f32 v202, v204, v205\n"
  "v_cvt_pk_bf16_f32 v203, v206, v207\n"
  "v_mfma_f32_16x16x32_bf16 v[216:219], v[48:51], v[236:239], 0\n"
  "v_cvt_pk_bf16_f32 v184, v184, v185\n"
  "v_cvt_pk_bf16_f32 v185, v186, v187\n"
  "v_cvt_pk_bf16_f32 v186, v188, v189\n"
  "v_cvt_pk_bf16_f32 v187, v190, v191\n"
  "v_mul_f32 v208, s57, v208\n"
  "v_mul_f32 v209, s57, v209\n"
  "v_mul_f32 v210, s57, v210\n"
  "v_mfma_f32_16x16x32_bf16 v[220:223], v[60:63], v[236:239], 0\n"
  "ds_write_b128 v249, v[184:187] offset:3200\n"
  "v_mul_f32 v211, s57, v211\n"
  "v_fma_f32 v251, -v208, v208, s46\n"
  "v_fma_f32 v252, -v209, v209, s46\n"
  "v_fma_f32 v253, -v210, v210, s46\n"
  "v_fma_f32 v255, -v211, v211, s46\n"
  "v_mul_f32 v251, v208, v251\n"
  "v_mfma_f32_16x16x32_bf16 v[216:219], v[52:55], v[240:243], v[216:219]\n"
  "ds_append v254 offset:62464\n"
  "v_mul_f32 v252, v209, v252\n"
  "v_mul_f32 v253, v210, v253\n"
  "v_mul_f32 v255, v211, v255\n"
  "v_exp_f32 v251, v251\n"
  "v_exp_f32 v252, v252\n"
  "v_mfma_f32_16x16x32_bf16 v[220:223], v[64:67], v[240:243], v[220:223]\n"
  "v_exp_f32 v253, v253\n"
  "v_exp_f32 v255, v255\n"
  "v_add_f32 v251, 1.0, v251\n"
  "v_add_f32 v252, 1.0, v252\n"
  "v_add_f32 v253, 1.0, v253\n"
  "v_mfma_f32_16x16x32_bf16 v[216:219], v[56:59], v[244:247], v[216:219]\n"
  "v_add_f32 v255, 1.0, v255\n"
  "v_rcp_f32 v251, v251\n"
  "v_rcp_f32 v252, v252\n"
  "v_rcp_f32 v253, v253\n"
  "v_mfma_f32_16x16x32_bf16 v[220:223], v[68:71], v[244:247], v[220:223]\n"
  "v_rcp_f32 v255, v255\n"
  "v_mul_f32 v208, v208, v251\n"
  "v_mul_f32 v209, v209, v252\n"
  "v_mul_f32 v210, v210, v253\n"
  "v_mul_f32 v211, v211, v255\n"
  "v_mfma_f32_16x16x32_bf16 v[144:147], v[120:123], v[192:195], v[144:147]\n"
  "v_cvt_pk_bf16_f32 v208, v208, v209\n"
  "v_cvt_pk_bf16_f32 v209, v210, v211\n"
  "v_mul_f32 v212, s57, v212\n"
  "v_mul_f32 v213, s57, v213\n"
  "v_mul_f32 v214, s57, v214\n"
  "v_mul_f32 v215, s57, v215\n"
  "v_fma_f32 v251, -v212, v212, s46\n"
  "v_mfma_f32_16x16x32_bf16 v[148:151], v[124:127], v[192:195], v[148:151]\n"
  "v_fma_f32 v252, -v213, v213, s46\n"
  "v_fma_f32 v253, -v214, v214, s46\n"
  "v_fma_f32 v255, -v215, v215, s46\n"
  "v_mul_f32 v251, v212, v251\n"
  "v_mul_f32 v252, v213, v252\n"
  "v_mul_f32 v253, v214, v253\n"
  "v_mfma_f32_16x16x32_bf16 v[152:155], v[128:131], v[192:195], v[152:155]\n"
  "v_mul_f32 v255, v215, v255\n"
  "v_exp_f32 v251, v251\n"
  "v_exp_f32 v252, v252\n"
  "v_exp_f32 v253, v253\n"
  "v_mfma_f32_16x16x32_bf16 v[156:159], v[132:135], v[192:195], v[156:159]\n"
  "v_exp_f32 v255, v255\n"
  "v_add_f32 v251, 1.0, v251\n"
  "v_add_f32 v252, 1.0, v252\n"
  "v_add_f32 v253, 1.0, v253\n"
  "v_add_f32 v255, 1.0, v255\n"
  "v_mfma_f32_16x16x32_bf16 v[160:163], v[136:139], v[192:195], v[160:163]\n"
  "v_rcp_f32 v251, v251\n"
  "v_rcp_f32 v252, v252\n"
  "v_rcp_f32 v253, v253\n"
  "v_rcp_f32 v255, v255\n"
  "v_mfma_f32_16x16x32_bf16 v[164:167], v[140:143], v[192:195], v[164:167]\n"
  "v_mul_f32 v212, v212, v251\n"
  "v_mul_f32 v213, v213, v252\n"
  "v_mul_f32 v214, v214, v253\n"
  "v_mul_f32 v215, v215, v255\n"
  "v_cvt_pk_bf16_f32 v210, v212, v213\n"
  "v_cvt_pk_bf16_f32 v211, v214, v215\n"
  "v_mfma_f32_16x16x32_bf16 v[168:171], v[72:75], v[200:203], 0\n"
  "v_mul_f32 v216, s57, v216\n"
  "v_mul_f32 v217, s57, v217\n"
  "v_mul_f32 v218, s57, v218\n"
  "v_mul_f32 v219, s57, v219\n"
  "v_fma_f32 v251, -v216, v216, s46\n"
  "v_fma_f32 v252, -v217, v217, s46\n"
  "v_fma_f32 v253, -v218, v218, s46\n"
  "v_fma_f32 v255, -v219, v219, s46\n"
  "v_mul_f32 v251, v216, v251\n"
  "v_mul_f32 v252, v217, v252\n"
  "v_mul_f32 v253, v218, v253\n"
  "v_mul_f32 v255, v219, v255\n"
  "v_exp_f32 v251, v251\n"
  "v_mfma_f32_16x16x32_bf16 v[172:175], v[76:79], v[200:203], 0\n"
  "v_exp_f32 v252, v252\n"
  "v_exp_f32 v253, v253\n"
  "v_exp_f32 v255, v255\n"
  "v_add_f32 v251, 1.0, v251\n"
  "v_add_f32 v252, 1.0, v252\n"
  "v_add_f32 v253, 1.0, v253\n"
  "v_add_f32 v255, 1.0, v255\n"
  "v_rcp_f32 v251, v251\n"
  "v_mfma_f32_16x16x32_bf16 v[176:179], v[80:83], v[200:203], 0\n"
  "v_rcp_f32 v252, v252\n"
  "v_rcp_f32 v253, v253\n"
  "v_rcp_f32 v255, v255\n"
  "v_mul_f32 v216, v216, v251\n"
  "v_mul_f32 v217, v217, v252\n"
  "v_mul_f32 v218, v218, v253\n"
  "v_mul_f32 v219, v219, v255\n"
  "v_cvt_pk_bf16_f32 v216, v216, v217\n"
  "v_cvt_pk_bf16_f32 v217, v218, v219\n"
  "v_mul_f32 v220, s57, v220\n"
  "v_mul_f32 v221, s57, v221\n"
  "v_mfma_f32_16x16x32_bf16 v[180:183], v[84:87], v[200:203], 0\n"
  "v_mul_f32 v222, s57, v222\n"
  "v_mul_f32 v223, s57, v223\n"
  "v_fma_f32 v251, -v220, v220, s46\n"
  "v_fma_f32 v252, -v221, v221, s46\n"
  "v_fma_f32 v253, -v222, v222, s46\n"
  "v_fma_f32 v255, -v223, v223, s46\n"
  "v_mul_f32 v251, v220, v251\n"
  "v_mul_f32 v252, v221, v252\n"
  "v_mul_f32 v253, v222, v253\n"
  "v_mul_f32 v255, v223, v255\n"
  "v_exp_f32 v251, v251\n"
  "v_exp_f32 v252, v252\n"
  "v_mfma_f32_16x16x32_bf16 v[184:187], v[88:91], v[200:203], 0\n"
  "v_cvt_pk_bf16_f32 v144, v144, v145\n"
  "v_cvt_pk_bf16_f32 v145, v146, v147\n"
  "v_cvt_pk_bf16_f32 v146, v148, v149\n"
  "v_cvt_pk_bf16_f32 v147, v150, v151\n"
  "v_exp_f32 v253, v253\n"
  "v_exp_f32 v255, v255\n"
  "v_add_f32 v251, 1.0, v251\n"
  "v_add_f32 v252, 1.0, v252\n"
  "v_add_f32 v253, 1.0, v253\n"
  "v_add_f32 v255, 1.0, v255\n"
  "v_mfma_f32_16x16x32_bf16 v[188:191], v[92:95], v[200:203], 0\n"
  "ds_write_b128 v249, v[144:147] offset:24576\n"
  "v_rcp_f32 v251, v251\n"
  "v_rcp_f32 v252, v252\n"
  "v_rcp_f32 v253, v253\n"
  "v_rcp_f32 v255, v255\n"
  "v_mul_f32 v220, v220, v251\n"
  "v_mul_f32 v221, v221, v252\n"
  "v_mul_f32 v222, v222, v253\n"
  "v_mul_f32 v223, v223, v255\n"
  "v_cvt_pk_bf16_f32 v218, v220, v221\n"
  "v_cvt_pk_bf16_f32 v219, v222, v223\n"
  "v_mfma_f32_16x16x32_bf16 v[168:171], v[96:99], v[208:211], v[168:171]\n"
  "v_cvt_pk_bf16_f32 v152, v152, v153\n"
  "v_cvt_pk_bf16_f32 v153, v154, v155\n"
  "v_cvt_pk_bf16_f32 v154, v156, v157\n"
  "v_cvt_pk_bf16_f32 v155, v158, v159\n"
  "v_mfma_f32_16x16x32_bf16 v[172:175], v[100:103], v[208:211], v[172:175]\n"
  "ds_write_b128 v249, v[152:155] offset:24640\n"
  "v_mfma_f32_16x16x32_bf16 v[176:179], v[104:107], v[208:211], v[176:179]\n"
  "v_mfma_f32_16x16x32_bf16 v[180:183], v[108:111], v[208:211], v[180:183]\n"
  "v_cvt_pk_bf16_f32 v160, v160, v161\n"
  "v_cvt_pk_bf16_f32 v161, v162, v163\n"
  "v_cvt_pk_bf16_f32 v162, v164, v165\n"
  "v_cvt_pk_bf16_f32 v163, v166, v167\n"
  "v_mfma_f32_16x16x32_bf16 v[184:187], v[112:115], v[208:211], v[184:187]\n"
  "ds_write_b128 v249, v[160:163] offset:24704\n"
  "v_mfma_f32_16x16x32_bf16 v[188:191], v[116:119], v[208:211], v[188:191]\n"
  "v_mfma_f32_16x16x32_bf16 v[168:171], v[120:123], v[216:219], v[168:171]\n"
  "v_mfma_f32_16x16x32_bf16 v[172:175], v[124:127], v[216:219], v[172:175]\n"
  "v_mfma_f32_16x16x32_bf16 v[176:179], v[128:131], v[216:219], v[176:179]\n"
  "v_mfma_f32_16x16x32_bf16 v[180:183], v[132:135], v[216:219], v[180:183]\n"
  "v_mfma_f32_16x16x32_bf16 v[184:187], v[136:139], v[216:219], v[184:187]\n"
  "v_mfma_f32_16x16x32_bf16 v[188:191], v[140:143], v[216:219], v[188:191]\n"
  "s_nop 2\n"
  "v_cvt_pk_bf16_f32 v168, v168, v169\n"
  "v_cvt_pk_bf16_f32 v169, v170, v171\n"
  "v_cvt_pk_bf16_f32 v170, v172, v173\n"
  "v_cvt_pk_bf16_f32 v171, v174, v175\n"
  "ds_write_b128 v249, v[168:171] offset:27648\n"
  "v_cvt_pk_bf16_f32 v176, v176, v177\n"
  "v_cvt_pk_bf16_f32 v177, v178, v179\n"
  "v_cvt_pk_bf16_f32 v178, v180, v181\n"
  "v_cvt_pk_bf16_f32 v179, v182, v183\n"
  "ds_write_b128 v249, v[176:179] offset:27712\n"
  "v_cvt_pk_bf16_f32 v184, v184, v185\n"
  "v_cvt_pk_bf16_f32 v185, v186, v187\n"
  "v_cvt_pk_bf16_f32 v186, v188, v189\n"
  "v_cvt_pk_bf16_f32 v187, v190, v191\n"
  "ds_write_b128 v249, v[184:187] offset:27776\n"
  "s_waitcnt lgkmcnt(0)\n"
  "s_barrier\n"
  "s_endpgm\n"
  "GFa_slow_%=:\n"
  "s_mov_b32 s48, 0x40000\n"
  "GFa_spin_%=:\n"
  "s_sleep 0\n"
  "ds_read_addtid_b32 v254 offset:62464\n"
  "s_waitcnt lgkmcnt(0)\n"
  "v_readfirstlane_b32 s22, v254\n"
  "s_nop 3\n"
  "s_cmp_ge_u32 s22, s23\n"
  "s_cbranch_scc1 GFa_back_%=\n"
  "s_sub_u32 s48, s48, 1\n"
  "s_cbranch_scc0 GFa_spin_%=\n"
  "s_branch GFa_back_%=\n"
  "GFb_slow_%=:\n"
  "s_mov_b32 s48, 0x40000\n"
  "GFb_spin_%=:\n"
  "s_sleep 0\n"
  "ds_read_addtid_b32 v254 offset:62464\n"
  "s_waitcnt lgkmcnt(0)\n"
  "v_readfirstlane_b32 s22, v254\n"
  "s_nop 3\n"
  "s_cmp_ge_u32 s22, s23\n"
  "s_cbranch_scc1 GFb_back_%=\n"
  "s_sub_u32 s48, s48, 1\n"
  "s_cbranch_scc0 GFb_spin_%=\n"
  "s_branch GFb_back_%=\n"
  "GFe_slow_%=:\n"
  "s_mov_b32 s48, 0x40000\n"
  "GFe_spin_%=:\n"
  "s_sleep 0\n"
  "ds_read_addtid_b32 v254 offset:62464\n"
  "s_waitcnt lgkmcnt(0)\n"
  "v_readfirstlane_b32 s22, v254\n"
  "s_nop 3\n"
  "s_cmp_ge_u32 s22, s23\n"
  "s_cbranch_scc1 GFe_back_%=\n"
  "s_sub_u32 s48, s48, 1\n"
  "s_cbranch_scc0 GFe_spin_%=\n"
  "s_branch GFe_back_%=\n"
  :: "s"(args), "s"(wg), "s"(wave), "s"(ldsbase)
  : "v0", "v1", "v2", "v3", "v4", "v5", "v6", "v7", "v8", "v9", "v10", "v11", "v12", "v13", "v14", "v15", "v16", "v17", "v18", "v19", "v20", "v21", "v22", "v23", "v24", "v25", "v26", "v27", "v28", "v29", "v30", "v31", "v32", "v33", "v34", "v35", "v36", "v37", "v38", "v39", "v40", "v41", "v42", "v43", "v44", "v45", "v46", "v47", "v48", "v49", "v50", "v51", "v52", "v53", "v54", "v55", "v56", "v57", "v58", "v59", "v60", "v61", "v62", "v63", "v64", "v65", "v66", "v67", "v68", "v69", "v70", "v71", "v72", "v73", "v74", "v75", "v76", "v77", "v78", "v79", "v80", "v81", "v82", "v83", "v84", "v85", "v86", "v87", "v88", "v89", "v90", "v91", "v92", "v93", "v94", "v95", "v96", "v97", "v98", "v99", "v100", "v101", "v102", "v103", "v104", "v105", "v106", "v107", "v108", "v109", "v110", "v111", "v112", "v113", "v114", "v115", "v116", "v117", "v118", "v119", "v120", "v121", "v122", "v123", "v124", "v125", "v126", "v127", "v128", "v129", "v130", "v131", "v132", "v133", "v134", "v135", "v136", "v137", "v138", "v139", "v140", "v141", "v142", "v143", "v144", "v145", "v146", "v147", "v148", "v149", "v150", "v151", "v152", "v153", "v154", "v155", "v156", "v157", "v158", "v159", "v160", "v161", "v162", "v163", "v164", "v165", "v166", "v167", "v168", "v169", "v170", "v171", "v172", "v173", "v174", "v175", "v176", "v177", "v178", "v179", "v180", "v181", "v182", "v183", "v184", "v185", "v186", "v187", "v188", "v189", "v190", "v191", "v192", "v193", "v194", "v195", "v196", "v197", "v198", "v199", "v200", "v201", "v202", "v203", "v204", "v205", "v206", "v207", "v208", "v209", "v210", "v211", "v212", "v213", "v214", "v215", "v216", "v217", "v218", "v219", "v220", "v221", "v222", "v223", "v224", "v225", "v226", "v227", "v228", "v229", "v230", "v231", "v232", "v233", "v234", "v235", "v236", "v237", "v238", "v239", "v240", "v241", "v242", "v243", "v244", "v245", "v246", "v247", "v248", "v249", "v250", "v251", "v252", "v253", "v254", "v255", "s8", "s9", "s10", "s11", "s12", "s13", "s14", "s15", "s16", "s17", "s18", "s19", "s20", "s21", "s22", "s23", "s24", "s25", "s26", "s27", "s28", "s29", "s30", "s31", "s36", "s37", "s38", "s39", "s40", "s41", "s42", "s43", "s44", "s45", "s46", "s47", "s48", "s49", "s50", "s51", "s52", "s53", "s54", "s55", "s56", "s57", "s58", "s59", "s60", "s61", "s62", "s63", "s64", "s65", "s66", "s67", "s68", "s69", "s70", "s71", "s72", "s73", "s74", "s75", "s76", "s77", "s78", "s79", "s80", "s81", "s82", "s83", "s84", "s85", "s86", "s87", "s88", "s89", "s90", "s91", "s92", "s93", "s94", "s95", "vcc", "scc", "memory");
}

extern "C" void kernel_launch(void* const* d_in, const int* in_sizes, int n_in, void* d_out, int out_size, void* d_ws, size_t ws_size, hipStream_t stream) {
  replk_fused<<<256, 512, 0, stream>>>(d_in[0], d_in[1], d_in[2], d_in[3], d_in[4], d_out);
}
```
